```python
import torch

def _softmax(x, axis=-1):
    return torch.softmax(x, dim=axis)
def _gelu(x):
    return torch.nn.functional.gelu(x, approximate="tanh")
def _silu(x):
    return torch.nn.functional.silu(x)
def _sigmoid(x):
    return torch.sigmoid(x)
def _layernorm(x, axis=-1, eps=1e-5):
    m = x.mean(dim=axis, keepdim=True)
    v = x.var(dim=axis, keepdim=True, unbiased=False)
    return (x - m) / torch.sqrt(v + eps)
def _scatter_add(tgt, idx, src, axis=0):
    return tgt.clone().index_add_(axis, idx, src)
def _segment_sum(src, seg, n):
    out = torch.zeros((n,) + src.shape[1:], dtype=src.dtype)
    return out.index_add_(0, seg, src)
def _pad(x, pw):
    flat = tuple(v for pair in reversed(pw) for v in pair)
    return torch.nn.functional.pad(x, flat)

kernel_name = 'sequential_preLN__spectral_fnet__swiglu_ffn__prompt'

def setup_inputs():

    _w = (lambda a: (lambda t: t.to(torch.bfloat16) if t.is_floating_point() else t)(torch.as_tensor(a)))
    def _A_setup():
        B, S, D = 1, 2048, 576
        ns = torch.arange(S)[:, None] * torch.arange(S)[None, :]
        nd = torch.arange(D)[:, None] * torch.arange(D)[None, :]
        Ws_re = torch.cos(-2.0 * torch.pi * ns / S).to(float)
        Ws_im = torch.sin(-2.0 * torch.pi * ns / S).to(float)
        Wd_re = torch.cos(-2.0 * torch.pi * nd / D).to(float)
        Wd_im = torch.sin(-2.0 * torch.pi * nd / D).to(float)
        return {
            "x": _w((lambda *s: torch.randn(*s, dtype=torch.bfloat16) * 0.05)(B, S, D)),
            "dft_seq_re": _w(Ws_re),
            "dft_seq_im": _w(Ws_im),
            "dft_hid_re": _w(Wd_re),
            "dft_hid_im": _w(Wd_im),
        }
    _A_in = _A_setup()

    def _B_setup():
        B, S, D, F = 1, 2048, 576, 4608
        return {
            "x": _w((lambda *s: torch.randn(*s, dtype=torch.bfloat16) * 0.05)(B, S, D)),
            "w_gate": _w((lambda *s: torch.randn(*s, dtype=torch.bfloat16) * 0.2)(D, F)),
            "w_up": _w((lambda *s: torch.randn(*s, dtype=torch.bfloat16) * 0.2)(D, F)),
            "w_down": _w((lambda *s: torch.randn(*s, dtype=torch.bfloat16) * 0.2)(F, D)),
        }
    _B_in = _B_setup()

    return {
        "A_x": _A_in["x"],
        "A_dft_seq_re": _A_in["dft_seq_re"],
        "A_dft_seq_im": _A_in["dft_seq_im"],
        "A_dft_hid_re": _A_in["dft_hid_re"],
        "A_dft_hid_im": _A_in["dft_hid_im"],
        "B_x": _B_in["x"],
        "B_w_gate": _B_in["w_gate"],
        "B_w_up": _B_in["w_up"],
        "B_w_down": _B_in["w_down"],
    }

def reference(A_x, A_dft_seq_re, A_dft_seq_im, A_dft_hid_re, A_dft_hid_im, B_x, B_w_gate, B_w_up, B_w_down):
    def _A_ref(x, dft_seq_re, dft_seq_im, dft_hid_re, dft_hid_im):
        a = torch.einsum("bsd,ed->bse", x, dft_hid_re)
        b = torch.einsum("bsd,ed->bse", x, dft_hid_im)
        y = torch.einsum("ts,bse->bte", dft_seq_re, a) - torch.einsum("ts,bse->bte", dft_seq_im, b)
        return y

    def _B_ref(x, w_gate, w_up, w_down):
        g = _silu(x @ w_gate)
        u = x @ w_up
        return (g * u) @ w_down

    x = A_x
    h = x + _A_ref(_layernorm(x, axis=-1), A_dft_seq_re, A_dft_seq_im, A_dft_hid_re, A_dft_hid_im)
    h2 = h + _B_ref(_layernorm(h, axis=-1), B_w_gate, B_w_up, B_w_down)
    return h2
```

```cpp
#include <hip/hip_runtime.h>
#include <hip/hip_bf16.h>
#include <stdint.h>

typedef unsigned short bf16_t;
typedef short bf16x8 __attribute__((ext_vector_type(8)));
typedef float f32x4 __attribute__((ext_vector_type(4)));

__device__ __forceinline__ float bf2f(bf16_t v) { return __uint_as_float(((unsigned)v) << 16); }
__device__ __forceinline__ bf16_t f2bf(float f) { __hip_bfloat16 b = __float2bfloat16(f); return *(bf16_t*)&b; }

constexpr int S = 2048, D = 576, F = 4608;

__global__ void k_ln(const bf16_t* in, bf16_t* out) {
  int row = blockIdx.x; int t = threadIdx.x;
  float v[9]; float s = 0;
  for (int i = 0; i < 9; ++i) { v[i] = bf2f(in[row * D + i * 64 + t]); s += v[i]; }
  for (int o = 32; o; o >>= 1) s += __shfl_xor(s, o);
  float m = s / D; float q = 0;
  for (int i = 0; i < 9; ++i) { float d = v[i] - m; q += d * d; }
  for (int o = 32; o; o >>= 1) q += __shfl_xor(q, o);
  float r = 1.0f / sqrtf(q / D + 1e-5f);
  for (int i = 0; i < 9; ++i) out[row * D + i * 64 + t] = f2bf((v[i] - m) * r);
}

template <bool BT, int EPI>
__global__ void __launch_bounds__(256) k_gemm(const bf16_t* A, int lda, const bf16_t* B, int ldb, int K,
    const bf16_t* A2, const bf16_t* B2, bf16_t* C, int ldc, const bf16_t* R) {
  __shared__ bf16_t sA[64][40], sB[64][40], sB2[64][40];
  int bm = blockIdx.y * 64, bn = blockIdx.x * 64;
  int t = threadIdx.x, wid = t >> 6, lane = t & 63, wr = wid >> 1, wc = wid & 1, fr = lane & 15, fq = lane >> 4;
  f32x4 acc[2][2] = {}, acc2[2][2] = {};
  for (int pass = 0; pass < (EPI == 1 ? 2 : 1); ++pass) {
    const bf16_t* Ap = pass ? A2 : A; const bf16_t* Bp = pass ? B2 : B;
    for (int k0 = 0; k0 < K; k0 += 32) {
      __syncthreads();
      for (int i = t; i < 64 * 32; i += 256) { int r = i >> 5, c = i & 31; sA[r][c] = Ap[(size_t)(bm + r) * lda + k0 + c]; }
      if (BT) { for (int i = t; i < 64 * 32; i += 256) { int r = i >> 5, c = i & 31; sB[r][c] = Bp[(size_t)(bn + r) * ldb + k0 + c]; } }
      else { for (int i = t; i < 64 * 32; i += 256) { int c = i >> 6, r = i & 63; sB[r][c] = Bp[(size_t)(k0 + c) * ldb + bn + r];
               if (EPI == 2) sB2[r][c] = B2[(size_t)(k0 + c) * ldb + bn + r]; } }
      __syncthreads();
      for (int m = 0; m < 2; ++m) for (int n = 0; n < 2; ++n) {
        bf16x8 a = *(const bf16x8*)&sA[wr * 32 + m * 16 + fr][fq * 8];
        bf16x8 b = *(const bf16x8*)&sB[wc * 32 + n * 16 + fr][fq * 8];
        if (EPI == 1 && pass == 1) acc2[m][n] = __builtin_amdgcn_mfma_f32_16x16x32_bf16(a, b, acc2[m][n], 0, 0, 0);
        else acc[m][n] = __builtin_amdgcn_mfma_f32_16x16x32_bf16(a, b, acc[m][n], 0, 0, 0);
        if (EPI == 2) { bf16x8 b2 = *(const bf16x8*)&sB2[wc * 32 + n * 16 + fr][fq * 8];
          acc2[m][n] = __builtin_amdgcn_mfma_f32_16x16x32_bf16(a, b2, acc2[m][n], 0, 0, 0); }
      }
    }
  }
  for (int m = 0; m < 2; ++m) for (int n = 0; n < 2; ++n) for (int j = 0; j < 4; ++j) {
    int r = bm + wr * 32 + m * 16 + fq * 4 + j, c = bn + wc * 32 + n * 16 + fr;
    float v = acc[m][n][j];
    if (EPI == 1) v = bf2f(R[(size_t)r * ldc + c]) + (v - acc2[m][n][j]);
    if (EPI == 2) { float g = v; g = g / (1.0f + __expf(-g)); v = g * acc2[m][n][j]; }
    if (EPI == 3) v = bf2f(R[(size_t)r * ldc + c]) + v;
    C[(size_t)r * ldc + c] = f2bf(v);
  }
}

extern "C" void kernel_launch(void* const* d_in, const int* in_sizes, int n_in,
                              void* d_out, int out_size, void* d_ws, size_t ws_size,
                              hipStream_t stream) {
  const bf16_t* x = (const bf16_t*)d_in[0];
  const bf16_t* cs_re = (const bf16_t*)d_in[1];
  const bf16_t* cs_im = (const bf16_t*)d_in[2];
  const bf16_t* wd_re = (const bf16_t*)d_in[3];
  const bf16_t* wd_im = (const bf16_t*)d_in[4];
  const bf16_t* wg = (const bf16_t*)d_in[6];
  const bf16_t* wu = (const bf16_t*)d_in[7];
  const bf16_t* wdn = (const bf16_t*)d_in[8];
  bf16_t* out = (bf16_t*)d_out;
  bf16_t* ws = (bf16_t*)d_ws;
  bf16_t* xn = ws; bf16_t* a = xn + S * D; bf16_t* b = a + S * D; bf16_t* h = b + S * D; bf16_t* hn = h + S * D; bf16_t* act = hn + S * D;
  k_ln<<<S, 64, 0, stream>>>(x, xn);
  k_gemm<true, 0><<<dim3(D / 64, S / 64), 256, 0, stream>>>(xn, D, wd_re, D, D, nullptr, nullptr, a, D, nullptr);
  k_gemm<true, 0><<<dim3(D / 64, S / 64), 256, 0, stream>>>(xn, D, wd_im, D, D, nullptr, nullptr, b, D, nullptr);
  k_gemm<false, 1><<<dim3(D / 64, S / 64), 256, 0, stream>>>(cs_re, S, a, D, S, cs_im, b, h, D, x);
  k_ln<<<S, 64, 0, stream>>>(h, hn);
  k_gemm<false, 2><<<dim3(F / 64, S / 64), 256, 0, stream>>>(hn, D, wg, F, D, nullptr, wu, act, F, nullptr);
  k_gemm<false, 3><<<dim3(D / 64, S / 64), 256, 0, stream>>>(act, F, wdn, D, F, nullptr, nullptr, out, D, h);
}
```

```cpp
#include <hip/hip_runtime.h>
#include <stdint.h>

typedef unsigned short bf16_t;
typedef short bf16x8 __attribute__((ext_vector_type(8)));
typedef short s16x4 __attribute__((ext_vector_type(4)));
typedef float f32x4 __attribute__((ext_vector_type(4)));
typedef float f32x2 __attribute__((ext_vector_type(2)));
typedef unsigned u32x4 __attribute__((ext_vector_type(4)));
typedef unsigned u32x2 __attribute__((ext_vector_type(2)));
typedef __bf16 bf2_t __attribute__((ext_vector_type(2)));
typedef __amdgpu_buffer_rsrc_t rsrc_t;

constexpr int S_ = 2048, D_ = 576, F_ = 4608;
constexpr float LN_EPS = 1e-5f;

constexpr unsigned WS_BP = 0;
constexpr unsigned BP_BYTES = 8u * 32u * 18432u;
constexpr unsigned WS_HT = WS_BP + BP_BYTES;
constexpr unsigned HT_BYTES = 128u * 72u * 256u;
constexpr unsigned WS_ST = WS_HT + HT_BYTES;
constexpr unsigned ST_BYTES = 2048u * 8u * 8u;
constexpr unsigned WS_ACT = WS_ST + ST_BYTES;
constexpr unsigned ACT_BYTES = 128u * 144u * 1024u;

#define LDSP(p) ((__attribute__((address_space(3))) void*)(p))
#define MKRS(p, n) __builtin_amdgcn_make_buffer_rsrc((void*)(p), 0, (n), 0x00020000)
#define GLDS(rs, m0base, imm, voff, soff)                                                              \
  asm volatile("s_add_u32 m0, %0, %1\n\ts_nop 0\n\tbuffer_load_dwordx4 %2, %3, %4 offen lds"          \
               ::"s"(m0base), "i"(imm), "v"(voff), "s"(rs), "s"(soff) : "memory", "scc")
#define GLDS_N(mask, rs, m0base, imm, voff, soff)                                                      \
  asm volatile("s_add_u32 m0, %0, %1\n\ts_mov_b64 exec, " #mask "\n\tbuffer_load_dwordx4 %2, %3, %4 offen lds\n\ts_mov_b64 exec, -1" \
               ::"s"(m0base), "i"(imm), "v"(voff), "s"(rs), "s"(soff) : "memory", "scc")
__device__ __forceinline__ u32x4 mksrd(const void* p, unsigned n) {
  const unsigned long long a = (unsigned long long)p;
  return u32x4{(unsigned)a, (unsigned)(a >> 32) & 0xffffu, n, 0x00020000u};
}
__device__ __forceinline__ unsigned lds_base(char* smem) { return (unsigned)(size_t)LDSP(smem); }
#define WAITV(n) asm volatile("s_waitcnt vmcnt(" #n ")" ::: "memory")
#define WAITL0() asm volatile("s_waitcnt lgkmcnt(0)" ::: "memory")
#define PIN(x) asm volatile("" : "+v"(x))
#define BAR() __builtin_amdgcn_s_barrier()
#define SCHED() __builtin_amdgcn_sched_barrier(0)
#define MFMA16(a, b, c) __builtin_amdgcn_mfma_f32_16x16x32_bf16((a), (b), (c), 0, 0, 0)
constexpr int AUX_SC1 = 16;
#define TS(var, j) do { } while (0)
#define TS_OUT(ws, kid, wid, lane, k, var) do { } while (0)
#define STAMP(ws, kid, slot) do { } while (0)

__device__ __forceinline__ unsigned pk_bf16(float lo, float hi) {
  f32x2 v = {lo, hi};
  bf2_t b = __builtin_convertvector(v, bf2_t);
  return __builtin_bit_cast(unsigned, b);
}
__device__ __forceinline__ float bf_lo(unsigned w) { return __uint_as_float(w << 16); }
__device__ __forceinline__ float bf_hi(unsigned w) { return __uint_as_float(w & 0xffff0000u); }
__device__ __forceinline__ float dot_ones(unsigned w, float acc) {
  const bf2_t ones = {(__bf16)1.0f, (__bf16)1.0f};
  return __builtin_amdgcn_fdot2_f32_bf16(__builtin_bit_cast(bf2_t, w), ones, acc, false);
}
__device__ __forceinline__ float dot_frag(bf16x8 f, float acc) {
  u32x4 u = __builtin_bit_cast(u32x4, f);
  acc = dot_ones(u[0], acc); acc = dot_ones(u[1], acc); acc = dot_ones(u[2], acc); acc = dot_ones(u[3], acc);
  return acc;
}

constexpr int K1_SLOT = 8192 + 18432, K1_NS = 5, K1_LDS = K1_NS * K1_SLOT + 512;

template <int NF>
__device__ __forceinline__ void k1_body(char* smem, u32x4 rsX, u32x4 rsW, rsrc_t rsO, int st, int et, int wid, int lane, int nf0, char* ws) {
  STAMP(ws, 0, 0);
  const int tid = wid * 64 + lane;
  const int sh = wid >> 2, q = lane >> 4, r16 = lane & 15, r8 = lane >> 3, c8 = lane & 7;
  const unsigned vx = (unsigned)(64 * st + 8 * wid + r8) * 1152u + (unsigned)((c8 ^ ((r8 & 3) | ((wid & 1) << 2))) * 16);
  const unsigned vw = (unsigned)((et & 3) * 144 + 8 * wid + r8) * 1152u + (unsigned)((c8 ^ r8) * 16);
  const int r2 = 128 + 2 * wid + r8;
  const unsigned vw2 = (unsigned)((et & 3) * 144 + r2) * 1152u + (unsigned)((c8 ^ (r2 & 7)) * 16);
  const unsigned mA = lds_base(smem) + wid * 1024, mP = lds_base(smem) + wid * 256;
  auto issue = [&](int kb) __attribute__((always_inline)) {
    const int so = (kb % K1_NS) * K1_SLOT;
    GLDS(rsX, mA, so, vx, kb * 128);
    GLDS(rsW, mA, so + 8192, vw, kb * 128);
    GLDS(rsW, mA, so + 16384, vw, kb * 128 + 64 * 1152);
    GLDS_N(0xffff, rsW, mP, so + 24576, vw2, kb * 128);
  };
  unsigned ax[2];
#pragma unroll
  for (int m = 0; m < 2; ++m) {
    const int row = 32 * sh + 8 * (r16 >> 2) + 4 * m + (r16 & 3);
    const int key = (row & 3) | ((row >> 1) & 4);
    ax[m] = (unsigned)(row * 128 + ((q ^ key) * 16));
  }
  const unsigned aw = (unsigned)(8192 + (nf0 * 16 + r16) * 128 + ((q ^ (r16 & 7)) * 16));

  f32x4 acc[2][NF];
  float wsum[NF];
#pragma unroll
  for (int n = 0; n < NF; ++n) { wsum[n] = 0.f; acc[0][n] = f32x4{0, 0, 0, 0}; acc[1][n] = f32x4{0, 0, 0, 0}; }
  float shift = 0.f;
  f32x2 s1 = {0.f, 0.f}, s2 = {0.f, 0.f};

  issue(0); issue(1); issue(2); issue(3);
#pragma unroll
  for (int kb = 0; kb < 9; ++kb) {
    if (kb <= 5) WAITV(12); else if (kb == 6) WAITV(8); else if (kb == 7) WAITV(4); else WAITV(0);
    BAR();
    if (kb == 0) STAMP(ws, 0, 1);
    if (kb + 4 < 9) issue(kb + 4);
    const char* sl = smem + (kb % K1_NS) * K1_SLOT;
    {
      u32x4 v = *(const u32x4*)(sl + tid * 16);
      if (kb == 0) shift = bf_lo(v[0]);
#pragma unroll
      for (int i = 0; i < 4; ++i) {
        f32x2 d = {bf_lo(v[i]) - shift, bf_hi(v[i]) - shift};
        s1 += d; s2 += d * d;
      }
    }
#pragma unroll
    for (int ks = 0; ks < 2; ++ks) {
      bf16x8 a0 = *(const bf16x8*)(sl + (ax[0] ^ (ks * 64)));
      bf16x8 a1 = *(const bf16x8*)(sl + (ax[1] ^ (ks * 64)));
#pragma unroll
      for (int n = 0; n < NF; ++n) {
        bf16x8 b = *(const bf16x8*)(sl + ((aw ^ (ks * 64)) + n * 2048));
        acc[0][n] = MFMA16(a0, b, acc[0][n]);
        acc[1][n] = MFMA16(a1, b, acc[1][n]);
        wsum[n] = dot_frag(b, wsum[n]);
      }
    }
#pragma unroll
    for (int n = 0; n < NF; ++n) PIN(wsum[n]);
    PIN(s1); PIN(s2);
    WAITL0();
    SCHED();
  }
  STAMP(ws, 0, 2);
  {
    const float a1 = s1[0] + s1[1], a2 = s2[0] + s2[1];
    float mean = shift + a1 * (1.f / 72.f);
    float m2 = a2 - a1 * a1 * (1.f / 72.f);
    float n = 72.f;
#pragma unroll
    for (int o = 1; o < 8; o <<= 1) {
      const float mo = __shfl_xor(mean, o), qo = __shfl_xor(m2, o);
      const float dl = mo - mean;
      m2 = m2 + qo + dl * dl * (n * 0.5f);
      mean = 0.5f * (mean + mo);
      n *= 2.f;
    }
    const float r = __builtin_amdgcn_rsqf(m2 * (1.f / 576.f) + LN_EPS);
    if (c8 == 0) *(f32x2*)(smem + K1_NS * K1_SLOT + (tid >> 3) * 8) = f32x2{r, mean * r};
  }
  WAITL0();
  BAR();
  f32x2 rs[8];
  {
    const char* p = smem + K1_NS * K1_SLOT + (32 * sh + 8 * q) * 8;
#pragma unroll
    for (int i = 0; i < 4; ++i) {
      f32x4 v = *(const f32x4*)(p + i * 16);
      rs[2 * i] = f32x2{v[0], v[1]}; rs[2 * i + 1] = f32x2{v[2], v[3]};
    }
  }
  const float sgn = et < 4 ? 1.f : -1.f;
  const int kt = (et < 4 ? 0 : 16) + (st >> 1), ksg = (st & 1) * 2 + sh;
#pragma unroll
  for (int n = 0; n < NF; ++n) {
    float ws = wsum[n];
    ws += __shfl_xor(ws, 16); ws += __shfl_xor(ws, 32);
    float o[8];
#pragma unroll
    for (int m = 0; m < 2; ++m)
#pragma unroll
      for (int j = 0; j < 4; ++j) o[4 * m + j] = sgn * (acc[m][n][j] * rs[4 * m + j][0] - rs[4 * m + j][1] * ws);
    u32x4 pk = {pk_bf16(o[0], o[1]), pk_bf16(o[2], o[3]), pk_bf16(o[4], o[5]), pk_bf16(o[6], o[7])};
    const int el = (nf0 + n) * 16 + r16;
    const int hi = el >= 72 ? 1 : 0, e72 = el - 72 * hi;
    const int etg = (et & 3) * 2 + hi;
    const unsigned inner = e72 < 64 ? (unsigned)((e72 >> 4) * 1024 + q * 256 + (e72 & 15) * 16) : (unsigned)(4096 + q * 128 + (e72 & 7) * 16);
    const unsigned off = (unsigned)((etg * 32 + kt) * 4 + ksg) * 4608u + inner;
    __builtin_amdgcn_raw_buffer_store_b128(pk, rsO, off, 0, AUX_SC1);
  }
  STAMP(ws, 0, 3);
}

__global__ __launch_bounds__(512, 2) void k1_hid(const bf16_t* x, const bf16_t* wre, const bf16_t* wim, char* ws) {
  __shared__ __attribute__((aligned(16))) char smem[K1_LDS];
  const int wid = __builtin_amdgcn_readfirstlane(threadIdx.x >> 6), lane = threadIdx.x & 63;
  const int b = blockIdx.x, xcd = b & 7, i = b >> 3;
  const int st = xcd * 4 + (i & 3), et = i >> 2;
  const u32x4 rsX = mksrd(x, S_ * D_ * 2);
  const u32x4 rsW = mksrd(et < 4 ? wre : wim, D_ * D_ * 2);
  rsrc_t rsO = MKRS(ws + WS_BP, BP_BYTES);
  const int w4 = wid & 3;
  if (wid == 0 || wid == 7) k1_body<3>(smem, rsX, rsW, rsO, st, et, wid, lane, wid == 0 ? 0 : 6, ws);
  else k1_body<2>(smem, rsX, rsW, rsO, st, et, wid, lane, wid < 4 ? 1 + 2 * w4 : 2 * w4, ws);
}

constexpr int K2_SLOT = 16384 + 18432, K2_XOFF = 4 * K2_SLOT, K2_LDS = K2_XOFF + 9216;
constexpr int K2_RSTR = 336, K2_RKQ = 64 * K2_RSTR, K2_SOFF = 4 * K2_RKQ;

__global__ __launch_bounds__(512, 2) void k2_seq(const bf16_t* x, const bf16_t* cre, const bf16_t* cim, char* ws) {
  __shared__ __attribute__((aligned(16))) char smem[K2_LDS];
  const int wid = __builtin_amdgcn_readfirstlane(threadIdx.x >> 6), lane = threadIdx.x & 63;
  const int b = blockIdx.x, xcd = b & 7, i = b >> 3;
  const int mt = (xcd >> 1) * 8 + (i & 7), nt = (xcd & 1) * 4 + (i >> 3);
  const u32x4 rsRe = mksrd(cre, S_ * S_ * 2), rsIm = mksrd(cim, S_ * S_ * 2);
  const u32x4 rsB = mksrd(ws + WS_BP, BP_BYTES);
  const u32x4 rsX = mksrd(x, S_ * D_ * 2);
  const int q = lane >> 4, r16 = lane & 15, r8 = lane >> 3, c8 = lane & 7;
  const int mh = wid & 1, kq = wid >> 1;

  const unsigned va = (unsigned)(64 * mt + 8 * wid + r8) * 4096u + (unsigned)((c8 ^ r8) * 16);
  const unsigned vb = (unsigned)(wid * 1024 + lane * 16);
  const unsigned vb2 = (unsigned)(16384 + wid * 256 + lane * 16);
  const unsigned mA = lds_base(smem) + wid * 1024, mP = lds_base(smem) + wid * 256;
  auto issue = [&](int j, u32x4 rsA) __attribute__((always_inline)) {
    const unsigned so = (unsigned)((j & 3) * K2_SLOT), m0 = mA + so, m1 = mP + so;
    const int sa = (j & 15) * 256, sb = (nt * 32 + j) * 18432;
    GLDS(rsA, m0, 0, va, sa);
    GLDS(rsA, m0, 8192, va, sa + 128);
    GLDS(rsB, m0, 16384, vb, sb);
    GLDS(rsB, m0, 16384 + 8192, vb, sb + 8192);
    GLDS_N(0xffff, rsB, m1, 16384 + 16384, vb2, sb);
  };
  const unsigned at0 = (unsigned)((kq >> 1) * 8192 + (32 * mh + r16) * 128 + ((((kq & 1) * 4 + q) ^ (r16 & 7)) * 16));
  const unsigned ae0 = (unsigned)(16384 + kq * 4608 + lane * 16);
  const unsigned aeh = (unsigned)(16384 + kq * 4608 + 4096 + q * 128 + (lane & 7) * 16);

  f32x4 acc[5][2];
#pragma unroll
  for (int f = 0; f < 5; ++f) { acc[f][0] = f32x4{0, 0, 0, 0}; acc[f][1] = f32x4{0, 0, 0, 0}; }
  auto compute = [&](int j) {
    const char* sl = smem + (j & 3) * K2_SLOT;
    bf16x8 t0 = *(const bf16x8*)(sl + at0), t1 = *(const bf16x8*)(sl + at0 + 2048);
    bf16x8 e[5];
#pragma unroll
    for (int f = 0; f < 4; ++f) e[f] = *(const bf16x8*)(sl + ae0 + f * 1024);
    e[4] = *(const bf16x8*)(sl + aeh);
#pragma unroll
    for (int f = 0; f < 5; ++f) { acc[f][0] = MFMA16(e[f], t0, acc[f][0]); acc[f][1] = MFMA16(e[f], t1, acc[f][1]); }
    WAITL0();
    SCHED();
  };

  STAMP(ws, 1, 0);
  issue(0, rsRe); issue(1, rsRe); issue(2, rsRe);
  for (int j = 0; j < 13; ++j) { WAITV(10); BAR(); if (j == 0) STAMP(ws, 1, 1); issue(j + 3, rsRe); compute(j); }
  for (int j = 13; j < 29; ++j) { WAITV(10); BAR(); issue(j + 3, rsIm); compute(j); }
  WAITV(10); BAR();
  {
    const int o0 = wid * 1024 + lane * 16, o1 = 8192 + wid * 128 + lane * 16;
    const int row0 = o0 / 144, row1 = o1 / 144;
    GLDS(rsX, mA, K2_XOFF, (unsigned)((64 * mt + row0) * 1152 + nt * 144 + (o0 - row0 * 144)), 0);
    GLDS_N(0xff, rsX, lds_base(smem) + wid * 128, K2_XOFF + 8192, (unsigned)((64 * mt + row1) * 1152 + nt * 144 + (o1 - row1 * 144)), 0);
  }
  compute(29);
  WAITV(7); BAR(); compute(30);
  WAITV(2); BAR(); compute(31);
  WAITV(0); BAR();
  STAMP(ws, 1, 2);
#pragma unroll
  for (int f = 0; f < 5; ++f)
#pragma unroll
    for (int tf = 0; tf < 2; ++tf)
      *(f32x4*)(smem + kq * K2_RKQ + (32 * mh + 16 * tf + r16) * K2_RSTR + (16 * f + 4 * q) * 4) = acc[f][tf];
  WAITL0(); BAR();
  const rsrc_t rsH = MKRS(ws + WS_HT, HT_BYTES);
  auto item = [&](int U) {
    const int t = lane;
    f32x4 lo = {0, 0, 0, 0}, hi = {0, 0, 0, 0};
#pragma unroll
    for (int k = 0; k < 4; ++k) {
      const char* p = smem + k * K2_RKQ + t * K2_RSTR + U * 32;
      lo += *(const f32x4*)p; hi += *(const f32x4*)(p + 16);
    }
    const u32x4 xv = *(const u32x4*)(smem + K2_XOFF + t * 144 + U * 16);
    u32x4 hv = {pk_bf16(lo[0] + bf_lo(xv[0]), lo[1] + bf_hi(xv[0])), pk_bf16(lo[2] + bf_lo(xv[1]), lo[3] + bf_hi(xv[1])),
                pk_bf16(hi[0] + bf_lo(xv[2]), hi[1] + bf_hi(xv[2])), pk_bf16(hi[2] + bf_lo(xv[3]), hi[3] + bf_hi(xv[3]))};
    float v[8] = {bf_lo(hv[0]), bf_hi(hv[0]), bf_lo(hv[1]), bf_hi(hv[1]), bf_lo(hv[2]), bf_hi(hv[2]), bf_lo(hv[3]), bf_hi(hv[3])};
    float m = 0.f;
#pragma unroll
    for (int k = 0; k < 8; ++k) m += v[k];
    m *= 0.125f;
    float m2 = 0.f;
#pragma unroll
    for (int k = 0; k < 8; ++k) { const float d = v[k] - m; m2 += d * d; }
    *(f32x2*)(smem + K2_SOFF + (t * 9 + U) * 8) = f32x2{m, m2};
    const int Ug = nt * 9 + U, tg = 64 * mt + t;
    if (Ug & 1) hv = u32x4{hv[2], hv[3], hv[0], hv[1]};
    __builtin_amdgcn_raw_buffer_store_b128(hv, rsH, (unsigned)(((tg >> 4) * 72 + Ug) * 256 + (tg & 15) * 16), 0, AUX_SC1);
  };
  item(wid);
  if (wid == 0) item(8);
  WAITL0(); BAR();
  if (wid == 0) {
    const char* p = smem + K2_SOFF + lane * 72;
    f32x2 s[9];
    float m = 0.f;
#pragma unroll
    for (int k = 0; k < 9; ++k) { s[k] = *(const f32x2*)(p + k * 8); m += s[k][0]; }
    m *= (1.f / 9.f);
    float m2 = 0.f;
#pragma unroll
    for (int k = 0; k < 9; ++k) { const float d = s[k][0] - m; m2 += s[k][1] + 8.f * d * d; }
    const rsrc_t rsS = MKRS(ws + WS_ST, ST_BYTES);
    __builtin_amdgcn_raw_buffer_store_b64(__builtin_bit_cast(u32x2, f32x2{m, m2}), rsS, (unsigned)(((64 * mt + lane) * 8 + nt) * 8), 0, AUX_SC1);
  }
  STAMP(ws, 1, 3);
}

constexpr int K3_SLOT = 16384 + 18432, K3_AUX = 4 * K3_SLOT, K3_TOK = K3_AUX + 16384, K3_LDS = K3_TOK + 2048;
constexpr int K3_CS = K3_AUX;

#define DSR128(dst, addr, off) asm volatile("ds_read_b128 %0, %1 offset:%2" : "=v"(dst) : "v"(addr), "i"(off))
#define DSRTR(dst, addr, off) asm volatile("ds_read_b64_tr_b16 %0, %1 offset:%2" : "=v"(dst) : "v"(addr), "i"(off))

template <int NCH, bool LEAD>
__device__ __forceinline__ void k3_body(char* smem, u32x4 rsH, u32x4 rsG, u32x4 rsU, u32x4 rsS, rsrc_t rsO, int tt, int ft, int wid, int lane, char* ws) {
  STAMP(ws, 2, 0);
  const int tg = wid & 3, q = lane >> 4, r16 = lane & 15;
  const int odd = ft & 1;
  const int p0 = odd;
  const int cL = odd ? 0 : 8;
  const unsigned vt = (unsigned)((tt * 16 + wid) * 72 * 256 + lane * 16);
  unsigned vw0, vw1, vw2;
  {
    const int o0 = wid * 1024 + lane * 16, r0 = o0 / 288;
    vw0 = (unsigned)(r0 * 9216 + ft * 288 + (o0 - r0 * 288));
    const int o1 = (wid == 0 ? 8192 : (wid - 1) * 1024) + lane * 16, r1 = o1 / 288;
    vw1 = (unsigned)(r1 * 9216 + ft * 288 + (o1 - r1 * 288));
    const int o2 = 7168 + wid * 256 + lane * 16, r2 = o2 / 288;
    vw2 = (unsigned)(r2 * 9216 + ft * 288 + (o2 - r2 * 288));
  }
  const u32x4 rsW1 = wid == 0 ? rsG : rsU;
  const unsigned mA = lds_base(smem) + wid * 1024, mP = lds_base(smem) + wid * 256;
  auto issue = [&](int j) __attribute__((always_inline)) {
    const unsigned so = (unsigned)((j & 3) * K3_SLOT), m0 = mA + so, m1 = mP + so;
    GLDS(rsH, m0, 0, vt, j * 1024);
    GLDS(rsH, m0, 8192, vt, j * 1024 + 8 * 72 * 256);
    GLDS(rsG, m0, 16384, vw0, j * 294912);
    GLDS(rsW1, m0, 16384 + 8192, vw1, j * 294912);
    GLDS_N(0xffff, rsU, m1, 16384 + 16384, vw2, j * 294912);
  };
  const unsigned lds0 = lds_base(smem);
  const unsigned atok = lds0 + (unsigned)(tg * 4096 + lane * 16);
  const int a4 = r16 >> 2, p4 = r16 & 3;
  const int cfirst = LEAD ? p0 : p0 + 4;
  const unsigned aw1 = lds0 + (unsigned)(16384 + (8 * q + 4 * (q & 1) + a4) * 288 + cfirst * 32 + p4 * 8);
  const unsigned aw2 = lds0 + (unsigned)(16384 + (8 * q + 4 * (1 - (q & 1)) + a4) * 288 + cfirst * 32 + p4 * 8);
  const int dL = (cL - cfirst) * 32;

  f32x4 ag[NCH][4], au[NCH][4];
#pragma unroll
  for (int c = 0; c < NCH; ++c)
#pragma unroll
    for (int t = 0; t < 4; ++t) { ag[c][t] = f32x4{0, 0, 0, 0}; au[c][t] = f32x4{0, 0, 0, 0}; }
  float cs[2 * NCH];
#pragma unroll
  for (int c = 0; c < 2 * NCH; ++c) cs[c] = 0.f;

  bf16x8 tk[4];
  s16x4 wl[2 * NCH], wh[2 * NCH];

  auto reads = [&](int j) {
    const unsigned so = (unsigned)((j & 3) * K3_SLOT);
    const unsigned bt = atok + so, b1 = aw1 + so, b2 = aw2 + so;
    DSR128(tk[0], bt, 0); DSR128(tk[1], bt, 1024); DSR128(tk[2], bt, 2048); DSR128(tk[3], bt, 3072);
#pragma unroll
    for (int c = 0; c < 4; ++c) {
      DSRTR(wl[2 * c], b1, c * 32); DSRTR(wh[2 * c], b2, c * 32);
      DSRTR(wl[2 * c + 1], b1, c * 32 + 9216); DSRTR(wh[2 * c + 1], b2, c * 32 + 9216);
    }
    if constexpr (NCH == 5) {
      const unsigned l1 = b1 + (unsigned)dL, l2 = b2 + (unsigned)dL;
      DSRTR(wl[8], l1, 0); DSRTR(wh[8], l2, 0);
      DSRTR(wl[9], l1, 9216); DSRTR(wh[9], l2, 9216);
    }
  };
  auto landed = [&]() {
    asm volatile("s_waitcnt lgkmcnt(0)" : "+v"(tk[0]), "+v"(tk[1]), "+v"(tk[2]), "+v"(tk[3]), "+v"(wl[0]), "+v"(wh[0]), "+v"(wl[1]), "+v"(wh[1]));
    asm volatile("" : "+v"(wl[2]), "+v"(wh[2]), "+v"(wl[3]), "+v"(wh[3]), "+v"(wl[4]), "+v"(wh[4]), "+v"(wl[5]), "+v"(wh[5]));
    asm volatile("" : "+v"(wl[6]), "+v"(wh[6]), "+v"(wl[7]), "+v"(wh[7]));
    if constexpr (NCH == 5) asm volatile("" : "+v"(wl[8]), "+v"(wh[8]), "+v"(wl[9]), "+v"(wh[9]));
  };
  auto wfrag = [&](int i) -> bf16x8 { return __builtin_shufflevector(wl[i], wh[i], 0, 1, 2, 3, 4, 5, 6, 7); };
  auto colsum = [&](int j) __attribute__((always_inline)) {
    if ((j & 3) == tg) {
#pragma unroll
      for (int i = 0; i < 2 * NCH; ++i) cs[i] = dot_frag(wfrag(i), cs[i]);
    }
#pragma unroll
    for (int i = 0; i < 2 * NCH; ++i) PIN(cs[i]);
  };
  auto cs_publish = [&]() __attribute__((always_inline)) {
#pragma unroll
    for (int i = 0; i < 2 * NCH; ++i) {
      float v = cs[i];
      v += __shfl_xor(v, 16); v += __shfl_xor(v, 32);
      if (lane < 16) *(float*)(smem + K3_CS + (tg * 18 + (LEAD ? 0 : 10) + i) * 64 + lane * 4) = v;
    }
  };
  auto mma = [&]() {
    __builtin_amdgcn_s_setprio(1);
#pragma unroll
    for (int c = 0; c < NCH; ++c) {
      const bf16x8 g = wfrag(2 * c), u = wfrag(2 * c + 1);
#pragma unroll
      for (int t = 0; t < 4; ++t) { ag[c][t] = MFMA16(g, tk[t], ag[c][t]); au[c][t] = MFMA16(u, tk[t], au[c][t]); }
    }
    __builtin_amdgcn_s_setprio(0);
  };

  issue(0);
  GLDS(rsS, mA, K3_AUX, (unsigned)(tt * 16384 + wid * 1024 + lane * 16), 0);
  GLDS(rsS, mA, K3_AUX + 8192, (unsigned)(tt * 16384 + wid * 1024 + lane * 16), 8192);
  issue(1); issue(2);
  WAITV(10); BAR();
  STAMP(ws, 2, 1);
  if constexpr (!LEAD) {
    const int t = tg * 64 + lane;
    const char* p = smem + K3_AUX + t * 64;
    f32x4 s[4];
    float m = 0.f;
#pragma unroll
    for (int k = 0; k < 4; ++k) { s[k] = *(const f32x4*)(p + k * 16); m += s[k][0] + s[k][2]; }
    m *= 0.125f;
    float m2 = 0.f;
#pragma unroll
    for (int k = 0; k < 4; ++k) { const float d0 = s[k][0] - m, d1 = s[k][2] - m; m2 += s[k][1] + s[k][3] + 72.f * (d0 * d0 + d1 * d1); }
    const float r = __builtin_amdgcn_rsqf(m2 * (1.f / 576.f) + LN_EPS);
    *(f32x2*)(smem + K3_TOK + t * 8) = f32x2{r, m};
    WAITL0();
    SCHED(); BAR(); SCHED();
  }
  auto vmwait = [&](int j) __attribute__((always_inline)) {
    if (j < 15) WAITV(10); else if (j == 15) WAITV(5); else WAITV(0);
  };
  unsigned ts0 = 0, ts1 = 0, ts2 = 0, ts3 = 0, ts4 = 0, ts5 = 0;
#pragma clang loop unroll(disable)
  for (int j = 0; j < 18; ++j) {
    TS(ts0, j);
    reads(j);
    if (j < 15) issue(j + 3);
    landed();
    TS(ts1, j);
    colsum(j);
    if (j == 17) { cs_publish(); WAITL0(); }
    if constexpr (!LEAD) vmwait(j);
    TS(ts2, j);
    SCHED(); BAR(); SCHED();
    TS(ts3, j);
    mma();
    TS(ts4, j);
    if constexpr (LEAD) vmwait(j);
    TS(ts5, j);
    SCHED();
    if (LEAD || j != 17) BAR();
    SCHED();
  }
  TS_OUT(ws, 2, wid, lane, 0, ts0); TS_OUT(ws, 2, wid, lane, 1, ts1); TS_OUT(ws, 2, wid, lane, 2, ts2);
  TS_OUT(ws, 2, wid, lane, 3, ts3); TS_OUT(ws, 2, wid, lane, 4, ts4); TS_OUT(ws, 2, wid, lane, 5, ts5);

  STAMP(ws, 2, 2);
  int tz = threadIdx.x;
  asm volatile("" : "+v"(tz));
  const int ln = tz & 63, eq = ln >> 4, e16 = ln & 15;
  f32x2 st[4];
#pragma unroll
  for (int t = 0; t < 4; ++t) st[t] = *(const f32x2*)(smem + K3_TOK + (tg * 64 + t * 16 + e16) * 8);
  constexpr float NLOG2E = -1.4426950408889634f;
  auto csum = [&](int i) __attribute__((always_inline)) -> f32x4 {
    const char* p = smem + K3_CS + ((LEAD ? 0 : 10) + i) * 64 + eq * 16;
    return (*(const f32x4*)p + *(const f32x4*)(p + 18 * 64)) + (*(const f32x4*)(p + 2 * 18 * 64) + *(const f32x4*)(p + 3 * 18 * 64));
  };
  auto actv = [&](int c, int t, f32x4 cg, f32x4 cu) __attribute__((always_inline)) -> u32x2 {
    const float r = st[t][0], mu = st[t][1];
    float o[4];
#pragma unroll
    for (int k = 0; k < 4; ++k) {
      const float g = (ag[c][t][k] - mu * cg[k]) * r;
      const float u = (au[c][t][k] - mu * cu[k]) * r;
      const float e = __builtin_amdgcn_exp2f(g * NLOG2E);
      o[k] = g * __builtin_amdgcn_rcpf(1.f + e) * u;
    }
    return u32x2{pk_bf16(o[0], o[1]), pk_bf16(o[2], o[3])};
  };
  const int fbase = ft * 144;
#pragma unroll
  for (int pr = 0; pr < 2; ++pr) {
    const int G = (fbase + (cfirst + 2 * pr) * 16) >> 5;
    const f32x4 cg0 = csum(4 * pr), cu0 = csum(4 * pr + 1), cg1 = csum(4 * pr + 2), cu1 = csum(4 * pr + 3);
#pragma unroll
    for (int t = 0; t < 4; ++t) {
      const u32x2 lo = actv(2 * pr, t, cg0, cu0), hi = actv(2 * pr + 1, t, cg1, cu1);
      const int T16 = tt * 16 + tg * 4 + t;
      __builtin_amdgcn_raw_buffer_store_b128(u32x4{lo[0], lo[1], hi[0], hi[1]}, rsO, (unsigned)((T16 * 144 + G) * 1024 + ln * 16), 0, AUX_SC1);
    }
    SCHED();
  }
  if constexpr (NCH == 5) {
    const int G = (fbase + cL * 16) >> 5, half = odd;
    const f32x4 cg0 = csum(8), cu0 = csum(9);
#pragma unroll
    for (int t = 0; t < 4; ++t) {
      const u32x2 v = actv(4, t, cg0, cu0);
      const int T16 = tt * 16 + tg * 4 + t;
      __builtin_amdgcn_raw_buffer_store_b64(v, rsO, (unsigned)((T16 * 144 + G) * 1024 + ln * 16 + half * 8), 0, AUX_SC1);
    }
  }
  STAMP(ws, 2, 3);
}

__global__ __launch_bounds__(512, 2) void k3_gu(const bf16_t* wg, const bf16_t* wu, char* ws) {
  __shared__ __attribute__((aligned(16))) char smem[K3_LDS];
  const int wid = __builtin_amdgcn_readfirstlane(threadIdx.x >> 6), lane = threadIdx.x & 63;
  const int b = blockIdx.x, xcd = b & 7, i = b >> 3;
  const int ft = xcd * 4 + (i & 3), tt = i >> 2;
  const u32x4 rsH = mksrd(ws + WS_HT, HT_BYTES), rsG = mksrd(wg, D_ * F_ * 2), rsU = mksrd(wu, D_ * F_ * 2);
  const u32x4 rsS = mksrd(ws + WS_ST, ST_BYTES);
  const rsrc_t rsO = MKRS(ws + WS_ACT, ACT_BYTES);
  if (wid < 4) k3_body<5, true>(smem, rsH, rsG, rsU, rsS, rsO, tt, ft, wid, lane, ws);
  else k3_body<4, false>(smem, rsH, rsG, rsU, rsS, rsO, tt, ft, wid, lane, ws);
}

constexpr int K4_SLOT = 16384 + 20480, K4_HOFF = 4 * K4_SLOT, K4_LDS = K4_HOFF + 10240;
constexpr int K4_RSTR = 272, K4_RKQ = 80 * K4_RSTR;

__global__ __launch_bounds__(512, 2) void k4_down(const bf16_t* wd, char* ws, bf16_t* out) {
  __shared__ __attribute__((aligned(16))) char smem[K4_LDS];
  const int wid = __builtin_amdgcn_readfirstlane(threadIdx.x >> 6), lane = threadIdx.x & 63;
  const int b = blockIdx.x, xcd = b & 7, i = b >> 3;
  const int id = xcd * 29 + (xcd < 2 ? xcd : 2) + i;
  const int tt = id / 9, et = id - tt * 9;
  const u32x4 rsW = mksrd(wd, F_ * D_ * 2), rsA = mksrd(ws + WS_ACT, ACT_BYTES), rsH = mksrd(ws + WS_HT, HT_BYTES);
  const int q = lane >> 4, r16 = lane & 15, r8 = lane >> 3, c8 = lane & 7;
  const int eh = wid & 1, kq = wid >> 1;

  const unsigned vw = (unsigned)((8 * wid + r8) * 1152 + et * 128 + ((c8 ^ (((r8 >> 1) & 3) << 1)) * 16));
  unsigned va0, va1, va2;
  {
    const int o0 = wid * 1024 + lane * 16, o1 = o0 + 8192, o2 = 16384 + wid * 512 + lane * 16;
    auto src = [&](int o) { int T = tt * 5 + (o >> 12); T = T < 128 ? T : 127; return (unsigned)(T * 144 * 1024 + (o & 4095)); };
    va0 = src(o0); va1 = src(o1); va2 = src(o2);
  }
  const unsigned lds0 = lds_base(smem);
  const unsigned mA = lds0 + wid * 1024, mP = lds0 + wid * 512;
  auto issue = [&](int j) __attribute__((always_inline)) {
    const unsigned so = (unsigned)((j & 3) * K4_SLOT), m0 = mA + so, m1 = mP + so;
    GLDS(rsW, m0, 0, vw, j * 147456);
    GLDS(rsW, m0, 8192, vw, j * 147456 + 73728);
    GLDS(rsA, m0, 16384, va0, j * 4096);
    GLDS(rsA, m0, 16384 + 8192, va1, j * 4096);
    GLDS_N(0xffffffff, rsA, m1, 16384 + 16384, va2, j * 4096);
  };
  const int a4 = r16 >> 2, p4 = r16 & 3;
  const int rowA = 32 * kq + 4 * q + a4, key = (rowA >> 1) & 3;
  const unsigned aw0 = lds0 + (unsigned)(rowA * 128 + (((2 * eh) ^ key) * 32) + p4 * 8);
  const unsigned aw1 = lds0 + (unsigned)(rowA * 128 + (((2 * eh + 1) ^ key) * 32) + p4 * 8);
  const unsigned aa = lds0 + (unsigned)(16384 + kq * 1024 + lane * 16);

  f32x4 acc[2][5];
#pragma unroll
  for (int t = 0; t < 5; ++t) { acc[0][t] = f32x4{0, 0, 0, 0}; acc[1][t] = f32x4{0, 0, 0, 0}; }
  auto compute = [&](int j) {
    const unsigned so = (unsigned)((j & 3) * K4_SLOT);
    const unsigned b0 = aw0 + so, b1 = aw1 + so, ba = aa + so;
    s16x4 w0l, w0h, w1l, w1h;
    bf16x8 tk[5];
    DSRTR(w0l, b0, 0); DSRTR(w0h, b0, 2048); DSRTR(w1l, b1, 0); DSRTR(w1h, b1, 2048);
    DSR128(tk[0], ba, 0); DSR128(tk[1], ba, 4096); DSR128(tk[2], ba, 8192); DSR128(tk[3], ba, 12288); DSR128(tk[4], ba, 16384);
    asm volatile("s_waitcnt lgkmcnt(0)" : "+v"(w0l), "+v"(w0h), "+v"(w1l), "+v"(w1h), "+v"(tk[0]), "+v"(tk[1]), "+v"(tk[2]), "+v"(tk[3]), "+v"(tk[4]));
    const bf16x8 w0 = __builtin_shufflevector(w0l, w0h, 0, 1, 2, 3, 4, 5, 6, 7), w1 = __builtin_shufflevector(w1l, w1h, 0, 1, 2, 3, 4, 5, 6, 7);
#pragma unroll
    for (int t = 0; t < 5; ++t) { acc[0][t] = MFMA16(w0, tk[t], acc[0][t]); acc[1][t] = MFMA16(w1, tk[t], acc[1][t]); }
    SCHED();
  };

  STAMP(ws, 3, 0);
  issue(0); issue(1); issue(2);
  for (int j = 0; j < 33; ++j) { WAITV(10); BAR(); if (j == 0) STAMP(ws, 3, 1); issue(j + 3); compute(j); }
  WAITV(10); BAR();
  {
    const int o0 = wid * 1024 + lane * 16, o1 = 8192 + wid * 256 + lane * 16;
    auto src = [&](int o) { int T = tt * 5 + (o >> 11); T = T < 128 ? T : 127; return (unsigned)((T * 72 + et * 8) * 256 + (o & 2047)); };
    GLDS(rsH, mA, K4_HOFF, src(o0), 0);
    GLDS_N(0xffff, rsH, lds0 + wid * 256, K4_HOFF + 8192, src(o1), 0);
  }
  compute(33);
  WAITV(7); BAR(); compute(34);
  WAITV(2); BAR(); compute(35);
  WAITV(0); BAR();
  STAMP(ws, 3, 2);
#pragma unroll
  for (int ef = 0; ef < 2; ++ef)
#pragma unroll
    for (int t = 0; t < 5; ++t)
      *(f32x4*)(smem + kq * K4_RKQ + (16 * t + r16) * K4_RSTR + (32 * eh + 16 * ef + 4 * q) * 4) = acc[ef][t];
  WAITL0(); BAR();
  const rsrc_t rsO = MKRS(out, S_ * D_ * 2);
  auto item = [&](int idx) {
    const int tok = idx >> 3, g8 = idx & 7;
    f32x4 lo = {0, 0, 0, 0}, hi = {0, 0, 0, 0};
#pragma unroll
    for (int k = 0; k < 4; ++k) {
      const char* p = smem + k * K4_RKQ + tok * K4_RSTR + g8 * 32;
      lo += *(const f32x4*)p; hi += *(const f32x4*)(p + 16);
    }
    u32x4 hv = *(const u32x4*)(smem + K4_HOFF + (tok >> 4) * 2048 + g8 * 256 + (tok & 15) * 16);
    if (g8 & 1) hv = u32x4{hv[2], hv[3], hv[0], hv[1]};
    const u32x4 ov = {pk_bf16(lo[0] + bf_lo(hv[0]), lo[1] + bf_hi(hv[0])), pk_bf16(lo[2] + bf_lo(hv[1]), lo[3] + bf_hi(hv[1])),
                      pk_bf16(hi[0] + bf_lo(hv[2]), hi[1] + bf_hi(hv[2])), pk_bf16(hi[2] + bf_lo(hv[3]), hi[3] + bf_hi(hv[3]))};
    const int tg = tt * 80 + tok;
    if (tg < S_) __builtin_amdgcn_raw_buffer_store_b128(ov, rsO, (unsigned)(tg * 1152 + et * 128 + g8 * 16), 0, 0);
  };
  item(wid * 64 + lane);
  if (wid < 2) item(512 + wid * 64 + lane);
  STAMP(ws, 3, 3);
}

extern "C" void kernel_launch(void* const* d_in, const int* in_sizes, int n_in, void* d_out, int out_size, void* d_ws, size_t ws_size,
                              hipStream_t stream) {
  const bf16_t* x = (const bf16_t*)d_in[0];
  const bf16_t* cre = (const bf16_t*)d_in[1];
  const bf16_t* cim = (const bf16_t*)d_in[2];
  const bf16_t* wre = (const bf16_t*)d_in[3];
  const bf16_t* wim = (const bf16_t*)d_in[4];
  const bf16_t* wg = (const bf16_t*)d_in[6];
  const bf16_t* wu = (const bf16_t*)d_in[7];
  const bf16_t* wd = (const bf16_t*)d_in[8];
  char* ws = (char*)d_ws;
  k1_hid<<<256, 512, 0, stream>>>(x, wre, wim, ws);
  k2_seq<<<256, 512, 0, stream>>>(x, cre, cim, ws);
  k3_gu<<<256, 512, 0, stream>>>(wg, wu, ws);
  k4_down<<<234, 512, 0, stream>>>(wd, ws, (bf16_t*)d_out);
}
```

```cpp
#include <hip/hip_runtime.h>
#include <stdint.h>

typedef unsigned short bf16_t;
typedef short bf16x8 __attribute__((ext_vector_type(8)));
typedef short s16x4 __attribute__((ext_vector_type(4)));
typedef float f32x4 __attribute__((ext_vector_type(4)));
typedef float f32x2 __attribute__((ext_vector_type(2)));
typedef unsigned u32x4 __attribute__((ext_vector_type(4)));
typedef unsigned u32x2 __attribute__((ext_vector_type(2)));
typedef __bf16 bf2_t __attribute__((ext_vector_type(2)));
typedef __amdgpu_buffer_rsrc_t rsrc_t;

constexpr int S_ = 2048, D_ = 576, F_ = 4608;
constexpr float LN_EPS = 1e-5f;

constexpr unsigned WS_BP = 0;
constexpr unsigned BP_BYTES = 8u * 32u * 18432u;
constexpr unsigned WS_HT = WS_BP + BP_BYTES;
constexpr unsigned HT_BYTES = 128u * 72u * 256u;
constexpr unsigned WS_ST = WS_HT + HT_BYTES;
constexpr unsigned ST_BYTES = 2048u * 8u * 8u;
constexpr unsigned WS_ACT = WS_ST + ST_BYTES;
constexpr unsigned ACT_BYTES = 128u * 144u * 1024u;
constexpr unsigned WS_CS = WS_ACT + ACT_BYTES;
constexpr unsigned CS_BYTES = 32u * 4608u;

#define LDSP(p) ((__attribute__((address_space(3))) void*)(p))
#define MKRS(p, n) __builtin_amdgcn_make_buffer_rsrc((void*)(p), 0, (n), 0x00020000)
#define GLDS(rs, m0base, imm, voff, soff)                                                              \
  asm volatile("s_add_u32 m0, %0, %1\n\ts_nop 0\n\tbuffer_load_dwordx4 %2, %3, %4 offen lds"          \
               ::"s"(m0base), "i"(imm), "v"(voff), "s"(rs), "s"(soff) : "memory", "scc")
#define GLDS_N(mask, rs, m0base, imm, voff, soff)                                                      \
  asm volatile("s_add_u32 m0, %0, %1\n\ts_mov_b64 exec, " #mask "\n\tbuffer_load_dwordx4 %2, %3, %4 offen lds\n\ts_mov_b64 exec, -1" \
               ::"s"(m0base), "i"(imm), "v"(voff), "s"(rs), "s"(soff) : "memory", "scc")
#define GLDS_36(rs, m0base, imm, voff, soff)                                                           \
  asm volatile("s_add_u32 m0, %0, %1\n\ts_mov_b32 exec_hi, 0xf\n\tbuffer_load_dwordx4 %2, %3, %4 offen lds\n\ts_mov_b32 exec_hi, -1" \
               ::"s"(m0base), "i"(imm), "v"(voff), "s"(rs), "s"(soff) : "memory", "scc")
#define TOUCH(rs, mscr, voff)                                                                          \
  asm volatile("s_mov_b32 m0, %0\n\ts_nop 0\n\tbuffer_load_dword %1, %2, 0 offen lds" ::"s"(mscr), "v"(voff), "s"(rs) : "memory")
__device__ __forceinline__ u32x4 mksrd(const void* p, unsigned n) {
  const unsigned long long a = (unsigned long long)p;
  return u32x4{(unsigned)a, (unsigned)(a >> 32) & 0xffffu, n, 0x00020000u};
}
__device__ __forceinline__ unsigned lds_base(char* smem) { return (unsigned)(size_t)LDSP(smem); }
#define WAITV(n) asm volatile("s_waitcnt vmcnt(" #n ")" ::: "memory")
#define WAITL0() asm volatile("s_waitcnt lgkmcnt(0)" ::: "memory")
#define PIN(x) asm volatile("" : "+v"(x))
#define BAR() __builtin_amdgcn_s_barrier()
#define SCHED() __builtin_amdgcn_sched_barrier(0)
#define MFMA16(a, b, c) __builtin_amdgcn_mfma_f32_16x16x32_bf16((a), (b), (c), 0, 0, 0)
constexpr int AUX_SC1 = 16;
#define K2_VAR 0
#define K4_VAR 0
#define TSX(var, j) do { } while (0)
#define TS_OUT(ws, kid, wid, lane, k, var) do { } while (0)
#define STAMP(ws, kid, slot) do { } while (0)

__device__ __forceinline__ unsigned pk_bf16(float lo, float hi) {
  f32x2 v = {lo, hi};
  bf2_t b = __builtin_convertvector(v, bf2_t);
  return __builtin_bit_cast(unsigned, b);
}
__device__ __forceinline__ float bf_lo(unsigned w) { return __uint_as_float(w << 16); }
__device__ __forceinline__ float bf_hi(unsigned w) { return __uint_as_float(w & 0xffff0000u); }
__device__ __forceinline__ float dot_ones(unsigned w, float acc) {
  const bf2_t ones = {(__bf16)1.0f, (__bf16)1.0f};
  return __builtin_amdgcn_fdot2_f32_bf16(__builtin_bit_cast(bf2_t, w), ones, acc, false);
}
__device__ __forceinline__ float dot_frag(bf16x8 f, float acc) {
  u32x4 u = __builtin_bit_cast(u32x4, f);
  acc = dot_ones(u[0], acc); acc = dot_ones(u[1], acc); acc = dot_ones(u[2], acc); acc = dot_ones(u[3], acc);
  return acc;
}

constexpr int K1_SLOT = 8192 + 18432, K1_NS = 5, K1_ST = K1_NS * K1_SLOT, K1_WS = K1_ST + 512, K1_SCR = K1_WS + 1152, K1_LDS = K1_SCR + 256;

template <int NF>
__device__ __forceinline__ void k1_body(char* smem, u32x4 rsX, u32x4 rsW, u32x4 rsC, rsrc_t rsO, int st, int et, int wid, int lane, int nf0,
                                        unsigned vhead, bool headx, char* ws) {
  STAMP(ws, 0, 0);
  const int tid = wid * 64 + lane;
  const int sh = wid >> 2, q = lane >> 4, r16 = lane & 15, r8 = lane >> 3, c8 = lane & 7;
  const unsigned vx = (unsigned)(64 * st + 8 * wid + r8) * 1152u + (unsigned)((c8 ^ ((r8 & 3) | ((wid & 1) << 2))) * 16);
  const unsigned vw = (unsigned)((et & 3) * 144 + 8 * wid + r8) * 1152u + (unsigned)((c8 ^ r8) * 16);
  const int r2 = 128 + 2 * wid + r8;
  const unsigned vw2 = (unsigned)((et & 3) * 144 + r2) * 1152u + (unsigned)((c8 ^ (r2 & 7)) * 16);
  const unsigned lds0 = lds_base(smem);
  const unsigned mA = lds0 + wid * 1024, mP = lds0 + wid * 256;
  auto issue1 = [&](int kb, int k) __attribute__((always_inline)) {
    const int so = (kb % K1_NS) * K1_SLOT;
    if (k == 0) GLDS(rsX, mA, so, vx, kb * 128);
    if (k == 1) GLDS(rsW, mA, so + 8192, vw, kb * 128);
    if (k == 2) GLDS(rsW, mA, so + 16384, vw, kb * 128 + 64 * 1152);
    if (k == 3) GLDS_N(0xffff, rsW, mP, so + 24576, vw2, kb * 128);
  };
  auto issue = [&](int kb) __attribute__((always_inline)) { issue1(kb, 0); issue1(kb, 1); issue1(kb, 2); issue1(kb, 3); };
  unsigned ax[2];
#pragma unroll
  for (int m = 0; m < 2; ++m) {
    const int row = 32 * sh + 8 * (r16 >> 2) + 4 * m + (r16 & 3);
    const int key = (row & 3) | ((row >> 1) & 4);
    ax[m] = (unsigned)(row * 128 + ((q ^ key) * 16)) ^ (unsigned)(sh * 64);
  }
  const unsigned aw = (unsigned)(8192 + (nf0 * 16 + r16) * 128 + ((q ^ (r16 & 7)) * 16)) ^ (unsigned)(sh * 64);

  f32x4 acc[2][NF];
  float wsum[NF];
#pragma unroll
  for (int n = 0; n < NF; ++n) { wsum[n] = 0.f; acc[0][n] = f32x4{0, 0, 0, 0}; acc[1][n] = f32x4{0, 0, 0, 0}; }
  float shift = 0.f;
  f32x2 s1 = {0.f, 0.f}, s2 = {0.f, 0.f};

  issue(0); issue(1);
  TOUCH(headx ? rsX : rsW, lds0 + K1_SCR, vhead);
  issue(2); issue(3);
#pragma unroll
  for (int kb = 0; kb < 9; ++kb) {
    if (kb <= 1) WAITV(13); else if (kb <= 4) WAITV(12); else if (kb == 5) WAITV(13); else if (kb == 6) WAITV(9); else if (kb == 7) WAITV(5); else WAITV(1);
    BAR();
    if (kb == 0) STAMP(ws, 0, 1);
    const char* sl = smem + (kb % K1_NS) * K1_SLOT;
    const bool stg = kb + 4 < 9;
    const u32x4 v = *(const u32x4*)(sl + tid * 16);
    bf16x8 a[2][2], b[2][NF];
#pragma unroll
    for (int ks = 0; ks < 2; ++ks) {
      a[ks][0] = *(const bf16x8*)(sl + (ax[0] ^ (ks * 64)));
      a[ks][1] = *(const bf16x8*)(sl + (ax[1] ^ (ks * 64)));
#pragma unroll
      for (int n = 0; n < NF; ++n) b[ks][n] = *(const bf16x8*)(sl + ((aw ^ (ks * 64)) + n * 2048));
    }
    SCHED();
    if (stg) { issue1(kb + 4, 0); issue1(kb + 4, 1); }
    SCHED();
    if (kb == 0) shift = bf_lo(v[0]);
#pragma unroll
    for (int i = 0; i < 4; ++i) {
      f32x2 d = {bf_lo(v[i]) - shift, bf_hi(v[i]) - shift};
      s1 += d; s2 += d * d;
    }
    PIN(s1); PIN(s2);
#pragma unroll
    for (int n = 0; n < NF; ++n) { acc[0][n] = MFMA16(a[0][0], b[0][n], acc[0][n]); acc[1][n] = MFMA16(a[0][1], b[0][n], acc[1][n]); }
    SCHED();
    if (stg) issue1(kb + 4, 2);
    SCHED();
#pragma unroll
    for (int n = 0; n < NF; ++n) { acc[0][n] = MFMA16(a[1][0], b[1][n], acc[0][n]); acc[1][n] = MFMA16(a[1][1], b[1][n], acc[1][n]); }
#pragma unroll
    for (int n = 0; n < NF; ++n) wsum[n] = dot_frag(b[0][n], wsum[n]);
#pragma unroll
    for (int n = 0; n < NF; ++n) PIN(wsum[n]);
    SCHED();
    if (stg) issue1(kb + 4, 3);
    if (kb == 4)
      TOUCH(rsC, lds0 + K1_SCR, (unsigned)(blockIdx.x * 32768 + (wid & 3) * 8192 + lane * 128));
    WAITL0();
    SCHED();
  }
  STAMP(ws, 0, 2);
  {
    const float a1 = s1[0] + s1[1], a2 = s2[0] + s2[1];
    float mean = shift + a1 * (1.f / 72.f);
    float m2 = a2 - a1 * a1 * (1.f / 72.f);
    float n = 72.f;
#pragma unroll
    for (int o = 1; o < 8; o <<= 1) {
      const float mo = __shfl_xor(mean, o), qo = __shfl_xor(m2, o);
      const float dl = mo - mean;
      m2 = m2 + qo + dl * dl * (n * 0.5f);
      mean = 0.5f * (mean + mo);
      n *= 2.f;
    }
    const float r = __builtin_amdgcn_rsqf(m2 * (1.f / 576.f) + LN_EPS);
    if (c8 == 0) *(f32x2*)(smem + K1_ST + (tid >> 3) * 8) = f32x2{r, mean * r};
#pragma unroll
    for (int n = 0; n < NF; ++n) {
      float w = wsum[n];
      w += __shfl_xor(w, 16); w += __shfl_xor(w, 32);
      if (q == 0) *(float*)(smem + K1_WS + sh * 576 + ((nf0 + n) * 16 + r16) * 4) = w;
    }
  }
  WAITL0();
  BAR();
  f32x2 rs[8];
  {
    const char* p = smem + K1_ST + (32 * sh + 8 * q) * 8;
#pragma unroll
    for (int i = 0; i < 4; ++i) {
      f32x4 v = *(const f32x4*)(p + i * 16);
      rs[2 * i] = f32x2{v[0], v[1]}; rs[2 * i + 1] = f32x2{v[2], v[3]};
    }
  }
  const float sgn = et < 4 ? 1.f : -1.f;
  const int kt = (et < 4 ? 0 : 16) + (st >> 1), ksg = (st & 1) * 2 + sh;
#pragma unroll
  for (int n = 0; n < NF; ++n) {
    const int el = (nf0 + n) * 16 + r16;
    const float ws = *(const float*)(smem + K1_WS + el * 4) + *(const float*)(smem + K1_WS + 576 + el * 4);
    float o[8];
#pragma unroll
    for (int m = 0; m < 2; ++m)
#pragma unroll
      for (int j = 0; j < 4; ++j) o[4 * m + j] = sgn * (acc[m][n][j] * rs[4 * m + j][0] - rs[4 * m + j][1] * ws);
    u32x4 pk = {pk_bf16(o[0], o[1]), pk_bf16(o[2], o[3]), pk_bf16(o[4], o[5]), pk_bf16(o[6], o[7])};
    const int hi = el >= 72 ? 1 : 0, e72 = el - 72 * hi;
    const int etg = (et & 3) * 2 + hi;
    const unsigned inner = e72 < 64 ? (unsigned)((e72 >> 4) * 1024 + q * 256 + (e72 & 15) * 16) : (unsigned)(4096 + q * 128 + (e72 & 7) * 16);
    const unsigned off = (unsigned)((etg * 32 + kt) * 4 + ksg) * 4608u + inner;
    __builtin_amdgcn_raw_buffer_store_b128(pk, rsO, off, 0, AUX_SC1);
  }
  WAITV(0);
  STAMP(ws, 0, 3);
}

__global__ __launch_bounds__(512, 2) void k1_hid(const bf16_t* x, const bf16_t* wre, const bf16_t* wim, const bf16_t* cre, const bf16_t* cim, char* ws) {
  __shared__ __attribute__((aligned(16))) char smem[K1_LDS];
  const int wid = __builtin_amdgcn_readfirstlane(threadIdx.x >> 6), lane = threadIdx.x & 63;
  const int b = blockIdx.x, xcd = b & 7, i = b >> 3;
  const int st = xcd * 4 + (i & 3), et = i >> 2;
  const u32x4 rsX = mksrd(x, S_ * D_ * 2);
  const u32x4 rsW = mksrd(et < 4 ? wre : wim, D_ * D_ * 2);
  const u32x4 rsC = mksrd(wid < 4 ? cre : cim, S_ * S_ * 2);
  rsrc_t rsO = MKRS(ws + WS_BP, BP_BYTES);
  unsigned vhead;
  if (wid == 7) {
    const int l = lane < 40 ? lane : 39, r = l / 5;
    vhead = (unsigned)((64 * st + 8 * et + r) * 1152 + (4 + l - r * 5) * 128);
  } else {
    int l = wid * 26 + (lane < 26 ? lane : 25);
    l = l < 180 ? l : 179;
    const int r = l / 5;
    vhead = (unsigned)(((et & 3) * 144 + 36 * (i & 3) + r) * 1152 + (4 + l - r * 5) * 128);
  }
  const int w4 = wid & 3;
  if (wid == 0 || wid == 7) k1_body<3>(smem, rsX, rsW, rsC, rsO, st, et, wid, lane, wid == 0 ? 0 : 6, vhead, wid == 7, ws);
  else k1_body<2>(smem, rsX, rsW, rsC, rsO, st, et, wid, lane, wid < 4 ? 1 + 2 * w4 : 2 * w4, vhead, false, ws);
}

constexpr int K2_SLOT = 16384 + 18432, K2_XOFF = 4 * K2_SLOT, K2_LDS = K2_XOFF + 9216;
constexpr int K2_RSTR = 336, K2_RKQ = 64 * K2_RSTR, K2_SOFF = 4 * K2_RKQ;

__device__ __forceinline__ int tid_cs(int wid, int lane) { return (wid - 1) * 64 + lane; }
constexpr int K2_CSO = 98304;
__global__ __launch_bounds__(512, 2) void k2_seq(const bf16_t* x, const bf16_t* cre, const bf16_t* cim, const bf16_t* wg, const bf16_t* wu, char* ws) {
  __shared__ __attribute__((aligned(16))) char smem[K2_LDS];
  const int wid = __builtin_amdgcn_readfirstlane(threadIdx.x >> 6), lane = threadIdx.x & 63;
  const int b = blockIdx.x, xcd = b & 7, i = b >> 3;
  const int mt = (xcd >> 1) * 8 + (i & 7), nt = (xcd & 1) * 4 + (i >> 3);
  const u32x4 rsRe = mksrd(cre, S_ * S_ * 2), rsIm = mksrd(cim, S_ * S_ * 2);
  const u32x4 rsB = mksrd(ws + WS_BP, BP_BYTES);
  const u32x4 rsX = mksrd(x, S_ * D_ * 2);
  const int q = lane >> 4, r16 = lane & 15, r8 = lane >> 3, c8 = lane & 7;
  const int mh = wid & 1, kq = wid >> 1;

  const unsigned va = (unsigned)(64 * mt + 8 * wid + r8) * 4096u + (unsigned)((c8 ^ r8) * 16);
  const unsigned vb = (unsigned)(wid * 1024 + lane * 16);
  const unsigned vb2 = (unsigned)(16384 + wid * 256 + lane * 16);
  const unsigned mA = lds_base(smem) + wid * 1024, mP = lds_base(smem) + wid * 256;
  auto issue1 = [&](int j, u32x4 rsA, int k) __attribute__((always_inline)) {
    const unsigned so = (unsigned)((j & 3) * K2_SLOT), m0 = mA + so, m1 = mP + so;
    const int sa = (j & 15) * 256, sb = (nt * 32 + j) * 18432;
    if (k == 0) GLDS(rsA, m0, 0, va, sa);
    if (k == 1) GLDS(rsA, m0, 8192, va, sa + 128);
    if (k == 2) GLDS(rsB, m0, 16384, vb, sb);
    if (k == 3) GLDS(rsB, m0, 16384 + 8192, vb, sb + 8192);
    if (k == 4) GLDS_N(0xffff, rsB, m1, 16384 + 16384, vb2, sb);
  };
  auto issue = [&](int j, u32x4 rsA) __attribute__((always_inline)) {
    issue1(j, rsA, 0); issue1(j, rsA, 1); issue1(j, rsA, 2); issue1(j, rsA, 3); issue1(j, rsA, 4);
  };
  const unsigned at0 = (unsigned)((kq >> 1) * 8192 + (32 * mh + r16) * 128 + ((((kq & 1) * 4 + q) ^ (r16 & 7)) * 16));
  const unsigned ae0 = (unsigned)(16384 + kq * 4608 + lane * 16);
  const unsigned aeh = (unsigned)(16384 + kq * 4608 + 4096 + q * 128 + (lane & 7) * 16);

  f32x4 acc[5][2];
#pragma unroll
  for (int f = 0; f < 5; ++f) { acc[f][0] = f32x4{0, 0, 0, 0}; acc[f][1] = f32x4{0, 0, 0, 0}; }
  auto stage = [&](int j, u32x4 rsA, bool stg) __attribute__((always_inline)) {
    const char* sl = smem + (j & 3) * K2_SLOT;
    bf16x8 t0 = *(const bf16x8*)(sl + at0), t1 = *(const bf16x8*)(sl + at0 + 2048);
    bf16x8 e[5];
#pragma unroll
    for (int f = 0; f < 4; ++f) e[f] = *(const bf16x8*)(sl + ae0 + f * 1024);
    e[4] = *(const bf16x8*)(sl + aeh);
    SCHED();
    if (stg) { issue1(j + 3, rsA, 0); issue1(j + 3, rsA, 1); }
    SCHED();
#pragma unroll
    for (int f = 0; f < 5; ++f) {
      acc[f][0] = MFMA16(e[f], t0, acc[f][0]); acc[f][1] = MFMA16(e[f], t1, acc[f][1]);
      SCHED();
      if (stg && f == 1) issue1(j + 3, rsA, 2);
      if (stg && f == 2) issue1(j + 3, rsA, 3);
      if (stg && f == 3) issue1(j + 3, rsA, 4);
      SCHED();
    }
  };
  auto compute = [&](int j) __attribute__((always_inline)) { stage(j, rsRe, false); };

  STAMP(ws, 1, 0);
  issue(0, rsRe); issue(1, rsRe); issue(2, rsRe);
  for (int j = 0; j < 13; ++j) { WAITV(10); BAR(); if (j == 0) STAMP(ws, 1, 1); stage(j, rsRe, true); }
  for (int j = 13; j < 29; ++j) { WAITV(10); BAR(); stage(j, rsIm, true); }
  WAITV(10); BAR();
  {
    const int o0 = wid * 1024 + lane * 16, o1 = 8192 + wid * 128 + lane * 16;
    const int row0 = o0 / 144, row1 = o1 / 144;
    GLDS(rsX, mA, K2_XOFF, (unsigned)((64 * mt + row0) * 1152 + nt * 144 + (o0 - row0 * 144)), 0);
    GLDS_N(0xff, rsX, lds_base(smem) + wid * 128, K2_XOFF + 8192, (unsigned)((64 * mt + row1) * 1152 + nt * 144 + (o1 - row1 * 144)), 0);
  }
  const int cft = xcd * 4 + (i & 3), cm = (i >> 2) & 1, crg = i >> 3;
  u32x4 wv[6];
  {
    const u32x4 rsW = mksrd(cm ? wu : wg, D_ * F_ * 2);
    const int l54 = lane < 54 ? lane : 53, r3 = l54 / 18;
    const unsigned vo = (unsigned)((crg * 144 + wid * 18 + r3) * 9216 + cft * 288 + (l54 - r3 * 18) * 16);
#pragma unroll
    for (int k = 0; k < 6; ++k)
      asm volatile("buffer_load_dwordx4 %0, %1, %2, %3 offen" : "=v"(wv[k]) : "v"(vo), "s"(rsW), "s"(k * 3 * 9216) : "memory");
  }
  compute(29);
  WAITV(13); BAR(); compute(30);
  WAITV(8); BAR(); compute(31);
  WAITV(6); BAR();
  STAMP(ws, 1, 2);
#pragma unroll
  for (int f = 0; f < 5; ++f)
#pragma unroll
    for (int tf = 0; tf < 2; ++tf)
      *(f32x4*)(smem + kq * K2_RKQ + (32 * mh + 16 * tf + r16) * K2_RSTR + (16 * f + 4 * q) * 4) = acc[f][tf];
  asm volatile("s_waitcnt vmcnt(0)" : "+v"(wv[0]), "+v"(wv[1]), "+v"(wv[2]), "+v"(wv[3]), "+v"(wv[4]), "+v"(wv[5]) :: "memory");
  {
    f32x4 lo = {0, 0, 0, 0}, hi = {0, 0, 0, 0};
#pragma unroll
    for (int k = 0; k < 6; ++k) {
      lo += f32x4{bf_lo(wv[k][0]), bf_hi(wv[k][0]), bf_lo(wv[k][1]), bf_hi(wv[k][1])};
      hi += f32x4{bf_lo(wv[k][2]), bf_hi(wv[k][2]), bf_lo(wv[k][3]), bf_hi(wv[k][3])};
    }
    if (lane < 54) {
      char* p = smem + K2_CSO + (wid * 3 * 144) * 4 + lane * 32;
      *(f32x4*)p = lo; *(f32x4*)(p + 16) = hi;
    }
  }
  WAITL0(); BAR();
  const rsrc_t rsH = MKRS(ws + WS_HT, HT_BYTES);
  auto item = [&](int U) {
    const int t = lane;
    f32x4 lo = {0, 0, 0, 0}, hi = {0, 0, 0, 0};
#pragma unroll
    for (int k = 0; k < 4; ++k) {
      const char* p = smem + k * K2_RKQ + t * K2_RSTR + U * 32;
      lo += *(const f32x4*)p; hi += *(const f32x4*)(p + 16);
    }
    const u32x4 xv = *(const u32x4*)(smem + K2_XOFF + t * 144 + U * 16);
    u32x4 hv = {pk_bf16(lo[0] + bf_lo(xv[0]), lo[1] + bf_hi(xv[0])), pk_bf16(lo[2] + bf_lo(xv[1]), lo[3] + bf_hi(xv[1])),
                pk_bf16(hi[0] + bf_lo(xv[2]), hi[1] + bf_hi(xv[2])), pk_bf16(hi[2] + bf_lo(xv[3]), hi[3] + bf_hi(xv[3]))};
    float v[8] = {bf_lo(hv[0]), bf_hi(hv[0]), bf_lo(hv[1]), bf_hi(hv[1]), bf_lo(hv[2]), bf_hi(hv[2]), bf_lo(hv[3]), bf_hi(hv[3])};
    float m = 0.f;
#pragma unroll
    for (int k = 0; k < 8; ++k) m += v[k];
    m *= 0.125f;
    float m2 = 0.f;
#pragma unroll
    for (int k = 0; k < 8; ++k) { const float d = v[k] - m; m2 += d * d; }
    *(f32x2*)(smem + K2_SOFF + (t * 9 + U) * 8) = f32x2{m, m2};
    const int Ug = nt * 9 + U, tg = 64 * mt + t;
    if (Ug & 1) hv = u32x4{hv[2], hv[3], hv[0], hv[1]};
    __builtin_amdgcn_raw_buffer_store_b128(hv, rsH, (unsigned)(((tg >> 4) * 72 + Ug) * 256 + (tg & 15) * 16), 0, AUX_SC1);
  };
  item(wid);
  if (wid == 0) item(8);
  WAITL0(); BAR();
  if (wid == 0) {
    const char* p = smem + K2_SOFF + lane * 72;
    f32x2 s[9];
    float m = 0.f;
#pragma unroll
    for (int k = 0; k < 9; ++k) { s[k] = *(const f32x2*)(p + k * 8); m += s[k][0]; }
    m *= (1.f / 9.f);
    float m2 = 0.f;
#pragma unroll
    for (int k = 0; k < 9; ++k) { const float d = s[k][0] - m; m2 += s[k][1] + 8.f * d * d; }
    const rsrc_t rsS = MKRS(ws + WS_ST, ST_BYTES);
    __builtin_amdgcn_raw_buffer_store_b64(__builtin_bit_cast(u32x2, f32x2{m, m2}), rsS, (unsigned)(((64 * mt + lane) * 8 + nt) * 8), 0, AUX_SC1);
  }
  if (wid >= 1 && wid <= 3) {
    const int c = tid_cs(wid, lane);
    if (c < 144) {
      float a = 0.f;
#pragma unroll
      for (int k = 0; k < 24; ++k) a += *(const float*)(smem + K2_CSO + (k * 144 + c) * 4);
      const rsrc_t rsC = MKRS(ws + WS_CS, CS_BYTES);
      __builtin_amdgcn_raw_buffer_store_b32(__float_as_uint(a), rsC, (unsigned)(cft * 4608 + crg * 1152 + cm * 576 + c * 4), 0, AUX_SC1);
    }
  }
  STAMP(ws, 1, 3);
}

constexpr int K3_SLOT = 16384 + 18432, K3_AUX = 4 * K3_SLOT, K3_TOK = K3_AUX + 16384, K3_CS = K3_TOK + 2048, K3_LDS = K3_CS + 4608;

#define DSR128(dst, addr, off) asm volatile("ds_read_b128 %0, %1 offset:%2" : "=v"(dst) : "v"(addr), "i"(off))
#define DSRTR(dst, addr, off) asm volatile("ds_read_b64_tr_b16 %0, %1 offset:%2" : "=v"(dst) : "v"(addr), "i"(off))
template <int V> struct IC { static constexpr int value = V; };

template <int NCH>
__device__ __forceinline__ void k3_body(char* smem, u32x4 rsH, u32x4 rsG, u32x4 rsU, u32x4 rsS, u32x4 rsC, u32x4 rsD, rsrc_t rsO, int tt, int ft,
                                        int wid, int lane, char* ws) {
  STAMP(ws, 2, 0);
  constexpr bool LEAD = NCH == 5;
  const int tg = wid & 3, q = lane >> 4, r16 = lane & 15;
  const int odd = ft & 1;
  const int p0 = odd;
  const int cL = odd ? 0 : 8;
  const unsigned vt = (unsigned)((tt * 16 + wid) * 72 * 256 + lane * 16);
  unsigned vw0, vw1, vw2;
  {
    const int o0 = wid * 1024 + lane * 16, r0 = o0 / 288;
    vw0 = (unsigned)(r0 * 9216 + ft * 288 + (o0 - r0 * 288));
    const int o1 = (wid == 0 ? 8192 : (wid - 1) * 1024) + lane * 16, r1 = o1 / 288;
    vw1 = (unsigned)(r1 * 9216 + ft * 288 + (o1 - r1 * 288));
    const int o2 = 7168 + wid * 256 + lane * 16, r2 = o2 / 288;
    vw2 = (unsigned)(r2 * 9216 + ft * 288 + (o2 - r2 * 288));
  }
  const u32x4 rsW1 = wid == 0 ? rsG : rsU;
  const unsigned lds0 = lds_base(smem);
  const unsigned mA = lds0 + wid * 1024, mP = lds0 + wid * 256;
  auto issue1 = [&](int j, int k) __attribute__((always_inline)) {
    const unsigned so = (unsigned)((j & 3) * K3_SLOT), m0 = mA + so, m1 = mP + so;
    if (k == 0) GLDS(rsH, m0, 0, vt, j * 1024);
    if (k == 1) GLDS(rsH, m0, 8192, vt, j * 1024 + 8 * 72 * 256);
    if (k == 2) GLDS(rsG, m0, 16384, vw0, j * 294912);
    if (k == 3) GLDS(rsW1, m0, 16384 + 8192, vw1, j * 294912);
    if (k == 4) GLDS_N(0xffff, rsU, m1, 16384 + 16384, vw2, j * 294912);
  };
  auto issue = [&](int j) __attribute__((always_inline)) { issue1(j, 0); issue1(j, 1); issue1(j, 2); issue1(j, 3); issue1(j, 4); };
  const unsigned atok = lds0 + (unsigned)(tg * 4096 + lane * 16);
  const int a4 = r16 >> 2, p4 = r16 & 3;
  const int cfirst = LEAD ? p0 : p0 + 4;
  const unsigned aw1 = lds0 + (unsigned)(16384 + (8 * q + 4 * (q & 1) + a4) * 288 + cfirst * 32 + p4 * 8);
  const unsigned aw2 = lds0 + (unsigned)(16384 + (8 * q + 4 * (1 - (q & 1)) + a4) * 288 + cfirst * 32 + p4 * 8);
  const int dL = (cL - cfirst) * 32;

  f32x4 ag[NCH][4], au[NCH][4];
#pragma unroll
  for (int c = 0; c < NCH; ++c)
#pragma unroll
    for (int t = 0; t < 4; ++t) { ag[c][t] = f32x4{0, 0, 0, 0}; au[c][t] = f32x4{0, 0, 0, 0}; }

  bf16x8 tk[2][4];
  s16x4 w[2][4];
  auto rdw = [&](int buf, unsigned b1, unsigned b2, int off, int i) __attribute__((always_inline)) {
    if (i == 0) DSRTR(w[buf][0], b1, off);
    if (i == 1) DSRTR(w[buf][1], b2, off);
    if (i == 2) DSRTR(w[buf][2], b1, off + 9216);
    if (i == 3) DSRTR(w[buf][3], b2, off + 9216);
  };
  auto stage = [&](int j, auto P_) __attribute__((always_inline)) {
    constexpr int P = decltype(P_)::value;
    constexpr bool NXT = true;
    const bool STG = j < 15;
    const unsigned so = (unsigned)((j & 3) * K3_SLOT), sn = (unsigned)(((j + 1) & 3) * K3_SLOT);
    const unsigned b1 = aw1 + so, b2 = aw2 + so, n1 = aw1 + sn, n2 = aw2 + sn, nt = atok + sn;
    const unsigned l1 = b1 + (unsigned)dL, l2 = b2 + (unsigned)dL;
#pragma unroll
    for (int c = 0; c < NCH; ++c) {
      const int cb = LEAD ? ((P + c) & 1) : (c & 1), nb = cb ^ 1;
      const bool last = c == NCH - 1;
      if (c > 0) asm volatile("s_waitcnt lgkmcnt(0)" : "+v"(w[cb][0]), "+v"(w[cb][1]), "+v"(w[cb][2]), "+v"(w[cb][3]));
      if (c == 0) TSX(ts[7], j); else TSX(ts[c - 1], j);
      if (P == 0 && last && j == 0) { WAITV(9); BAR(); }
      const bf16x8 g = __builtin_shufflevector(w[cb][0], w[cb][1], 0, 1, 2, 3, 4, 5, 6, 7);
      const bf16x8 u = __builtin_shufflevector(w[cb][2], w[cb][3], 0, 1, 2, 3, 4, 5, 6, 7);
      SCHED();
#pragma unroll
      for (int t = 0; t < 4; ++t) {
        ag[c][t] = MFMA16(g, tk[P][t], ag[c][t]); au[c][t] = MFMA16(u, tk[P][t], au[c][t]);
        SCHED();
        if (!last) {
          if (t < 2) {
            const bool lc = LEAD && c == 3;
            rdw(nb, lc ? l1 : b1, lc ? l2 : b2, lc ? 0 : (c + 1) * 32, 2 * t);
            rdw(nb, lc ? l1 : b1, lc ? l2 : b2, lc ? 0 : (c + 1) * 32, 2 * t + 1);
          }
        } else if (NXT) {
          if (t == 0) { DSR128(tk[P ^ 1][0], nt, 0); rdw(nb, n1, n2, 0, 0); rdw(nb, n1, n2, 0, 1); }
          if (t == 1) { rdw(nb, n1, n2, 0, 2); rdw(nb, n1, n2, 0, 3); DSR128(tk[P ^ 1][1], nt, 1024); }
          if (t == 2) { DSR128(tk[P ^ 1][2], nt, 2048); DSR128(tk[P ^ 1][3], nt, 3072); }
        }
        if (STG) {
          if (t == 2) issue1(j + 3, LEAD ? c : (c == 0 ? 0 : c + 1));
          if (!LEAD && c == 0 && t == 3) issue1(j + 3, 1);
        }
        SCHED();
      }
    }
    if (NXT)
      asm volatile("s_waitcnt lgkmcnt(0)" : "+v"(tk[P ^ 1][0]), "+v"(tk[P ^ 1][1]), "+v"(tk[P ^ 1][2]), "+v"(tk[P ^ 1][3]),
                   "+v"(w[LEAD ? (P ^ 1) : 0][0]), "+v"(w[LEAD ? (P ^ 1) : 0][1]), "+v"(w[LEAD ? (P ^ 1) : 0][2]), "+v"(w[LEAD ? (P ^ 1) : 0][3]));
    TSX(ts[5], j);
  };

  issue(0);
  GLDS(rsS, mA, K3_AUX, (unsigned)(tt * 16384 + wid * 1024 + lane * 16), 0);
  GLDS(rsS, mA, K3_AUX + 8192, (unsigned)(tt * 16384 + wid * 1024 + lane * 16), 8192);
  GLDS_36(rsC, lds0 + wid * 576, K3_CS, (unsigned)(ft * 4608 + wid * 576 + lane * 16), 0);
  issue(1); issue(2);
  WAITV(10); BAR();
  STAMP(ws, 2, 1);
  DSR128(tk[0][0], atok, 0); DSR128(tk[0][1], atok, 1024); DSR128(tk[0][2], atok, 2048); DSR128(tk[0][3], atok, 3072);
  rdw(0, aw1, aw2, 0, 0); rdw(0, aw1, aw2, 0, 1); rdw(0, aw1, aw2, 0, 2); rdw(0, aw1, aw2, 0, 3);
  if constexpr (!LEAD) {
    const int t = tg * 64 + lane;
    const char* p = smem + K3_AUX + t * 64;
    f32x4 s[4];
    float m = 0.f;
#pragma unroll
    for (int k = 0; k < 4; ++k) { s[k] = *(const f32x4*)(p + k * 16); m += s[k][0] + s[k][2]; }
    m *= 0.125f;
    float m2 = 0.f;
#pragma unroll
    for (int k = 0; k < 4; ++k) { const float d0 = s[k][0] - m, d1 = s[k][2] - m; m2 += s[k][1] + s[k][3] + 72.f * (d0 * d0 + d1 * d1); }
    const float r = __builtin_amdgcn_rsqf(m2 * (1.f / 576.f) + LN_EPS);
    *(f32x2*)(smem + K3_TOK + t * 8) = f32x2{r, m};
  }
  asm volatile("s_waitcnt lgkmcnt(0)" : "+v"(tk[0][0]), "+v"(tk[0][1]), "+v"(tk[0][2]), "+v"(tk[0][3]), "+v"(w[0][0]), "+v"(w[0][1]), "+v"(w[0][2]), "+v"(w[0][3]) :: "memory");
  SCHED();

#pragma clang loop unroll(disable)
  for (int j = 0; j < 18; j += 2) {
    stage(j, IC<0>{});
    if (j < 14) WAITV(5);
    else if (j == 14) {
      int tz = threadIdx.x;
      asm volatile("" : "+v"(tz));
      int l = tz & 63;
      l = wid * 21 + (l < 20 ? l : 20);
      l = l < 161 ? l : 161;
      TOUCH(rsD, lds0 + K3_AUX, (unsigned)(blockIdx.x * 20736 + l * 128));
      WAITV(6);
    }
    if (j < 16) BAR();
    stage(j + 1, IC<1>{});
    if (j < 14) WAITV(5); else WAITV(1);
    if (j < 16) BAR();
  }

  STAMP(ws, 2, 2);
  int tz = threadIdx.x;
  asm volatile("" : "+v"(tz));
  const int ln = tz & 63, eq = ln >> 4, e16 = ln & 15;
  f32x2 st[4];
#pragma unroll
  for (int t = 0; t < 4; ++t) st[t] = *(const f32x2*)(smem + K3_TOK + (tg * 64 + t * 16 + e16) * 8);
  constexpr float NLOG2E = -1.4426950408889634f;
  auto csum = [&](int ch, int m) __attribute__((always_inline)) -> f32x4 {
    const char* p = smem + K3_CS + m * 576 + ch * 64 + eq * 16;
    return (*(const f32x4*)p + *(const f32x4*)(p + 1152)) + (*(const f32x4*)(p + 2 * 1152) + *(const f32x4*)(p + 3 * 1152));
  };
  auto actv = [&](int c, int t, f32x4 cg, f32x4 cu) __attribute__((always_inline)) -> u32x2 {
    const float r = st[t][0], mu = st[t][1];
    float o[4];
#pragma unroll
    for (int k = 0; k < 4; ++k) {
      const float g = (ag[c][t][k] - mu * cg[k]) * r;
      const float u = (au[c][t][k] - mu * cu[k]) * r;
      const float e = __builtin_amdgcn_exp2f(g * NLOG2E);
      o[k] = g * __builtin_amdgcn_rcpf(1.f + e) * u;
    }
    return u32x2{pk_bf16(o[0], o[1]), pk_bf16(o[2], o[3])};
  };
  const int fbase = ft * 144;
#pragma unroll
  for (int pr = 0; pr < 2; ++pr) {
    const int c0 = cfirst + 2 * pr;
    const int G = (fbase + c0 * 16) >> 5;
    const f32x4 cg0 = csum(c0, 0), cu0 = csum(c0, 1), cg1 = csum(c0 + 1, 0), cu1 = csum(c0 + 1, 1);
#pragma unroll
    for (int t = 0; t < 4; ++t) {
      const u32x2 lo = actv(2 * pr, t, cg0, cu0), hi = actv(2 * pr + 1, t, cg1, cu1);
      const int T16 = tt * 16 + tg * 4 + t;
      __builtin_amdgcn_raw_buffer_store_b128(u32x4{lo[0], lo[1], hi[0], hi[1]}, rsO, (unsigned)((T16 * 144 + G) * 1024 + ln * 16), 0, AUX_SC1);
    }
    SCHED();
  }
  if constexpr (NCH == 5) {
    const int G = (fbase + cL * 16) >> 5, half = odd;
    const f32x4 cg0 = csum(cL, 0), cu0 = csum(cL, 1);
#pragma unroll
    for (int t = 0; t < 4; ++t) {
      const u32x2 v = actv(4, t, cg0, cu0);
      const int T16 = tt * 16 + tg * 4 + t;
      __builtin_amdgcn_raw_buffer_store_b64(v, rsO, (unsigned)((T16 * 144 + G) * 1024 + ln * 16 + half * 8), 0, AUX_SC1);
    }
  }
  WAITV(0);
  STAMP(ws, 2, 3);
}

__global__ __launch_bounds__(512, 2) void k3_gu(const bf16_t* wg, const bf16_t* wu, const bf16_t* wd, char* ws) {
  __shared__ __attribute__((aligned(16))) char smem[K3_LDS];
  const int wid = __builtin_amdgcn_readfirstlane(threadIdx.x >> 6), lane = threadIdx.x & 63;
  const int b = blockIdx.x, xcd = b & 7, i = b >> 3;
  const int ft = xcd * 4 + (i & 3), tt = i >> 2;
  const u32x4 rsH = mksrd(ws + WS_HT, HT_BYTES), rsG = mksrd(wg, D_ * F_ * 2), rsU = mksrd(wu, D_ * F_ * 2);
  const u32x4 rsS = mksrd(ws + WS_ST, ST_BYTES), rsC = mksrd(ws + WS_CS, CS_BYTES), rsD = mksrd(wd, F_ * D_ * 2);
  const rsrc_t rsO = MKRS(ws + WS_ACT, ACT_BYTES);
  if (wid < 4) k3_body<5>(smem, rsH, rsG, rsU, rsS, rsC, rsD, rsO, tt, ft, wid, lane, ws);
  else k3_body<4>(smem, rsH, rsG, rsU, rsS, rsC, rsD, rsO, tt, ft, wid, lane, ws);
}

constexpr int K4_SLOT = 16384 + 20480, K4_HOFF = 4 * K4_SLOT, K4_LDS = K4_HOFF + 10240;
constexpr int K4_RSTR = 272, K4_RKQ = 80 * K4_RSTR;

__global__ __launch_bounds__(512, 2) void k4_down(const bf16_t* wd, char* ws, bf16_t* out) {
  __shared__ __attribute__((aligned(16))) char smem[K4_LDS];
  const int wid = __builtin_amdgcn_readfirstlane(threadIdx.x >> 6), lane = threadIdx.x & 63;
  const int b = blockIdx.x, xcd = b & 7, i = b >> 3;
  const int id = xcd * 29 + (xcd < 2 ? xcd : 2) + i;
  const int tt = id / 9, et = id - tt * 9;
  const u32x4 rsW = mksrd(wd, F_ * D_ * 2), rsA = mksrd(ws + WS_ACT, ACT_BYTES), rsH = mksrd(ws + WS_HT, HT_BYTES);
  const int q = lane >> 4, r16 = lane & 15, r8 = lane >> 3, c8 = lane & 7;
  const int eh = wid & 1, kq = wid >> 1;

  const unsigned vw = (unsigned)((8 * wid + r8) * 1152 + et * 128 + ((c8 ^ (((r8 >> 1) & 3) << 1)) * 16));
  unsigned va0, va1, va2;
  {
    const int o0 = wid * 1024 + lane * 16, o1 = o0 + 8192, o2 = 16384 + wid * 512 + lane * 16;
    auto src = [&](int o) { int T = tt * 5 + (o >> 12); T = T < 128 ? T : 127; return (unsigned)(T * 144 * 1024 + (o & 4095)); };
    va0 = src(o0); va1 = src(o1); va2 = src(o2);
  }
  const unsigned lds0 = lds_base(smem);
  const unsigned mA = lds0 + wid * 1024, mP = lds0 + wid * 512;
  auto issue1 = [&](int j, int k) __attribute__((always_inline)) {
    const unsigned so = (unsigned)((j & 3) * K4_SLOT), m0 = mA + so, m1 = mP + so;
    if (k == 0) GLDS(rsW, m0, 0, vw, j * 147456);
    if (k == 1) GLDS(rsW, m0, 8192, vw, j * 147456 + 73728);
    if (k == 2) GLDS(rsA, m0, 16384, va0, j * 4096);
    if (k == 3) GLDS(rsA, m0, 16384 + 8192, va1, j * 4096);
    if (k == 4) GLDS_N(0xffffffff, rsA, m1, 16384 + 16384, va2, j * 4096);
  };
  auto issue = [&](int j) __attribute__((always_inline)) { issue1(j, 0); issue1(j, 1); issue1(j, 2); issue1(j, 3); issue1(j, 4); };
  const int a4 = r16 >> 2, p4 = r16 & 3;
  const int rowA = 32 * kq + 4 * q + a4, key = (rowA >> 1) & 3;
  const unsigned aw0 = lds0 + (unsigned)(rowA * 128 + (((2 * eh) ^ key) * 32) + p4 * 8);
  const unsigned aw1 = lds0 + (unsigned)(rowA * 128 + (((2 * eh + 1) ^ key) * 32) + p4 * 8);
  const unsigned aa = lds0 + (unsigned)(16384 + kq * 1024 + lane * 16);

  f32x4 acc[2][5];
#pragma unroll
  for (int t = 0; t < 5; ++t) { acc[0][t] = f32x4{0, 0, 0, 0}; acc[1][t] = f32x4{0, 0, 0, 0}; }
  auto stage = [&](int j, bool stg) __attribute__((always_inline)) {
    const unsigned so = (unsigned)((j & 3) * K4_SLOT);
    const unsigned b0 = aw0 + so, b1 = aw1 + so, ba = aa + so;
    s16x4 w0l, w0h, w1l, w1h;
    bf16x8 tk[5];
    DSRTR(w0l, b0, 0); DSRTR(w0h, b0, 2048); DSRTR(w1l, b1, 0); DSRTR(w1h, b1, 2048);
    DSR128(tk[0], ba, 0); DSR128(tk[1], ba, 4096); DSR128(tk[2], ba, 8192); DSR128(tk[3], ba, 12288); DSR128(tk[4], ba, 16384);
    if (stg) { issue1(j + 3, 0); issue1(j + 3, 1); }
    asm volatile("s_waitcnt lgkmcnt(0)" : "+v"(w0l), "+v"(w0h), "+v"(w1l), "+v"(w1h), "+v"(tk[0]), "+v"(tk[1]), "+v"(tk[2]), "+v"(tk[3]), "+v"(tk[4]));
    const bf16x8 w0 = __builtin_shufflevector(w0l, w0h, 0, 1, 2, 3, 4, 5, 6, 7), w1 = __builtin_shufflevector(w1l, w1h, 0, 1, 2, 3, 4, 5, 6, 7);
    SCHED();
#pragma unroll
    for (int t = 0; t < 5; ++t) {
      acc[0][t] = MFMA16(w0, tk[t], acc[0][t]); acc[1][t] = MFMA16(w1, tk[t], acc[1][t]);
      SCHED();
      if (stg && t == 1) issue1(j + 3, 2);
      if (stg && t == 2) issue1(j + 3, 3);
      if (stg && t == 3) issue1(j + 3, 4);
      SCHED();
    }
  };
  auto compute = [&](int j) __attribute__((always_inline)) { stage(j, false); };

  STAMP(ws, 3, 0);
  issue(0); issue(1); issue(2);
  for (int j = 0; j < 33; ++j) { WAITV(10); BAR(); if (j == 0) STAMP(ws, 3, 1); stage(j, true); }
  WAITV(10); BAR();
  {
    const int o0 = wid * 1024 + lane * 16, o1 = 8192 + wid * 256 + lane * 16;
    auto src = [&](int o) { int T = tt * 5 + (o >> 11); T = T < 128 ? T : 127; return (unsigned)((T * 72 + et * 8) * 256 + (o & 2047)); };
    GLDS(rsH, mA, K4_HOFF, src(o0), 0);
    GLDS_N(0xffff, rsH, lds0 + wid * 256, K4_HOFF + 8192, src(o1), 0);
  }
  compute(33);
  WAITV(7); BAR(); compute(34);
  WAITV(2); BAR(); compute(35);
  WAITV(0); BAR();
  STAMP(ws, 3, 2);
#pragma unroll
  for (int ef = 0; ef < 2; ++ef)
#pragma unroll
    for (int t = 0; t < 5; ++t)
      *(f32x4*)(smem + kq * K4_RKQ + (16 * t + r16) * K4_RSTR + (32 * eh + 16 * ef + 4 * q) * 4) = acc[ef][t];
  WAITL0(); BAR();
  const rsrc_t rsO = MKRS(out, S_ * D_ * 2);
  auto item = [&](int idx) {
    const int tok = idx >> 3, g8 = idx & 7;
    f32x4 lo = {0, 0, 0, 0}, hi = {0, 0, 0, 0};
#pragma unroll
    for (int k = 0; k < 4; ++k) {
      const char* p = smem + k * K4_RKQ + tok * K4_RSTR + g8 * 32;
      lo += *(const f32x4*)p; hi += *(const f32x4*)(p + 16);
    }
    u32x4 hv = *(const u32x4*)(smem + K4_HOFF + (tok >> 4) * 2048 + g8 * 256 + (tok & 15) * 16);
    if (g8 & 1) hv = u32x4{hv[2], hv[3], hv[0], hv[1]};
    const u32x4 ov = {pk_bf16(lo[0] + bf_lo(hv[0]), lo[1] + bf_hi(hv[0])), pk_bf16(lo[2] + bf_lo(hv[1]), lo[3] + bf_hi(hv[1])),
                      pk_bf16(hi[0] + bf_lo(hv[2]), hi[1] + bf_hi(hv[2])), pk_bf16(hi[2] + bf_lo(hv[3]), hi[3] + bf_hi(hv[3]))};
    const int tg = tt * 80 + tok;
    if (tg < S_) __builtin_amdgcn_raw_buffer_store_b128(ov, rsO, (unsigned)(tg * 1152 + et * 128 + g8 * 16), 0, 0);
  };
  item(wid * 64 + lane);
  if (wid < 2) item(512 + wid * 64 + lane);
  STAMP(ws, 3, 3);
}

extern "C" void kernel_launch(void* const* d_in, const int* in_sizes, int n_in, void* d_out, int out_size, void* d_ws, size_t ws_size,
                              hipStream_t stream) {
  const bf16_t* x = (const bf16_t*)d_in[0];
  const bf16_t* cre = (const bf16_t*)d_in[1];
  const bf16_t* cim = (const bf16_t*)d_in[2];
  const bf16_t* wre = (const bf16_t*)d_in[3];
  const bf16_t* wim = (const bf16_t*)d_in[4];
  const bf16_t* wg = (const bf16_t*)d_in[6];
  const bf16_t* wu = (const bf16_t*)d_in[7];
  const bf16_t* wd = (const bf16_t*)d_in[8];
  char* ws = (char*)d_ws;
  k1_hid<<<256, 512, 0, stream>>>(x, wre, wim, cre, cim, ws);
  k2_seq<<<256, 512, 0, stream>>>(x, cre, cim, wg, wu, ws);
  k3_gu<<<256, 512, 0, stream>>>(wg, wu, wd, ws);
  k4_down<<<234, 512, 0, stream>>>(wd, ws, (bf16_t*)d_out);
}
```

```cpp
#include <hip/hip_runtime.h>
#include <stdint.h>
#include <cstdio>
#define DIAG 1

typedef unsigned short bf16_t;
typedef short bf16x8 __attribute__((ext_vector_type(8)));
typedef short s16x4 __attribute__((ext_vector_type(4)));
typedef float f32x4 __attribute__((ext_vector_type(4)));
typedef float f32x2 __attribute__((ext_vector_type(2)));
typedef unsigned u32x4 __attribute__((ext_vector_type(4)));
typedef unsigned u32x2 __attribute__((ext_vector_type(2)));
typedef __bf16 bf2_t __attribute__((ext_vector_type(2)));
typedef __amdgpu_buffer_rsrc_t rsrc_t;

constexpr int S_ = 2048, D_ = 576, F_ = 4608;
constexpr float LN_EPS = 1e-5f;

constexpr unsigned WS_BP = 0;
constexpr unsigned BP_BYTES = 8u * 32u * 18432u;
constexpr unsigned WS_HT = WS_BP + BP_BYTES;
constexpr unsigned HT_BYTES = 128u * 72u * 256u;
constexpr unsigned WS_ST = WS_HT + HT_BYTES;
constexpr unsigned ST_BYTES = 2048u * 8u * 8u;
constexpr unsigned WS_ACT = WS_ST + ST_BYTES;
constexpr unsigned ACT_BYTES = 128u * 144u * 1024u;
constexpr unsigned WS_CS = WS_ACT + ACT_BYTES;
constexpr unsigned CS_BYTES = 32u * 4608u;

#define LDSP(p) ((__attribute__((address_space(3))) void*)(p))
#define MKRS(p, n) __builtin_amdgcn_make_buffer_rsrc((void*)(p), 0, (n), 0x00020000)
#define GLDS(rs, m0base, imm, voff, soff)                                                              \
  asm volatile("s_add_u32 m0, %0, %1\n\ts_nop 0\n\tbuffer_load_dwordx4 %2, %3, %4 offen lds"          \
               ::"s"(m0base), "i"(imm), "v"(voff), "s"(rs), "s"(soff) : "memory", "scc")
#define GLDS_N(mask, rs, m0base, imm, voff, soff)                                                      \
  asm volatile("s_add_u32 m0, %0, %1\n\ts_mov_b64 exec, " #mask "\n\tbuffer_load_dwordx4 %2, %3, %4 offen lds\n\ts_mov_b64 exec, -1" \
               ::"s"(m0base), "i"(imm), "v"(voff), "s"(rs), "s"(soff) : "memory", "scc")
#define GLDS_36(rs, m0base, imm, voff, soff)                                                           \
  asm volatile("s_add_u32 m0, %0, %1\n\ts_mov_b32 exec_hi, 0xf\n\tbuffer_load_dwordx4 %2, %3, %4 offen lds\n\ts_mov_b32 exec_hi, -1" \
               ::"s"(m0base), "i"(imm), "v"(voff), "s"(rs), "s"(soff) : "memory", "scc")
#define TOUCH(rs, mscr, voff)                                                                          \
  asm volatile("s_mov_b32 m0, %0\n\ts_nop 0\n\tbuffer_load_dword %1, %2, 0 offen lds" ::"s"(mscr), "v"(voff), "s"(rs) : "memory")
__device__ __forceinline__ u32x4 mksrd(const void* p, unsigned n) {
  const unsigned long long a = (unsigned long long)p;
  return u32x4{(unsigned)a, (unsigned)(a >> 32) & 0xffffu, n, 0x00020000u};
}
__device__ __forceinline__ unsigned lds_base(char* smem) { return (unsigned)(size_t)LDSP(smem); }
#define WAITV(n) asm volatile("s_waitcnt vmcnt(" #n ")" ::: "memory")
#define WAITL0() asm volatile("s_waitcnt lgkmcnt(0)" ::: "memory")
#define PIN(x) asm volatile("" : "+v"(x))
#define BAR() __builtin_amdgcn_s_barrier()
#define SCHED() __builtin_amdgcn_sched_barrier(0)
#define MFMA16(a, b, c) __builtin_amdgcn_mfma_f32_16x16x32_bf16((a), (b), (c), 0, 0, 0)
constexpr int AUX_SC1 = 16;
template <int V> struct IC { static constexpr int value = V; };
#define K2_VAR 0
#define K4_VAR 0
#define TSX(var, j) do { } while (0)
#define TS_OUT(ws, kid, wid, lane, k, var) do { } while (0)
#define STAMP(ws, kid, slot) do { if (threadIdx.x == 0) { unsigned long long* p_ = (unsigned long long*)((ws) + (64u << 20) + (kid) * 65536 + blockIdx.x * 128 + (slot) * 16); \
  p_[0] = __builtin_amdgcn_s_memtime(); p_[1] = __builtin_amdgcn_s_memrealtime(); } } while (0)

__device__ __forceinline__ unsigned pk_bf16(float lo, float hi) {
  f32x2 v = {lo, hi};
  bf2_t b = __builtin_convertvector(v, bf2_t);
  return __builtin_bit_cast(unsigned, b);
}
__device__ __forceinline__ float bf_lo(unsigned w) { return __uint_as_float(w << 16); }
__device__ __forceinline__ float bf_hi(unsigned w) { return __uint_as_float(w & 0xffff0000u); }
__device__ __forceinline__ float dot_ones(unsigned w, float acc) {
  const bf2_t ones = {(__bf16)1.0f, (__bf16)1.0f};
  return __builtin_amdgcn_fdot2_f32_bf16(__builtin_bit_cast(bf2_t, w), ones, acc, false);
}
__device__ __forceinline__ float dot_frag(bf16x8 f, float acc) {
  u32x4 u = __builtin_bit_cast(u32x4, f);
  acc = dot_ones(u[0], acc); acc = dot_ones(u[1], acc); acc = dot_ones(u[2], acc); acc = dot_ones(u[3], acc);
  return acc;
}

constexpr int K1_SLOT = 8192 + 18432, K1_NS = 5, K1_ST = K1_NS * K1_SLOT, K1_WS = K1_ST + 512, K1_SCR = K1_WS + 1152, K1_LDS = K1_SCR + 256;

template <int NF>
__device__ __forceinline__ void k1_body(char* smem, u32x4 rsX, u32x4 rsW, u32x4 rsC, rsrc_t rsO, int st, int et, int wid, int lane, int nf0,
                                        unsigned vhead, bool headx, char* ws) {
  STAMP(ws, 0, 0);
  const int tid = wid * 64 + lane;
  const int sh = wid >> 2, q = lane >> 4, r16 = lane & 15, r8 = lane >> 3, c8 = lane & 7;
  const unsigned vx = (unsigned)(64 * st + 8 * wid + r8) * 1152u + (unsigned)((c8 ^ ((r8 & 3) | ((wid & 1) << 2))) * 16);
  const unsigned vw = (unsigned)((et & 3) * 144 + 8 * wid + r8) * 1152u + (unsigned)((c8 ^ r8) * 16);
  const int r2 = 128 + 2 * wid + r8;
  const unsigned vw2 = (unsigned)((et & 3) * 144 + r2) * 1152u + (unsigned)((c8 ^ (r2 & 7)) * 16);
  const unsigned lds0 = lds_base(smem);
  const unsigned mA = lds0 + wid * 1024, mP = lds0 + wid * 256;
  auto issue1 = [&](int kb, int k) __attribute__((always_inline)) {
    const int so = (kb % K1_NS) * K1_SLOT;
    if (k == 0) GLDS(rsX, mA, so, vx, kb * 128);
    if (k == 1) GLDS(rsW, mA, so + 8192, vw, kb * 128);
    if (k == 2) GLDS(rsW, mA, so + 16384, vw, kb * 128 + 64 * 1152);
    if (k == 3) GLDS_N(0xffff, rsW, mP, so + 24576, vw2, kb * 128);
  };
  auto issue = [&](int kb) __attribute__((always_inline)) { issue1(kb, 0); issue1(kb, 1); issue1(kb, 2); issue1(kb, 3); };
  unsigned ax[2];
#pragma unroll
  for (int m = 0; m < 2; ++m) {
    const int row = 32 * sh + 8 * (r16 >> 2) + 4 * m + (r16 & 3);
    const int key = (row & 3) | ((row >> 1) & 4);
    ax[m] = (unsigned)(row * 128 + ((q ^ key) * 16)) ^ (unsigned)(sh * 64);
  }
  const unsigned aw = (unsigned)(8192 + (nf0 * 16 + r16) * 128 + ((q ^ (r16 & 7)) * 16)) ^ (unsigned)(sh * 64);

  f32x4 acc[2][NF];
  float wsum[NF];
#pragma unroll
  for (int n = 0; n < NF; ++n) { wsum[n] = 0.f; acc[0][n] = f32x4{0, 0, 0, 0}; acc[1][n] = f32x4{0, 0, 0, 0}; }
  float shift = 0.f;
  f32x2 s1 = {0.f, 0.f}, s2 = {0.f, 0.f};

  struct Fr { u32x4 v; bf16x8 a[2][2], b[2][NF]; };
  Fr fr[2];
  auto rd = [&](Fr& f, int kb, int part) __attribute__((always_inline)) {
    const char* sl = smem + (kb % K1_NS) * K1_SLOT;
    if (part == 0) {
      f.a[0][0] = *(const bf16x8*)(sl + ax[0]); f.a[0][1] = *(const bf16x8*)(sl + ax[1]);
#pragma unroll
      for (int n = 0; n < NF; ++n) f.b[0][n] = *(const bf16x8*)(sl + aw + n * 2048);
    } else {
      f.v = *(const u32x4*)(sl + tid * 16);
      f.a[1][0] = *(const bf16x8*)(sl + (ax[0] ^ 64)); f.a[1][1] = *(const bf16x8*)(sl + (ax[1] ^ 64));
#pragma unroll
      for (int n = 0; n < NF; ++n) f.b[1][n] = *(const bf16x8*)(sl + ((aw ^ 64) + n * 2048));
    }
  };
  issue(0); issue(1);
  TOUCH(headx ? rsX : rsW, lds0 + K1_SCR, vhead);
  issue(2); issue(3); issue(4);
  WAITV(17); BAR();
  STAMP(ws, 0, 1);
  rd(fr[0], 0, 0); rd(fr[0], 0, 1);
  WAITL0();
  WAITV(13); BAR();
#pragma unroll
  for (int kb = 0; kb < 9; ++kb) {
    const Fr& c = fr[kb & 1];
    Fr& nx = fr[(kb & 1) ^ 1];
    const bool stg = kb + 5 < 9;
    if (stg) issue1(kb + 5, 0);
    if (kb == 4)
      TOUCH(rsC, lds0 + K1_SCR, (unsigned)(blockIdx.x * 32768 + (wid & 3) * 8192 + lane * 128));
    SCHED();
#pragma unroll
    for (int n = 0; n < NF; ++n) { acc[0][n] = MFMA16(c.a[0][0], c.b[0][n], acc[0][n]); acc[1][n] = MFMA16(c.a[0][1], c.b[0][n], acc[1][n]); }
    SCHED();
    if (kb < 8) rd(nx, kb + 1, 0);
    SCHED();
    if (stg) issue1(kb + 5, 1);
    SCHED();
    if (kb == 0) shift = bf_lo(c.v[0]);
#pragma unroll
    for (int i = 0; i < 4; ++i) {
      f32x2 d = {bf_lo(c.v[i]) - shift, bf_hi(c.v[i]) - shift};
      s1 += d; s2 += d * d;
    }
    PIN(s1); PIN(s2);
    SCHED();
    if (stg) issue1(kb + 5, 2);
    SCHED();
#pragma unroll
    for (int n = 0; n < NF; ++n) { acc[0][n] = MFMA16(c.a[1][0], c.b[1][n], acc[0][n]); acc[1][n] = MFMA16(c.a[1][1], c.b[1][n], acc[1][n]); }
    SCHED();
    if (kb < 8) rd(nx, kb + 1, 1);
    SCHED();
#pragma unroll
    for (int n = 0; n < NF; ++n) wsum[n] = dot_frag(c.b[0][n], wsum[n]);
#pragma unroll
    for (int n = 0; n < NF; ++n) PIN(wsum[n]);
    SCHED();
    if (stg) issue1(kb + 5, 3);
    WAITL0();
    if (kb <= 3) WAITV(12); else if (kb == 4) WAITV(9); else if (kb == 5) WAITV(5); else if (kb == 6) WAITV(1);
    if (kb < 7) BAR();
    SCHED();
  }
  STAMP(ws, 0, 2);
  {
    const float a1 = s1[0] + s1[1], a2 = s2[0] + s2[1];
    float mean = shift + a1 * (1.f / 72.f);
    float m2 = a2 - a1 * a1 * (1.f / 72.f);
    float n = 72.f;
#pragma unroll
    for (int o = 1; o < 8; o <<= 1) {
      const float mo = __shfl_xor(mean, o), qo = __shfl_xor(m2, o);
      const float dl = mo - mean;
      m2 = m2 + qo + dl * dl * (n * 0.5f);
      mean = 0.5f * (mean + mo);
      n *= 2.f;
    }
    const float r = __builtin_amdgcn_rsqf(m2 * (1.f / 576.f) + LN_EPS);
    if (c8 == 0) *(f32x2*)(smem + K1_ST + (tid >> 3) * 8) = f32x2{r, mean * r};
#pragma unroll
    for (int n = 0; n < NF; ++n) {
      float w = wsum[n];
      w += __shfl_xor(w, 16); w += __shfl_xor(w, 32);
      if (q == 0) *(float*)(smem + K1_WS + sh * 576 + ((nf0 + n) * 16 + r16) * 4) = w;
    }
  }
  WAITL0();
  BAR();
  f32x2 rs[8];
  {
    const char* p = smem + K1_ST + (32 * sh + 8 * q) * 8;
#pragma unroll
    for (int i = 0; i < 4; ++i) {
      f32x4 v = *(const f32x4*)(p + i * 16);
      rs[2 * i] = f32x2{v[0], v[1]}; rs[2 * i + 1] = f32x2{v[2], v[3]};
    }
  }
  const float sgn = et < 4 ? 1.f : -1.f;
  const int kt = (et < 4 ? 0 : 16) + (st >> 1), ksg = (st & 1) * 2 + sh;
#pragma unroll
  for (int n = 0; n < NF; ++n) {
    const int el = (nf0 + n) * 16 + r16;
    const float ws = *(const float*)(smem + K1_WS + el * 4) + *(const float*)(smem + K1_WS + 576 + el * 4);
    float o[8];
#pragma unroll
    for (int m = 0; m < 2; ++m)
#pragma unroll
      for (int j = 0; j < 4; ++j) o[4 * m + j] = sgn * (acc[m][n][j] * rs[4 * m + j][0] - rs[4 * m + j][1] * ws);
    u32x4 pk = {pk_bf16(o[0], o[1]), pk_bf16(o[2], o[3]), pk_bf16(o[4], o[5]), pk_bf16(o[6], o[7])};
    const int hi = el >= 72 ? 1 : 0, e72 = el - 72 * hi;
    const int etg = (et & 3) * 2 + hi;
    const unsigned inner = e72 < 64 ? (unsigned)((e72 >> 4) * 1024 + q * 256 + (e72 & 15) * 16) : (unsigned)(4096 + q * 128 + (e72 & 7) * 16);
    const unsigned off = (unsigned)((etg * 32 + kt) * 4 + ksg) * 4608u + inner;
    __builtin_amdgcn_raw_buffer_store_b128(pk, rsO, off, 0, AUX_SC1);
  }
  WAITV(0);
  STAMP(ws, 0, 3);
}

__global__ __launch_bounds__(512, 2) void k1_hid(const bf16_t* x, const bf16_t* wre, const bf16_t* wim, const bf16_t* cre, const bf16_t* cim, char* ws) {
  __shared__ __attribute__((aligned(16))) char smem[K1_LDS];
  const int wid = __builtin_amdgcn_readfirstlane(threadIdx.x >> 6), lane = threadIdx.x & 63;
  const int b = blockIdx.x, xcd = b & 7, i = b >> 3;
  const int st = xcd * 4 + (i & 3), et = i >> 2;
  const u32x4 rsX = mksrd(x, S_ * D_ * 2);
  const u32x4 rsW = mksrd(et < 4 ? wre : wim, D_ * D_ * 2);
  const u32x4 rsC = mksrd(wid < 4 ? cre : cim, S_ * S_ * 2);
  rsrc_t rsO = MKRS(ws + WS_BP, BP_BYTES);
  unsigned vhead;
  if (wid == 7) {
    const int l = lane < 32 ? lane : 31;
    vhead = (unsigned)((64 * st + 8 * et + (l >> 2)) * 1152 + (5 + (l & 3)) * 128);
  } else {
    int l = wid * 21 + (lane < 21 ? lane : 20);
    l = l < 144 ? l : 143;
    vhead = (unsigned)(((et & 3) * 144 + 36 * (i & 3) + (l >> 2)) * 1152 + (5 + (l & 3)) * 128);
  }
  const int w4 = wid & 3;
  if (wid == 0 || wid == 7) k1_body<3>(smem, rsX, rsW, rsC, rsO, st, et, wid, lane, wid == 0 ? 0 : 6, vhead, wid == 7, ws);
  else k1_body<2>(smem, rsX, rsW, rsC, rsO, st, et, wid, lane, wid < 4 ? 1 + 2 * w4 : 2 * w4, vhead, false, ws);
}

constexpr int K2_SLOT = 16384 + 18432, K2_XOFF = 4 * K2_SLOT, K2_LDS = K2_XOFF + 9216;
constexpr int K2_RSTR = 336, K2_RKQ = 64 * K2_RSTR, K2_SOFF = 4 * K2_RKQ;

__device__ __forceinline__ int tid_cs(int wid, int lane) { return (wid - 1) * 64 + lane; }
constexpr int K2_CSO = 98304;
__global__ __launch_bounds__(512, 2) void k2_seq(const bf16_t* x, const bf16_t* cre, const bf16_t* cim, const bf16_t* wg, const bf16_t* wu, char* ws) {
  __shared__ __attribute__((aligned(16))) char smem[K2_LDS];
  const int wid = __builtin_amdgcn_readfirstlane(threadIdx.x >> 6), lane = threadIdx.x & 63;
  const int b = blockIdx.x, xcd = b & 7, i = b >> 3;
  const int mt = (xcd >> 1) * 8 + (i & 7), nt = (xcd & 1) * 4 + (i >> 3);
  const u32x4 rsRe = mksrd(cre, S_ * S_ * 2), rsIm = mksrd(cim, S_ * S_ * 2);
  const u32x4 rsB = mksrd(ws + WS_BP, BP_BYTES);
  const u32x4 rsX = mksrd(x, S_ * D_ * 2);
  const int q = lane >> 4, r16 = lane & 15, r8 = lane >> 3, c8 = lane & 7;
  const int mh = wid & 1, kq = wid >> 1;

  const unsigned va = (unsigned)(64 * mt + 8 * wid + r8) * 4096u + (unsigned)((c8 ^ r8) * 16);
  const unsigned vb = (unsigned)(wid * 1024 + lane * 16);
  const unsigned vb2 = (unsigned)(16384 + wid * 256 + lane * 16);
  const unsigned mA = lds_base(smem) + wid * 1024, mP = lds_base(smem) + wid * 256;
  auto issue1 = [&](int j, u32x4 rsA, int k) __attribute__((always_inline)) {
    const unsigned so = (unsigned)((j & 3) * K2_SLOT), m0 = mA + so, m1 = mP + so;
    const int sa = (j & 15) * 256, sb = (nt * 32 + j) * 18432;
    if (k == 0) GLDS(rsA, m0, 0, va, sa);
    if (k == 1) GLDS(rsA, m0, 8192, va, sa + 128);
    if (k == 2) GLDS(rsB, m0, 16384, vb, sb);
    if (k == 3) GLDS(rsB, m0, 16384 + 8192, vb, sb + 8192);
    if (k == 4) GLDS_N(0xffff, rsB, m1, 16384 + 16384, vb2, sb);
  };
  auto issue = [&](int j, u32x4 rsA) __attribute__((always_inline)) {
    issue1(j, rsA, 0); issue1(j, rsA, 1); issue1(j, rsA, 2); issue1(j, rsA, 3); issue1(j, rsA, 4);
  };
  const unsigned at0 = (unsigned)((kq >> 1) * 8192 + (32 * mh + r16) * 128 + ((((kq & 1) * 4 + q) ^ (r16 & 7)) * 16));
  const unsigned ae0 = (unsigned)(16384 + kq * 4608 + lane * 16);
  const unsigned aeh = (unsigned)(16384 + kq * 4608 + 4096 + q * 128 + (lane & 7) * 16);

  f32x4 acc[5][2];
#pragma unroll
  for (int f = 0; f < 5; ++f) { acc[f][0] = f32x4{0, 0, 0, 0}; acc[f][1] = f32x4{0, 0, 0, 0}; }
  struct Fr { bf16x8 t0, t1, e[5]; };
  Fr fr[2];
  auto rd = [&](Fr& f, int j, int part) __attribute__((always_inline)) {
    const char* sl = smem + (j & 3) * K2_SLOT;
    if (part == 0) { f.t0 = *(const bf16x8*)(sl + at0); f.t1 = *(const bf16x8*)(sl + at0 + 2048); }
    if (part == 1) { f.e[0] = *(const bf16x8*)(sl + ae0); f.e[1] = *(const bf16x8*)(sl + ae0 + 1024); }
    if (part == 2) { f.e[2] = *(const bf16x8*)(sl + ae0 + 2048); f.e[3] = *(const bf16x8*)(sl + ae0 + 3072); }
    if (part == 3) f.e[4] = *(const bf16x8*)(sl + aeh);
  };
  const int cft = xcd * 4 + (i & 3), cm = (i >> 2) & 1, crg = i >> 3;
  u32x4 wv[6];
  auto stage = [&](int j, auto P_, u32x4 rsA, auto MODE_, auto RD_) __attribute__((always_inline)) {
    constexpr int P = decltype(P_)::value, MODE = decltype(MODE_)::value, RD = decltype(RD_)::value;
    const Fr& c = fr[P];
    Fr& nx = fr[P ^ 1];
#pragma unroll
    for (int f = 0; f < 5; ++f) {
      if (MODE == 1) issue1(j + 4, rsA, f);
      if (MODE == 2 && f == 0) {
        const int o0 = wid * 1024 + lane * 16, o1 = 8192 + wid * 128 + lane * 16;
        const int row0 = o0 / 144, row1 = o1 / 144;
        GLDS(rsX, mA, K2_XOFF, (unsigned)((64 * mt + row0) * 1152 + nt * 144 + (o0 - row0 * 144)), 0);
        GLDS_N(0xff, rsX, lds_base(smem) + wid * 128, K2_XOFF + 8192, (unsigned)((64 * mt + row1) * 1152 + nt * 144 + (o1 - row1 * 144)), 0);
      }
      if (MODE == 2 && f >= 2) {
        const u32x4 rsW = mksrd(cm ? wu : wg, D_ * F_ * 2);
        const int l54 = lane < 54 ? lane : 53, r3 = l54 / 18;
        const unsigned vo = (unsigned)((crg * 144 + wid * 18 + r3) * 9216 + cft * 288 + (l54 - r3 * 18) * 16);
#pragma unroll
        for (int k = 2 * (f - 2); k < 2 * (f - 2) + 2; ++k)
          asm volatile("buffer_load_dwordx4 %0, %1, %2, %3 offen" : "=v"(wv[k]) : "v"(vo), "s"(rsW), "s"(k * 3 * 9216) : "memory");
      }
      SCHED();
      acc[f][0] = MFMA16(c.e[f], c.t0, acc[f][0]); acc[f][1] = MFMA16(c.e[f], c.t1, acc[f][1]);
      SCHED();
      if (RD && f < 4) rd(nx, j + 1, f);
      SCHED();
    }
    WAITL0();
  };
  using I0 = IC<0>; using I1 = IC<1>; using I2 = IC<2>;

  STAMP(ws, 1, 0);
  issue(0, rsRe); issue(1, rsRe); issue(2, rsRe); issue(3, rsRe);
  WAITV(15); BAR();
  STAMP(ws, 1, 1);
  rd(fr[0], 0, 0); rd(fr[0], 0, 1); rd(fr[0], 0, 2); rd(fr[0], 0, 3);
  WAITL0();
  WAITV(10); BAR();
  for (int j = 0; j < 12; j += 2) {
    stage(j, I0{}, rsRe, I1{}, I1{}); WAITV(10); BAR();
    stage(j + 1, I1{}, rsRe, I1{}, I1{}); WAITV(10); BAR();
  }
  for (int j = 12; j < 28; j += 2) {
    stage(j, I0{}, rsIm, I1{}, I1{}); WAITV(10); BAR();
    stage(j + 1, I1{}, rsIm, I1{}, I1{}); WAITV(10); BAR();
  }
  stage(28, I0{}, rsIm, I2{}, I1{}); WAITV(13); BAR();
  stage(29, I1{}, rsIm, I0{}, I1{}); WAITV(8); BAR();
  stage(30, I0{}, rsIm, I0{}, I1{});
  stage(31, I1{}, rsIm, I0{}, I0{});
  WAITV(6); BAR();
  STAMP(ws, 1, 2);
#pragma unroll
  for (int f = 0; f < 5; ++f)
#pragma unroll
    for (int tf = 0; tf < 2; ++tf)
      *(f32x4*)(smem + kq * K2_RKQ + (32 * mh + 16 * tf + r16) * K2_RSTR + (16 * f + 4 * q) * 4) = acc[f][tf];
  asm volatile("s_waitcnt vmcnt(0)" : "+v"(wv[0]), "+v"(wv[1]), "+v"(wv[2]), "+v"(wv[3]), "+v"(wv[4]), "+v"(wv[5]) :: "memory");
  {
    f32x4 lo = {0, 0, 0, 0}, hi = {0, 0, 0, 0};
#pragma unroll
    for (int k = 0; k < 6; ++k) {
      lo += f32x4{bf_lo(wv[k][0]), bf_hi(wv[k][0]), bf_lo(wv[k][1]), bf_hi(wv[k][1])};
      hi += f32x4{bf_lo(wv[k][2]), bf_hi(wv[k][2]), bf_lo(wv[k][3]), bf_hi(wv[k][3])};
    }
    if (lane < 54) {
      char* p = smem + K2_CSO + (wid * 3 * 144) * 4 + lane * 32;
      *(f32x4*)p = lo; *(f32x4*)(p + 16) = hi;
    }
  }
  WAITL0(); BAR();
  const rsrc_t rsH = MKRS(ws + WS_HT, HT_BYTES);
  auto item = [&](int U) {
    const int t = lane;
    f32x4 lo = {0, 0, 0, 0}, hi = {0, 0, 0, 0};
#pragma unroll
    for (int k = 0; k < 4; ++k) {
      const char* p = smem + k * K2_RKQ + t * K2_RSTR + U * 32;
      lo += *(const f32x4*)p; hi += *(const f32x4*)(p + 16);
    }
    const u32x4 xv = *(const u32x4*)(smem + K2_XOFF + t * 144 + U * 16);
    u32x4 hv = {pk_bf16(lo[0] + bf_lo(xv[0]), lo[1] + bf_hi(xv[0])), pk_bf16(lo[2] + bf_lo(xv[1]), lo[3] + bf_hi(xv[1])),
                pk_bf16(hi[0] + bf_lo(xv[2]), hi[1] + bf_hi(xv[2])), pk_bf16(hi[2] + bf_lo(xv[3]), hi[3] + bf_hi(xv[3]))};
    float v[8] = {bf_lo(hv[0]), bf_hi(hv[0]), bf_lo(hv[1]), bf_hi(hv[1]), bf_lo(hv[2]), bf_hi(hv[2]), bf_lo(hv[3]), bf_hi(hv[3])};
    float m = 0.f;
#pragma unroll
    for (int k = 0; k < 8; ++k) m += v[k];
    m *= 0.125f;
    float m2 = 0.f;
#pragma unroll
    for (int k = 0; k < 8; ++k) { const float d = v[k] - m; m2 += d * d; }
    *(f32x2*)(smem + K2_SOFF + (t * 9 + U) * 8) = f32x2{m, m2};
    const int Ug = nt * 9 + U, tg = 64 * mt + t;
    if (Ug & 1) hv = u32x4{hv[2], hv[3], hv[0], hv[1]};
    __builtin_amdgcn_raw_buffer_store_b128(hv, rsH, (unsigned)(((tg >> 4) * 72 + Ug) * 256 + (tg & 15) * 16), 0, AUX_SC1);
  };
  item(wid);
  if (wid == 0) item(8);
  WAITL0(); BAR();
  if (wid == 0) {
    const char* p = smem + K2_SOFF + lane * 72;
    f32x2 s[9];
    float m = 0.f;
#pragma unroll
    for (int k = 0; k < 9; ++k) { s[k] = *(const f32x2*)(p + k * 8); m += s[k][0]; }
    m *= (1.f / 9.f);
    float m2 = 0.f;
#pragma unroll
    for (int k = 0; k < 9; ++k) { const float d = s[k][0] - m; m2 += s[k][1] + 8.f * d * d; }
    const rsrc_t rsS = MKRS(ws + WS_ST, ST_BYTES);
    __builtin_amdgcn_raw_buffer_store_b64(__builtin_bit_cast(u32x2, f32x2{m, m2}), rsS, (unsigned)(((64 * mt + lane) * 8 + nt) * 8), 0, AUX_SC1);
  }
  if (wid >= 1 && wid <= 3) {
    const int c = tid_cs(wid, lane);
    if (c < 144) {
      float a = 0.f;
#pragma unroll
      for (int k = 0; k < 24; ++k) a += *(const float*)(smem + K2_CSO + (k * 144 + c) * 4);
      const rsrc_t rsC = MKRS(ws + WS_CS, CS_BYTES);
      __builtin_amdgcn_raw_buffer_store_b32(__float_as_uint(a), rsC, (unsigned)(cft * 4608 + crg * 1152 + cm * 576 + c * 4), 0, AUX_SC1);
    }
  }
  STAMP(ws, 1, 3);
}

constexpr int K3_SLOT = 16384 + 18432, K3_AUX = 4 * K3_SLOT, K3_TOK = K3_AUX + 16384, K3_CS = K3_TOK + 2048, K3_LDS = K3_CS + 4608;

#define DSR128(dst, addr, off) asm volatile("ds_read_b128 %0, %1 offset:%2" : "=v"(dst) : "v"(addr), "i"(off))
#define DSRTR(dst, addr, off) asm volatile("ds_read_b64_tr_b16 %0, %1 offset:%2" : "=v"(dst) : "v"(addr), "i"(off))

template <int NCH>
__device__ __forceinline__ void k3_body(char* smem, u32x4 rsH, u32x4 rsG, u32x4 rsU, u32x4 rsS, u32x4 rsC, u32x4 rsD, rsrc_t rsO, int tt, int ft,
                                        int wid, int lane, char* ws) {
  STAMP(ws, 2, 0);
  constexpr bool LEAD = NCH == 5;
  const int tg = wid & 3, q = lane >> 4, r16 = lane & 15;
  const int odd = ft & 1;
  const int p0 = odd;
  const int cL = odd ? 0 : 8;
  const unsigned vt = (unsigned)((tt * 16 + wid) * 72 * 256 + lane * 16);
  unsigned vw0, vw1, vw2;
  {
    const int o0 = wid * 1024 + lane * 16, r0 = o0 / 288;
    vw0 = (unsigned)(r0 * 9216 + ft * 288 + (o0 - r0 * 288));
    const int o1 = (wid == 0 ? 8192 : (wid - 1) * 1024) + lane * 16, r1 = o1 / 288;
    vw1 = (unsigned)(r1 * 9216 + ft * 288 + (o1 - r1 * 288));
    const int o2 = 7168 + wid * 256 + lane * 16, r2 = o2 / 288;
    vw2 = (unsigned)(r2 * 9216 + ft * 288 + (o2 - r2 * 288));
  }
  const u32x4 rsW1 = wid == 0 ? rsG : rsU;
  const unsigned lds0 = lds_base(smem);
  const unsigned mA = lds0 + wid * 1024, mP = lds0 + wid * 256;
  auto issue1 = [&](int j, int k) __attribute__((always_inline)) {
    const unsigned so = (unsigned)((j & 3) * K3_SLOT), m0 = mA + so, m1 = mP + so;
    if (k == 0) GLDS(rsH, m0, 0, vt, j * 1024);
    if (k == 1) GLDS(rsH, m0, 8192, vt, j * 1024 + 8 * 72 * 256);
    if (k == 2) GLDS(rsG, m0, 16384, vw0, j * 294912);
    if (k == 3) GLDS(rsW1, m0, 16384 + 8192, vw1, j * 294912);
    if (k == 4) GLDS_N(0xffff, rsU, m1, 16384 + 16384, vw2, j * 294912);
  };
  auto issue = [&](int j) __attribute__((always_inline)) { issue1(j, 0); issue1(j, 1); issue1(j, 2); issue1(j, 3); issue1(j, 4); };
  const unsigned atok = lds0 + (unsigned)(tg * 4096 + lane * 16);
  const int a4 = r16 >> 2, p4 = r16 & 3;
  const int cfirst = LEAD ? p0 : p0 + 4;
  const unsigned aw1 = lds0 + (unsigned)(16384 + (8 * q + 4 * (q & 1) + a4) * 288 + cfirst * 32 + p4 * 8);
  const unsigned aw2 = lds0 + (unsigned)(16384 + (8 * q + 4 * (1 - (q & 1)) + a4) * 288 + cfirst * 32 + p4 * 8);
  const int dL = (cL - cfirst) * 32;

  f32x4 ag[NCH][4], au[NCH][4];
#pragma unroll
  for (int c = 0; c < NCH; ++c)
#pragma unroll
    for (int t = 0; t < 4; ++t) { ag[c][t] = f32x4{0, 0, 0, 0}; au[c][t] = f32x4{0, 0, 0, 0}; }

  bf16x8 tk[2][4];
  s16x4 w[2][4];
  auto rdw = [&](int buf, unsigned b1, unsigned b2, int off, int i) __attribute__((always_inline)) {
    if (i == 0) DSRTR(w[buf][0], b1, off);
    if (i == 1) DSRTR(w[buf][1], b2, off);
    if (i == 2) DSRTR(w[buf][2], b1, off + 9216);
    if (i == 3) DSRTR(w[buf][3], b2, off + 9216);
  };
  auto stage = [&](int j, auto P_) __attribute__((always_inline)) {
    constexpr int P = decltype(P_)::value;
    constexpr bool NXT = true;
    const bool STG = j < 15;
    const unsigned so = (unsigned)((j & 3) * K3_SLOT), sn = (unsigned)(((j + 1) & 3) * K3_SLOT);
    const unsigned b1 = aw1 + so, b2 = aw2 + so, n1 = aw1 + sn, n2 = aw2 + sn, nt = atok + sn;
    const unsigned l1 = b1 + (unsigned)dL, l2 = b2 + (unsigned)dL;
#pragma unroll
    for (int c = 0; c < NCH; ++c) {
      const int cb = LEAD ? ((P + c) & 1) : (c & 1), nb = cb ^ 1;
      const bool last = c == NCH - 1;
      if (c > 0) asm volatile("s_waitcnt lgkmcnt(0)" : "+v"(w[cb][0]), "+v"(w[cb][1]), "+v"(w[cb][2]), "+v"(w[cb][3]));
      if (c == 0) TSX(ts[7], j); else TSX(ts[c - 1], j);
      if (P == 0 && last && j == 0) { WAITV(9); BAR(); }
      const bf16x8 g = __builtin_shufflevector(w[cb][0], w[cb][1], 0, 1, 2, 3, 4, 5, 6, 7);
      const bf16x8 u = __builtin_shufflevector(w[cb][2], w[cb][3], 0, 1, 2, 3, 4, 5, 6, 7);
      SCHED();
#pragma unroll
      for (int t = 0; t < 4; ++t) {
        ag[c][t] = MFMA16(g, tk[P][t], ag[c][t]); au[c][t] = MFMA16(u, tk[P][t], au[c][t]);
        SCHED();
        if (!last) {
          if (t < 2) {
            const bool lc = LEAD && c == 3;
            rdw(nb, lc ? l1 : b1, lc ? l2 : b2, lc ? 0 : (c + 1) * 32, 2 * t);
            rdw(nb, lc ? l1 : b1, lc ? l2 : b2, lc ? 0 : (c + 1) * 32, 2 * t + 1);
          }
        } else if (NXT) {
          if (t == 0) { DSR128(tk[P ^ 1][0], nt, 0); rdw(nb, n1, n2, 0, 0); rdw(nb, n1, n2, 0, 1); }
          if (t == 1) { rdw(nb, n1, n2, 0, 2); rdw(nb, n1, n2, 0, 3); DSR128(tk[P ^ 1][1], nt, 1024); }
          if (t == 2) { DSR128(tk[P ^ 1][2], nt, 2048); DSR128(tk[P ^ 1][3], nt, 3072); }
        }
        if (STG) {
          if (t == 2) issue1(j + 3, LEAD ? c : (c == 0 ? 0 : c + 1));
          if (!LEAD && c == 0 && t == 3) issue1(j + 3, 1);
        }
        SCHED();
      }
    }
    if (NXT)
      asm volatile("s_waitcnt lgkmcnt(0)" : "+v"(tk[P ^ 1][0]), "+v"(tk[P ^ 1][1]), "+v"(tk[P ^ 1][2]), "+v"(tk[P ^ 1][3]),
                   "+v"(w[LEAD ? (P ^ 1) : 0][0]), "+v"(w[LEAD ? (P ^ 1) : 0][1]), "+v"(w[LEAD ? (P ^ 1) : 0][2]), "+v"(w[LEAD ? (P ^ 1) : 0][3]));
    TSX(ts[5], j);
  };

  issue(0);
  GLDS(rsS, mA, K3_AUX, (unsigned)(tt * 16384 + wid * 1024 + lane * 16), 0);
  GLDS(rsS, mA, K3_AUX + 8192, (unsigned)(tt * 16384 + wid * 1024 + lane * 16), 8192);
  GLDS_36(rsC, lds0 + wid * 576, K3_CS, (unsigned)(ft * 4608 + wid * 576 + lane * 16), 0);
  issue(1); issue(2);
  WAITV(10); BAR();
  STAMP(ws, 2, 1);
  DSR128(tk[0][0], atok, 0); DSR128(tk[0][1], atok, 1024); DSR128(tk[0][2], atok, 2048); DSR128(tk[0][3], atok, 3072);
  rdw(0, aw1, aw2, 0, 0); rdw(0, aw1, aw2, 0, 1); rdw(0, aw1, aw2, 0, 2); rdw(0, aw1, aw2, 0, 3);
  if constexpr (!LEAD) {
    const int t = tg * 64 + lane;
    const char* p = smem + K3_AUX + t * 64;
    f32x4 s[4];
    float m = 0.f;
#pragma unroll
    for (int k = 0; k < 4; ++k) { s[k] = *(const f32x4*)(p + k * 16); m += s[k][0] + s[k][2]; }
    m *= 0.125f;
    float m2 = 0.f;
#pragma unroll
    for (int k = 0; k < 4; ++k) { const float d0 = s[k][0] - m, d1 = s[k][2] - m; m2 += s[k][1] + s[k][3] + 72.f * (d0 * d0 + d1 * d1); }
    const float r = __builtin_amdgcn_rsqf(m2 * (1.f / 576.f) + LN_EPS);
    *(f32x2*)(smem + K3_TOK + t * 8) = f32x2{r, m};
  }
  asm volatile("s_waitcnt lgkmcnt(0)" : "+v"(tk[0][0]), "+v"(tk[0][1]), "+v"(tk[0][2]), "+v"(tk[0][3]), "+v"(w[0][0]), "+v"(w[0][1]), "+v"(w[0][2]), "+v"(w[0][3]) :: "memory");
  SCHED();

#pragma clang loop unroll(disable)
  for (int j = 0; j < 18; j += 2) {
    stage(j, IC<0>{});
    if (j < 14) WAITV(5);
    else if (j == 14) {
      int tz = threadIdx.x;
      asm volatile("" : "+v"(tz));
      int l = tz & 63;
      l = wid * 21 + (l < 20 ? l : 20);
      l = l < 161 ? l : 161;
      TOUCH(rsD, lds0 + K3_AUX, (unsigned)(blockIdx.x * 20736 + l * 128));
      WAITV(6);
    }
    if (j < 16) BAR();
    stage(j + 1, IC<1>{});
    if (j < 14) WAITV(5); else WAITV(1);
    if (j < 16) BAR();
  }

  STAMP(ws, 2, 2);
  int tz = threadIdx.x;
  asm volatile("" : "+v"(tz));
  const int ln = tz & 63, eq = ln >> 4, e16 = ln & 15;
  f32x2 st[4];
#pragma unroll
  for (int t = 0; t < 4; ++t) st[t] = *(const f32x2*)(smem + K3_TOK + (tg * 64 + t * 16 + e16) * 8);
  constexpr float NLOG2E = -1.4426950408889634f;
  auto csum = [&](int ch, int m) __attribute__((always_inline)) -> f32x4 {
    const char* p = smem + K3_CS + m * 576 + ch * 64 + eq * 16;
    return (*(const f32x4*)p + *(const f32x4*)(p + 1152)) + (*(const f32x4*)(p + 2 * 1152) + *(const f32x4*)(p + 3 * 1152));
  };
  auto actv = [&](int c, int t, f32x4 cg, f32x4 cu) __attribute__((always_inline)) -> u32x2 {
    const float r = st[t][0], mu = st[t][1];
    float o[4];
#pragma unroll
    for (int k = 0; k < 4; ++k) {
      const float g = (ag[c][t][k] - mu * cg[k]) * r;
      const float u = (au[c][t][k] - mu * cu[k]) * r;
      const float e = __builtin_amdgcn_exp2f(g * NLOG2E);
      o[k] = g * __builtin_amdgcn_rcpf(1.f + e) * u;
    }
    return u32x2{pk_bf16(o[0], o[1]), pk_bf16(o[2], o[3])};
  };
  const int fbase = ft * 144;
#pragma unroll
  for (int pr = 0; pr < 2; ++pr) {
    const int c0 = cfirst + 2 * pr;
    const int G = (fbase + c0 * 16) >> 5;
    const f32x4 cg0 = csum(c0, 0), cu0 = csum(c0, 1), cg1 = csum(c0 + 1, 0), cu1 = csum(c0 + 1, 1);
#pragma unroll
    for (int t = 0; t < 4; ++t) {
      const u32x2 lo = actv(2 * pr, t, cg0, cu0), hi = actv(2 * pr + 1, t, cg1, cu1);
      const int T16 = tt * 16 + tg * 4 + t;
      __builtin_amdgcn_raw_buffer_store_b128(u32x4{lo[0], lo[1], hi[0], hi[1]}, rsO, (unsigned)((T16 * 144 + G) * 1024 + ln * 16), 0, AUX_SC1);
    }
    SCHED();
  }
  if constexpr (NCH == 5) {
    const int G = (fbase + cL * 16) >> 5, half = odd;
    const f32x4 cg0 = csum(cL, 0), cu0 = csum(cL, 1);
#pragma unroll
    for (int t = 0; t < 4; ++t) {
      const u32x2 v = actv(4, t, cg0, cu0);
      const int T16 = tt * 16 + tg * 4 + t;
      __builtin_amdgcn_raw_buffer_store_b64(v, rsO, (unsigned)((T16 * 144 + G) * 1024 + ln * 16 + half * 8), 0, AUX_SC1);
    }
  }
  WAITV(0);
  STAMP(ws, 2, 3);
}

__global__ __launch_bounds__(512, 2) void k3_gu(const bf16_t* wg, const bf16_t* wu, const bf16_t* wd, char* ws) {
  __shared__ __attribute__((aligned(16))) char smem[K3_LDS];
  const int wid = __builtin_amdgcn_readfirstlane(threadIdx.x >> 6), lane = threadIdx.x & 63;
  const int b = blockIdx.x, xcd = b & 7, i = b >> 3;
  const int ft = xcd * 4 + (i & 3), tt = i >> 2;
  const u32x4 rsH = mksrd(ws + WS_HT, HT_BYTES), rsG = mksrd(wg, D_ * F_ * 2), rsU = mksrd(wu, D_ * F_ * 2);
  const u32x4 rsS = mksrd(ws + WS_ST, ST_BYTES), rsC = mksrd(ws + WS_CS, CS_BYTES), rsD = mksrd(wd, F_ * D_ * 2);
  const rsrc_t rsO = MKRS(ws + WS_ACT, ACT_BYTES);
  if (wid < 4) k3_body<5>(smem, rsH, rsG, rsU, rsS, rsC, rsD, rsO, tt, ft, wid, lane, ws);
  else k3_body<4>(smem, rsH, rsG, rsU, rsS, rsC, rsD, rsO, tt, ft, wid, lane, ws);
}

constexpr int K4_SLOT = 16384 + 20480, K4_HOFF = 4 * K4_SLOT, K4_LDS = K4_HOFF + 10240;
constexpr int K4_RSTR = 272, K4_RKQ = 80 * K4_RSTR;

__global__ __launch_bounds__(512, 2) void k4_down(const bf16_t* wd, char* ws, bf16_t* out) {
  __shared__ __attribute__((aligned(16))) char smem[K4_LDS];
  const int wid = __builtin_amdgcn_readfirstlane(threadIdx.x >> 6), lane = threadIdx.x & 63;
  const int b = blockIdx.x, xcd = b & 7, i = b >> 3;
  const int id = xcd * 29 + (xcd < 2 ? xcd : 2) + i;
  const int tt = id / 9, et = id - tt * 9;
  const u32x4 rsW = mksrd(wd, F_ * D_ * 2), rsA = mksrd(ws + WS_ACT, ACT_BYTES), rsH = mksrd(ws + WS_HT, HT_BYTES);
  const int q = lane >> 4, r16 = lane & 15, r8 = lane >> 3, c8 = lane & 7;
  const int eh = wid & 1, kq = wid >> 1;

  const unsigned vw = (unsigned)((8 * wid + r8) * 1152 + et * 128 + ((c8 ^ (((r8 >> 1) & 3) << 1)) * 16));
  unsigned va0, va1, va2;
  {
    const int o0 = wid * 1024 + lane * 16, o1 = o0 + 8192, o2 = 16384 + wid * 512 + lane * 16;
    auto src = [&](int o) { int T = tt * 5 + (o >> 12); T = T < 128 ? T : 127; return (unsigned)(T * 144 * 1024 + (o & 4095)); };
    va0 = src(o0); va1 = src(o1); va2 = src(o2);
  }
  const unsigned lds0 = lds_base(smem);
  const unsigned mA = lds0 + wid * 1024, mP = lds0 + wid * 512;
  auto issue1 = [&](int j, int k) __attribute__((always_inline)) {
    const unsigned so = (unsigned)((j & 3) * K4_SLOT), m0 = mA + so, m1 = mP + so;
    const int jj = j;
    if (k == 0) GLDS(rsW, m0, 0, vw, jj * 147456);
    if (k == 1) GLDS(rsW, m0, 8192, vw, jj * 147456 + 73728);
    if (k == 2) GLDS(rsA, m0, 16384, va0, jj * 4096);
    if (k == 3) GLDS(rsA, m0, 16384 + 8192, va1, jj * 4096);
    if (k == 4) GLDS_N(0xffffffff, rsA, m1, 16384 + 16384, va2, jj * 4096);
  };
  auto issue = [&](int j) __attribute__((always_inline)) { issue1(j, 0); issue1(j, 1); issue1(j, 2); issue1(j, 3); issue1(j, 4); };
  const int a4 = r16 >> 2, p4 = r16 & 3;
  const int rowA = 32 * kq + 4 * q + a4, key = (rowA >> 1) & 3;
  const unsigned aw0 = lds0 + (unsigned)(rowA * 128 + (((2 * eh) ^ key) * 32) + p4 * 8);
  const unsigned aw1 = lds0 + (unsigned)(rowA * 128 + (((2 * eh + 1) ^ key) * 32) + p4 * 8);
  const unsigned aa = lds0 + (unsigned)(16384 + kq * 1024 + lane * 16);

  f32x4 acc[2][5];
#pragma unroll
  for (int t = 0; t < 5; ++t) { acc[0][t] = f32x4{0, 0, 0, 0}; acc[1][t] = f32x4{0, 0, 0, 0}; }
  struct Fr { s16x4 w0l, w0h, w1l, w1h; bf16x8 tk[5]; };
  Fr fr[2];
  auto rd = [&](Fr& f, int j, int part) __attribute__((always_inline)) {
    const unsigned so = (unsigned)((j & 3) * K4_SLOT);
    if (part == 0) { const unsigned b0 = aw0 + so; DSRTR(f.w0l, b0, 0); DSRTR(f.w0h, b0, 2048); }
    if (part == 1) { const unsigned b1 = aw1 + so; DSRTR(f.w1l, b1, 0); DSRTR(f.w1h, b1, 2048); }
    if (part == 2) { const unsigned ba = aa + so; DSR128(f.tk[0], ba, 0); DSR128(f.tk[1], ba, 4096); }
    if (part == 3) { const unsigned ba = aa + so; DSR128(f.tk[2], ba, 8192); DSR128(f.tk[3], ba, 12288); DSR128(f.tk[4], ba, 16384); }
  };
  auto landed = [&](Fr& f) __attribute__((always_inline)) {
    asm volatile("s_waitcnt lgkmcnt(0)" : "+v"(f.w0l), "+v"(f.w0h), "+v"(f.w1l), "+v"(f.w1h), "+v"(f.tk[0]), "+v"(f.tk[1]), "+v"(f.tk[2]), "+v"(f.tk[3]), "+v"(f.tk[4]));
  };
  auto stage = [&](int j, auto P_, auto MODE_, auto RD_) __attribute__((always_inline)) {
    constexpr int P = decltype(P_)::value, MODE = decltype(MODE_)::value, RD = decltype(RD_)::value;
    const Fr& c = fr[P];
    Fr& nx = fr[P ^ 1];
    const bf16x8 w0 = __builtin_shufflevector(c.w0l, c.w0h, 0, 1, 2, 3, 4, 5, 6, 7), w1 = __builtin_shufflevector(c.w1l, c.w1h, 0, 1, 2, 3, 4, 5, 6, 7);
#pragma unroll
    for (int t = 0; t < 5; ++t) {
      if (MODE == 1) issue1(j + 4, t);
      if (MODE == 2 && t == 0) {
        const int o0 = wid * 1024 + lane * 16, o1 = 8192 + wid * 256 + lane * 16;
        auto src = [&](int o) { int T = tt * 5 + (o >> 11); T = T < 128 ? T : 127; return (unsigned)((T * 72 + et * 8) * 256 + (o & 2047)); };
        GLDS(rsH, mA, K4_HOFF, src(o0), 0);
        GLDS_N(0xffff, rsH, lds0 + wid * 256, K4_HOFF + 8192, src(o1), 0);
      }
      SCHED();
      acc[0][t] = MFMA16(w0, c.tk[t], acc[0][t]); acc[1][t] = MFMA16(w1, c.tk[t], acc[1][t]);
      SCHED();
      if (RD && t < 4) rd(nx, j + 1, t);
      SCHED();
    }
    if (RD) landed(nx);
  };
  using I0 = IC<0>; using I1 = IC<1>; using I2 = IC<2>;

  STAMP(ws, 3, 0);
  issue(0); issue(1); issue(2); issue(3);
  WAITV(15); BAR();
  STAMP(ws, 3, 1);
  rd(fr[0], 0, 0); rd(fr[0], 0, 1); rd(fr[0], 0, 2); rd(fr[0], 0, 3);
  landed(fr[0]);
  WAITV(10); BAR();
  for (int j = 0; j < 32; j += 2) {
    stage(j, I0{}, I1{}, I1{}); WAITV(10); BAR();
    stage(j + 1, I1{}, I1{}, I1{}); WAITV(10); BAR();
  }
  stage(32, I0{}, I2{}, I1{}); WAITV(7); BAR();
  stage(33, I1{}, I0{}, I1{}); WAITV(2); BAR();
  stage(34, I0{}, I0{}, I1{});
  stage(35, I1{}, I0{}, I0{});
  WAITV(0); BAR();
  STAMP(ws, 3, 2);
#pragma unroll
  for (int ef = 0; ef < 2; ++ef)
#pragma unroll
    for (int t = 0; t < 5; ++t)
      *(f32x4*)(smem + kq * K4_RKQ + (16 * t + r16) * K4_RSTR + (32 * eh + 16 * ef + 4 * q) * 4) = acc[ef][t];
  WAITL0(); BAR();
  const rsrc_t rsO = MKRS(out, S_ * D_ * 2);
  auto item = [&](int idx) {
    const int tok = idx >> 3, g8 = idx & 7;
    f32x4 lo = {0, 0, 0, 0}, hi = {0, 0, 0, 0};
#pragma unroll
    for (int k = 0; k < 4; ++k) {
      const char* p = smem + k * K4_RKQ + tok * K4_RSTR + g8 * 32;
      lo += *(const f32x4*)p; hi += *(const f32x4*)(p + 16);
    }
    u32x4 hv = *(const u32x4*)(smem + K4_HOFF + (tok >> 4) * 2048 + g8 * 256 + (tok & 15) * 16);
    if (g8 & 1) hv = u32x4{hv[2], hv[3], hv[0], hv[1]};
    const u32x4 ov = {pk_bf16(lo[0] + bf_lo(hv[0]), lo[1] + bf_hi(hv[0])), pk_bf16(lo[2] + bf_lo(hv[1]), lo[3] + bf_hi(hv[1])),
                      pk_bf16(hi[0] + bf_lo(hv[2]), hi[1] + bf_hi(hv[2])), pk_bf16(hi[2] + bf_lo(hv[3]), hi[3] + bf_hi(hv[3]))};
    const int tg = tt * 80 + tok;
    if (tg < S_) __builtin_amdgcn_raw_buffer_store_b128(ov, rsO, (unsigned)(tg * 1152 + et * 128 + g8 * 16), 0, 0);
  };
  item(wid * 64 + lane);
  if (wid < 2) item(512 + wid * 64 + lane);
  STAMP(ws, 3, 3);
}

__global__ void k5_diag(char* ws) {
  __shared__ unsigned long long mn[16], mx[16];
  __shared__ float cy[12];
  const int b = threadIdx.x;
  if (b < 16) { mn[b] = ~0ull; mx[b] = 0; }
  if (b < 12) cy[b] = 0.f;
  __syncthreads();
  for (int k = 0; k < 4; ++k) {
    if (k == 3 && b >= 234) continue;
    const unsigned long long* p = (const unsigned long long*)(ws + (64u << 20) + k * 65536 + b * 128);
    for (int s = 0; s < 4; ++s) { atomicMin(&mn[k * 4 + s], p[2 * s + 1]); atomicMax(&mx[k * 4 + s], p[2 * s + 1]); }
    for (int s = 0; s < 3; ++s) atomicAdd(&cy[k * 3 + s], (float)(p[2 * s + 2] - p[2 * s]) / (k == 3 ? 234.f : 256.f));
  }
  __syncthreads();
  if (b == 0) {
    const unsigned long long t0 = mn[0];
    printf("TL K1 l0 %.2f le %.2f end %.2f-%.2f (%.0f %.0f %.0f) K2 st %.2f l0 %.2f le %.2f end %.2f-%.2f (%.0f %.0f %.0f) K3 st %.2f l0 %.2f le %.2f end %.2f (%.0f %.0f %.0f) K4 st %.2f l0 %.2f le %.2f end %.2f (%.0f %.0f %.0f)\n",
      (mx[1]-t0)/100.0, (mx[2]-t0)/100.0, (mn[3]-t0)/100.0, (mx[3]-t0)/100.0, cy[0], cy[1], cy[2],
      (mn[4]-t0)/100.0, (mx[5]-t0)/100.0, (mx[6]-t0)/100.0, (mn[7]-t0)/100.0, (mx[7]-t0)/100.0, cy[3], cy[4], cy[5],
      (mn[8]-t0)/100.0, (mx[9]-t0)/100.0, (mx[10]-t0)/100.0, (mx[11]-t0)/100.0, cy[6], cy[7], cy[8],
      (mn[12]-t0)/100.0, (mx[13]-t0)/100.0, (mx[14]-t0)/100.0, (mx[15]-t0)/100.0, cy[9], cy[10], cy[11]);
  }
}

extern "C" void kernel_launch(void* const* d_in, const int* in_sizes, int n_in, void* d_out, int out_size, void* d_ws, size_t ws_size,
                              hipStream_t stream) {
  const bf16_t* x = (const bf16_t*)d_in[0];
  const bf16_t* cre = (const bf16_t*)d_in[1];
  const bf16_t* cim = (const bf16_t*)d_in[2];
  const bf16_t* wre = (const bf16_t*)d_in[3];
  const bf16_t* wim = (const bf16_t*)d_in[4];
  const bf16_t* wg = (const bf16_t*)d_in[6];
  const bf16_t* wu = (const bf16_t*)d_in[7];
  const bf16_t* wd = (const bf16_t*)d_in[8];
  char* ws = (char*)d_ws;
  k1_hid<<<256, 512, 0, stream>>>(x, wre, wim, cre, cim, ws);
  k2_seq<<<256, 512, 0, stream>>>(x, cre, cim, wg, wu, ws);
  k3_gu<<<256, 512, 0, stream>>>(wg, wu, wd, ws);
  k4_down<<<234, 512, 0, stream>>>(wd, ws, (bf16_t*)d_out);
  k5_diag<<<1, 256, 0, stream>>>(ws);
}
```

```cpp
#include <hip/hip_runtime.h>
#include <stdint.h>

typedef unsigned short bf16_t;
typedef short bf16x8 __attribute__((ext_vector_type(8)));
typedef short s16x4 __attribute__((ext_vector_type(4)));
typedef float f32x4 __attribute__((ext_vector_type(4)));
typedef float f32x2 __attribute__((ext_vector_type(2)));
typedef unsigned u32x4 __attribute__((ext_vector_type(4)));
typedef unsigned u32x2 __attribute__((ext_vector_type(2)));
typedef __bf16 bf2_t __attribute__((ext_vector_type(2)));
typedef __amdgpu_buffer_rsrc_t rsrc_t;

constexpr int S_ = 2048, D_ = 576, F_ = 4608;
constexpr float LN_EPS = 1e-5f;

constexpr unsigned WS_BP = 0;
constexpr unsigned BP_BYTES = 8u * 32u * 18432u;
constexpr unsigned WS_HT = WS_BP + BP_BYTES;
constexpr unsigned HT_BYTES = 128u * 72u * 256u;
constexpr unsigned WS_ST = WS_HT + HT_BYTES;
constexpr unsigned ST_BYTES = 2048u * 8u * 8u;
constexpr unsigned WS_ACT = WS_ST + ST_BYTES;
constexpr unsigned ACT_BYTES = 128u * 144u * 1024u;
constexpr unsigned WS_CS = WS_ACT + ACT_BYTES;
constexpr unsigned CS_BYTES = 32u * 4608u;

#define LDSP(p) ((__attribute__((address_space(3))) void*)(p))
#define MKRS(p, n) __builtin_amdgcn_make_buffer_rsrc((void*)(p), 0, (n), 0x00020000)
#define GLDS(rs, m0base, imm, voff, soff)                                                              \
  asm volatile("s_add_u32 m0, %0, %1\n\ts_nop 0\n\tbuffer_load_dwordx4 %2, %3, %4 offen lds"          \
               ::"s"(m0base), "i"(imm), "v"(voff), "s"(rs), "s"(soff) : "memory", "scc")
#define GLDS_N(mask, rs, m0base, imm, voff, soff)                                                      \
  asm volatile("s_add_u32 m0, %0, %1\n\ts_mov_b64 exec, " #mask "\n\tbuffer_load_dwordx4 %2, %3, %4 offen lds\n\ts_mov_b64 exec, -1" \
               ::"s"(m0base), "i"(imm), "v"(voff), "s"(rs), "s"(soff) : "memory", "scc")
#define GLDS_36(rs, m0base, imm, voff, soff)                                                           \
  asm volatile("s_add_u32 m0, %0, %1\n\ts_mov_b32 exec_hi, 0xf\n\tbuffer_load_dwordx4 %2, %3, %4 offen lds\n\ts_mov_b32 exec_hi, -1" \
               ::"s"(m0base), "i"(imm), "v"(voff), "s"(rs), "s"(soff) : "memory", "scc")
#define TOUCH(rs, mscr, voff)                                                                          \
  asm volatile("s_mov_b32 m0, %0\n\ts_nop 0\n\tbuffer_load_dword %1, %2, 0 offen lds" ::"s"(mscr), "v"(voff), "s"(rs) : "memory")
__device__ __forceinline__ u32x4 mksrd(const void* p, unsigned n) {
  const unsigned long long a = (unsigned long long)p;
  return u32x4{(unsigned)a, (unsigned)(a >> 32) & 0xffffu, n, 0x00020000u};
}
#define CODE_SRD(dst, sym, n)                                                                          \
  do {                                                                                                 \
    unsigned lo_, hi_;                                                                                 \
    asm volatile("s_getpc_b64 s[20:21]\n\ts_add_u32 %0, s20, " sym "@rel32@lo+4\n\ts_addc_u32 %1, s21, " sym "@rel32@hi+12" \
                 : "=s"(lo_), "=s"(hi_)::"s20", "s21", "scc");                                         \
    dst = u32x4{lo_, hi_ & 0xffffu, (n), 0x00020000u};                                                 \
  } while (0)
#define CODE_TOUCH(sym, n, mscr, i, wid)                                                               \
  do { u32x4 rc_; CODE_SRD(rc_, sym, n); TOUCH(rc_, mscr, (unsigned)(((i) * 8 + (wid)) * 128)); } while (0)
__device__ __forceinline__ unsigned lds_base(char* smem) { return (unsigned)(size_t)LDSP(smem); }
#define WAITV(n) asm volatile("s_waitcnt vmcnt(" #n ")" ::: "memory")
#define WAITL0() asm volatile("s_waitcnt lgkmcnt(0)" ::: "memory")
#define PIN(x) asm volatile("" : "+v"(x))
#define BAR() __builtin_amdgcn_s_barrier()
#define SCHED() __builtin_amdgcn_sched_barrier(0)
#define MFMA16(a, b, c) __builtin_amdgcn_mfma_f32_16x16x32_bf16((a), (b), (c), 0, 0, 0)
constexpr int AUX_SC1 = 16;
template <int V> struct IC { static constexpr int value = V; };
#define CT2_MODE 1
#define CT3_MODE 2
#define K2_VAR 0
#define K4_VAR 0
#define TSX(var, j) do { } while (0)
#define TS_OUT(ws, kid, wid, lane, k, var) do { } while (0)
#define STAMP(ws, kid, slot) do { } while (0)

__device__ __forceinline__ unsigned pk_bf16(float lo, float hi) {
  f32x2 v = {lo, hi};
  bf2_t b = __builtin_convertvector(v, bf2_t);
  return __builtin_bit_cast(unsigned, b);
}
__device__ __forceinline__ float bf_lo(unsigned w) { return __uint_as_float(w << 16); }
__device__ __forceinline__ float bf_hi(unsigned w) { return __uint_as_float(w & 0xffff0000u); }
__device__ __forceinline__ float dot_ones(unsigned w, float acc) {
  const bf2_t ones = {(__bf16)1.0f, (__bf16)1.0f};
  return __builtin_amdgcn_fdot2_f32_bf16(__builtin_bit_cast(bf2_t, w), ones, acc, false);
}
__device__ __forceinline__ float dot_frag(bf16x8 f, float acc) {
  u32x4 u = __builtin_bit_cast(u32x4, f);
  acc = dot_ones(u[0], acc); acc = dot_ones(u[1], acc); acc = dot_ones(u[2], acc); acc = dot_ones(u[3], acc);
  return acc;
}

constexpr int K1_SLOT = 8192 + 18432, K1_NS = 5, K1_ST = K1_NS * K1_SLOT, K1_WS = K1_ST + 512, K1_SCR = K1_WS + 1152, K1_LDS = K1_SCR + 256;

template <int NF>
__device__ __forceinline__ void k1_body(char* smem, u32x4 rsX, u32x4 rsW, u32x4 rsC, rsrc_t rsO, int st, int et, int wid, int lane, int nf0,
                                        unsigned vhead, bool headx, char* ws) {
  STAMP(ws, 0, 0);
  const int tid = wid * 64 + lane;
  const int sh = wid >> 2, q = lane >> 4, r16 = lane & 15, r8 = lane >> 3, c8 = lane & 7;
  const unsigned vx = (unsigned)(64 * st + 8 * wid + r8) * 1152u + (unsigned)((c8 ^ ((r8 & 3) | ((wid & 1) << 2))) * 16);
  const unsigned vw = (unsigned)((et & 3) * 144 + 8 * wid + r8) * 1152u + (unsigned)((c8 ^ r8) * 16);
  const int r2 = 128 + 2 * wid + r8;
  const unsigned vw2 = (unsigned)((et & 3) * 144 + r2) * 1152u + (unsigned)((c8 ^ (r2 & 7)) * 16);
  const unsigned lds0 = lds_base(smem);
  const unsigned mA = lds0 + wid * 1024, mP = lds0 + wid * 256;
  auto issue1 = [&](int kb, int k) __attribute__((always_inline)) {
    const int so = (kb % K1_NS) * K1_SLOT;
    if (k == 0) GLDS(rsX, mA, so, vx, kb * 128);
    if (k == 1) GLDS(rsW, mA, so + 8192, vw, kb * 128);
    if (k == 2) GLDS(rsW, mA, so + 16384, vw, kb * 128 + 64 * 1152);
    if (k == 3) GLDS_N(0xffff, rsW, mP, so + 24576, vw2, kb * 128);
  };
  auto issue = [&](int kb) __attribute__((always_inline)) { issue1(kb, 0); issue1(kb, 1); issue1(kb, 2); issue1(kb, 3); };
  unsigned ax[2];
#pragma unroll
  for (int m = 0; m < 2; ++m) {
    const int row = 32 * sh + 8 * (r16 >> 2) + 4 * m + (r16 & 3);
    const int key = (row & 3) | ((row >> 1) & 4);
    ax[m] = (unsigned)(row * 128 + ((q ^ key) * 16)) ^ (unsigned)(sh * 64);
  }
  const unsigned aw = (unsigned)(8192 + (nf0 * 16 + r16) * 128 + ((q ^ (r16 & 7)) * 16)) ^ (unsigned)(sh * 64);

  f32x4 acc[2][NF];
  float wsum[NF];
#pragma unroll
  for (int n = 0; n < NF; ++n) { wsum[n] = 0.f; acc[0][n] = f32x4{0, 0, 0, 0}; acc[1][n] = f32x4{0, 0, 0, 0}; }
  float shift = 0.f;
  f32x2 s1 = {0.f, 0.f}, s2 = {0.f, 0.f};

  struct Fr { u32x4 v; bf16x8 a[2][2], b[2][NF]; };
  Fr fr[2];
  auto rd = [&](Fr& f, int kb, int part) __attribute__((always_inline)) {
    const char* sl = smem + (kb % K1_NS) * K1_SLOT;
    if (part == 0) {
      f.a[0][0] = *(const bf16x8*)(sl + ax[0]); f.a[0][1] = *(const bf16x8*)(sl + ax[1]);
#pragma unroll
      for (int n = 0; n < NF; ++n) f.b[0][n] = *(const bf16x8*)(sl + aw + n * 2048);
    } else {
      f.v = *(const u32x4*)(sl + tid * 16);
      f.a[1][0] = *(const bf16x8*)(sl + (ax[0] ^ 64)); f.a[1][1] = *(const bf16x8*)(sl + (ax[1] ^ 64));
#pragma unroll
      for (int n = 0; n < NF; ++n) f.b[1][n] = *(const bf16x8*)(sl + ((aw ^ 64) + n * 2048));
    }
  };
  CODE_TOUCH("k1_hid", 32768u, lds0 + K1_SCR, blockIdx.x >> 3, wid);
  issue(0); issue(1);
  TOUCH(headx ? rsX : rsW, lds0 + K1_SCR, vhead);
  issue(2); issue(3); issue(4);
  WAITV(17); BAR();
  STAMP(ws, 0, 1);
  rd(fr[0], 0, 0); rd(fr[0], 0, 1);
  WAITL0();
  WAITV(13); BAR();
#pragma unroll
  for (int kb = 0; kb < 9; ++kb) {
    const Fr& c = fr[kb & 1];
    Fr& nx = fr[(kb & 1) ^ 1];
    const bool stg = kb + 5 < 9;
    if (stg) issue1(kb + 5, 0);
    if (kb == 4)
      TOUCH(rsC, lds0 + K1_SCR, (unsigned)(blockIdx.x * 32768 + (wid & 3) * 8192 + lane * 128));
    SCHED();
#pragma unroll
    for (int n = 0; n < NF; ++n) { acc[0][n] = MFMA16(c.a[0][0], c.b[0][n], acc[0][n]); acc[1][n] = MFMA16(c.a[0][1], c.b[0][n], acc[1][n]); }
    SCHED();
    if (kb < 8) rd(nx, kb + 1, 0);
    SCHED();
    if (stg) issue1(kb + 5, 1);
    SCHED();
    if (kb == 0) shift = bf_lo(c.v[0]);
#pragma unroll
    for (int i = 0; i < 4; ++i) {
      f32x2 d = {bf_lo(c.v[i]) - shift, bf_hi(c.v[i]) - shift};
      s1 += d; s2 += d * d;
    }
    PIN(s1); PIN(s2);
    SCHED();
    if (stg) issue1(kb + 5, 2);
    SCHED();
#pragma unroll
    for (int n = 0; n < NF; ++n) { acc[0][n] = MFMA16(c.a[1][0], c.b[1][n], acc[0][n]); acc[1][n] = MFMA16(c.a[1][1], c.b[1][n], acc[1][n]); }
    SCHED();
    if (kb < 8) rd(nx, kb + 1, 1);
    SCHED();
#pragma unroll
    for (int n = 0; n < NF; ++n) wsum[n] = dot_frag(c.b[0][n], wsum[n]);
#pragma unroll
    for (int n = 0; n < NF; ++n) PIN(wsum[n]);
    SCHED();
    if (stg) issue1(kb + 5, 3);
    WAITL0();
    if (kb <= 3) WAITV(12); else if (kb == 4) WAITV(9); else if (kb == 5) WAITV(5); else if (kb == 6) WAITV(1);
    if (kb < 7) BAR();
    SCHED();
  }
  STAMP(ws, 0, 2);
  {
    const float a1 = s1[0] + s1[1], a2 = s2[0] + s2[1];
    float mean = shift + a1 * (1.f / 72.f);
    float m2 = a2 - a1 * a1 * (1.f / 72.f);
    float n = 72.f;
#pragma unroll
    for (int o = 1; o < 8; o <<= 1) {
      const float mo = __shfl_xor(mean, o), qo = __shfl_xor(m2, o);
      const float dl = mo - mean;
      m2 = m2 + qo + dl * dl * (n * 0.5f);
      mean = 0.5f * (mean + mo);
      n *= 2.f;
    }
    const float r = __builtin_amdgcn_rsqf(m2 * (1.f / 576.f) + LN_EPS);
    if (c8 == 0) *(f32x2*)(smem + K1_ST + (tid >> 3) * 8) = f32x2{r, mean * r};
#pragma unroll
    for (int n = 0; n < NF; ++n) {
      float w = wsum[n];
      w += __shfl_xor(w, 16); w += __shfl_xor(w, 32);
      if (q == 0) *(float*)(smem + K1_WS + sh * 576 + ((nf0 + n) * 16 + r16) * 4) = w;
    }
  }
  WAITL0();
  BAR();
  f32x2 rs[8];
  {
    const char* p = smem + K1_ST + (32 * sh + 8 * q) * 8;
#pragma unroll
    for (int i = 0; i < 4; ++i) {
      f32x4 v = *(const f32x4*)(p + i * 16);
      rs[2 * i] = f32x2{v[0], v[1]}; rs[2 * i + 1] = f32x2{v[2], v[3]};
    }
  }
  const float sgn = et < 4 ? 1.f : -1.f;
  const int kt = (et < 4 ? 0 : 16) + (st >> 1), ksg = (st & 1) * 2 + sh;
#pragma unroll
  for (int n = 0; n < NF; ++n) {
    const int el = (nf0 + n) * 16 + r16;
    const float ws = *(const float*)(smem + K1_WS + el * 4) + *(const float*)(smem + K1_WS + 576 + el * 4);
    float o[8];
#pragma unroll
    for (int m = 0; m < 2; ++m)
#pragma unroll
      for (int j = 0; j < 4; ++j) o[4 * m + j] = sgn * (acc[m][n][j] * rs[4 * m + j][0] - rs[4 * m + j][1] * ws);
    u32x4 pk = {pk_bf16(o[0], o[1]), pk_bf16(o[2], o[3]), pk_bf16(o[4], o[5]), pk_bf16(o[6], o[7])};
    const int hi = el >= 72 ? 1 : 0, e72 = el - 72 * hi;
    const int etg = (et & 3) * 2 + hi;
    const unsigned inner = e72 < 64 ? (unsigned)((e72 >> 4) * 1024 + q * 256 + (e72 & 15) * 16) : (unsigned)(4096 + q * 128 + (e72 & 7) * 16);
    const unsigned off = (unsigned)((etg * 32 + kt) * 4 + ksg) * 4608u + inner;
    __builtin_amdgcn_raw_buffer_store_b128(pk, rsO, off, 0, AUX_SC1);
  }
  WAITV(0);
  STAMP(ws, 0, 3);
}

extern "C" __global__ __launch_bounds__(512, 2) void k1_hid(const bf16_t* x, const bf16_t* wre, const bf16_t* wim, const bf16_t* cre, const bf16_t* cim, char* ws) {
  __shared__ __attribute__((aligned(16))) char smem[K1_LDS];
  const int wid = __builtin_amdgcn_readfirstlane(threadIdx.x >> 6), lane = threadIdx.x & 63;
  const int b = blockIdx.x, xcd = b & 7, i = b >> 3;
  const int st = xcd * 4 + (i & 3), et = i >> 2;
  const u32x4 rsX = mksrd(x, S_ * D_ * 2);
  const u32x4 rsW = mksrd(et < 4 ? wre : wim, D_ * D_ * 2);
  const u32x4 rsC = mksrd(wid < 4 ? cre : cim, S_ * S_ * 2);
  rsrc_t rsO = MKRS(ws + WS_BP, BP_BYTES);
  unsigned vhead;
  if (wid == 7) {
    const int l = lane < 32 ? lane : 31;
    vhead = (unsigned)((64 * st + 8 * et + (l >> 2)) * 1152 + (5 + (l & 3)) * 128);
  } else {
    int l = wid * 21 + (lane < 21 ? lane : 20);
    l = l < 144 ? l : 143;
    vhead = (unsigned)(((et & 3) * 144 + 36 * (i & 3) + (l >> 2)) * 1152 + (5 + (l & 3)) * 128);
  }
  const int w4 = wid & 3;
  if (wid == 0 || wid == 7) k1_body<3>(smem, rsX, rsW, rsC, rsO, st, et, wid, lane, wid == 0 ? 0 : 6, vhead, wid == 7, ws);
  else k1_body<2>(smem, rsX, rsW, rsC, rsO, st, et, wid, lane, wid < 4 ? 1 + 2 * w4 : 2 * w4, vhead, false, ws);
}

constexpr int K2_SLOT = 16384 + 18432, K2_XOFF = 4 * K2_SLOT, K2_SCR = K2_XOFF + 9216, K2_LDS = K2_SCR + 256;
constexpr unsigned CT2_BYTES = 30080u, CT3_BYTES = 20608u;
constexpr int K2_RSTR = 336, K2_RKQ = 64 * K2_RSTR, K2_SOFF = 4 * K2_RKQ;

__device__ __forceinline__ int tid_cs(int wid, int lane) { return (wid - 1) * 64 + lane; }
constexpr int K2_CSO = 98304;
extern "C" __global__ __launch_bounds__(512, 2) void k2_seq(const bf16_t* x, const bf16_t* cre, const bf16_t* cim, const bf16_t* wg, const bf16_t* wu, char* ws) {
  __shared__ __attribute__((aligned(16))) char smem[K2_LDS];
  const int wid = __builtin_amdgcn_readfirstlane(threadIdx.x >> 6), lane = threadIdx.x & 63;
  const int b = blockIdx.x, xcd = b & 7, i = b >> 3;
  const int mt = (xcd >> 1) * 8 + (i & 7), nt = (xcd & 1) * 4 + (i >> 3);
  const u32x4 rsRe = mksrd(cre, S_ * S_ * 2), rsIm = mksrd(cim, S_ * S_ * 2);
  const u32x4 rsB = mksrd(ws + WS_BP, BP_BYTES);
  const u32x4 rsX = mksrd(x, S_ * D_ * 2);
  const int q = lane >> 4, r16 = lane & 15, r8 = lane >> 3, c8 = lane & 7;
  const int mh = wid & 1, kq = wid >> 1;

  const unsigned va = (unsigned)(64 * mt + 8 * wid + r8) * 4096u + (unsigned)((c8 ^ r8) * 16);
  const unsigned vb = (unsigned)(wid * 1024 + lane * 16);
  const unsigned vb2 = (unsigned)(16384 + wid * 256 + lane * 16);
  const unsigned mA = lds_base(smem) + wid * 1024, mP = lds_base(smem) + wid * 256;
  auto issue1 = [&](int j, u32x4 rsA, int k) __attribute__((always_inline)) {
    const unsigned so = (unsigned)((j & 3) * K2_SLOT), m0 = mA + so, m1 = mP + so;
    const int sa = (j & 15) * 256, sb = (nt * 32 + j) * 18432;
    if (k == 0) GLDS(rsA, m0, 0, va, sa);
    if (k == 1) GLDS(rsA, m0, 8192, va, sa + 128);
    if (k == 2) GLDS(rsB, m0, 16384, vb, sb);
    if (k == 3) GLDS(rsB, m0, 16384 + 8192, vb, sb + 8192);
    if (k == 4) GLDS_N(0xffff, rsB, m1, 16384 + 16384, vb2, sb);
  };
  auto issue = [&](int j, u32x4 rsA) __attribute__((always_inline)) {
    issue1(j, rsA, 0); issue1(j, rsA, 1); issue1(j, rsA, 2); issue1(j, rsA, 3); issue1(j, rsA, 4);
  };
  const unsigned at0 = (unsigned)((kq >> 1) * 8192 + (32 * mh + r16) * 128 + ((((kq & 1) * 4 + q) ^ (r16 & 7)) * 16));
  const unsigned ae0 = (unsigned)(16384 + kq * 4608 + lane * 16);
  const unsigned aeh = (unsigned)(16384 + kq * 4608 + 4096 + q * 128 + (lane & 7) * 16);

  f32x4 acc[5][2];
#pragma unroll
  for (int f = 0; f < 5; ++f) { acc[f][0] = f32x4{0, 0, 0, 0}; acc[f][1] = f32x4{0, 0, 0, 0}; }
  struct Fr { bf16x8 t0, t1, e[5]; };
  Fr fr[2];
  auto rd = [&](Fr& f, int j, int part) __attribute__((always_inline)) {
    const char* sl = smem + (j & 3) * K2_SLOT;
    if (part == 0) { f.t0 = *(const bf16x8*)(sl + at0); f.t1 = *(const bf16x8*)(sl + at0 + 2048); }
    if (part == 1) { f.e[0] = *(const bf16x8*)(sl + ae0); f.e[1] = *(const bf16x8*)(sl + ae0 + 1024); }
    if (part == 2) { f.e[2] = *(const bf16x8*)(sl + ae0 + 2048); f.e[3] = *(const bf16x8*)(sl + ae0 + 3072); }
    if (part == 3) f.e[4] = *(const bf16x8*)(sl + aeh);
  };
  const int cft = xcd * 4 + (i & 3), cm = (i >> 2) & 1, crg = i >> 3;
  u32x4 wv[6];
  auto stage = [&](int j, auto P_, u32x4 rsA, auto MODE_, auto RD_) __attribute__((always_inline)) {
    constexpr int P = decltype(P_)::value, MODE = decltype(MODE_)::value, RD = decltype(RD_)::value;
    const Fr& c = fr[P];
    Fr& nx = fr[P ^ 1];
#pragma unroll
    for (int f = 0; f < 5; ++f) {
      if (MODE == 1) issue1(j + 4, rsA, f);
      if (MODE == 2 && f == 0) {
        CODE_TOUCH("k2_seq", CT2_BYTES, lds_base(smem) + K2_SCR, i, wid);
        const int o0 = wid * 1024 + lane * 16, o1 = 8192 + wid * 128 + lane * 16;
        const int row0 = o0 / 144, row1 = o1 / 144;
        GLDS(rsX, mA, K2_XOFF, (unsigned)((64 * mt + row0) * 1152 + nt * 144 + (o0 - row0 * 144)), 0);
        GLDS_N(0xff, rsX, lds_base(smem) + wid * 128, K2_XOFF + 8192, (unsigned)((64 * mt + row1) * 1152 + nt * 144 + (o1 - row1 * 144)), 0);
      }
      if (MODE == 2 && f >= 2) {
        const u32x4 rsW = mksrd(cm ? wu : wg, D_ * F_ * 2);
        const int l54 = lane < 54 ? lane : 53, r3 = l54 / 18;
        const unsigned vo = (unsigned)((crg * 144 + wid * 18 + r3) * 9216 + cft * 288 + (l54 - r3 * 18) * 16);
#pragma unroll
        for (int k = 2 * (f - 2); k < 2 * (f - 2) + 2; ++k)
          asm volatile("buffer_load_dwordx4 %0, %1, %2, %3 offen" : "=v"(wv[k]) : "v"(vo), "s"(rsW), "s"(k * 3 * 9216) : "memory");
      }
      SCHED();
      acc[f][0] = MFMA16(c.e[f], c.t0, acc[f][0]); acc[f][1] = MFMA16(c.e[f], c.t1, acc[f][1]);
      SCHED();
      if (RD && f < 4) rd(nx, j + 1, f);
      SCHED();
    }
    WAITL0();
  };
  using I0 = IC<0>; using I1 = IC<1>; using I2 = IC<2>;

  STAMP(ws, 1, 0);
  issue(0, rsRe); issue(1, rsRe); issue(2, rsRe); issue(3, rsRe);
  WAITV(15); BAR();
  STAMP(ws, 1, 1);
  rd(fr[0], 0, 0); rd(fr[0], 0, 1); rd(fr[0], 0, 2); rd(fr[0], 0, 3);
  WAITL0();
  WAITV(10); BAR();
  for (int j = 0; j < 12; j += 2) {
    stage(j, I0{}, rsRe, I1{}, I1{}); WAITV(10); BAR();
    stage(j + 1, I1{}, rsRe, I1{}, I1{}); WAITV(10); BAR();
  }
  for (int j = 12; j < 28; j += 2) {
    stage(j, I0{}, rsIm, I1{}, I1{}); WAITV(10); BAR();
    stage(j + 1, I1{}, rsIm, I1{}, I1{}); WAITV(10); BAR();
  }
  stage(28, I0{}, rsIm, I2{}, I1{}); WAITV(14); BAR();
  stage(29, I1{}, rsIm, I0{}, I1{}); WAITV(9); BAR();
  stage(30, I0{}, rsIm, I0{}, I1{});
  stage(31, I1{}, rsIm, I0{}, I0{});
  WAITV(6); BAR();
  STAMP(ws, 1, 2);
#pragma unroll
  for (int f = 0; f < 5; ++f)
#pragma unroll
    for (int tf = 0; tf < 2; ++tf)
      *(f32x4*)(smem + kq * K2_RKQ + (32 * mh + 16 * tf + r16) * K2_RSTR + (16 * f + 4 * q) * 4) = acc[f][tf];
  asm volatile("s_waitcnt vmcnt(0)" : "+v"(wv[0]), "+v"(wv[1]), "+v"(wv[2]), "+v"(wv[3]), "+v"(wv[4]), "+v"(wv[5]) :: "memory");
  {
    f32x4 lo = {0, 0, 0, 0}, hi = {0, 0, 0, 0};
#pragma unroll
    for (int k = 0; k < 6; ++k) {
      lo += f32x4{bf_lo(wv[k][0]), bf_hi(wv[k][0]), bf_lo(wv[k][1]), bf_hi(wv[k][1])};
      hi += f32x4{bf_lo(wv[k][2]), bf_hi(wv[k][2]), bf_lo(wv[k][3]), bf_hi(wv[k][3])};
    }
    if (lane < 54) {
      char* p = smem + K2_CSO + (wid * 3 * 144) * 4 + lane * 32;
      *(f32x4*)p = lo; *(f32x4*)(p + 16) = hi;
    }
  }
  WAITL0(); BAR();
  const rsrc_t rsH = MKRS(ws + WS_HT, HT_BYTES);
  auto item = [&](int U) {
    const int t = lane;
    f32x4 lo = {0, 0, 0, 0}, hi = {0, 0, 0, 0};
#pragma unroll
    for (int k = 0; k < 4; ++k) {
      const char* p = smem + k * K2_RKQ + t * K2_RSTR + U * 32;
      lo += *(const f32x4*)p; hi += *(const f32x4*)(p + 16);
    }
    const u32x4 xv = *(const u32x4*)(smem + K2_XOFF + t * 144 + U * 16);
    u32x4 hv = {pk_bf16(lo[0] + bf_lo(xv[0]), lo[1] + bf_hi(xv[0])), pk_bf16(lo[2] + bf_lo(xv[1]), lo[3] + bf_hi(xv[1])),
                pk_bf16(hi[0] + bf_lo(xv[2]), hi[1] + bf_hi(xv[2])), pk_bf16(hi[2] + bf_lo(xv[3]), hi[3] + bf_hi(xv[3]))};
    float v[8] = {bf_lo(hv[0]), bf_hi(hv[0]), bf_lo(hv[1]), bf_hi(hv[1]), bf_lo(hv[2]), bf_hi(hv[2]), bf_lo(hv[3]), bf_hi(hv[3])};
    float m = 0.f;
#pragma unroll
    for (int k = 0; k < 8; ++k) m += v[k];
    m *= 0.125f;
    float m2 = 0.f;
#pragma unroll
    for (int k = 0; k < 8; ++k) { const float d = v[k] - m; m2 += d * d; }
    *(f32x2*)(smem + K2_SOFF + (t * 9 + U) * 8) = f32x2{m, m2};
    const int Ug = nt * 9 + U, tg = 64 * mt + t;
    if (Ug & 1) hv = u32x4{hv[2], hv[3], hv[0], hv[1]};
    __builtin_amdgcn_raw_buffer_store_b128(hv, rsH, (unsigned)(((tg >> 4) * 72 + Ug) * 256 + (tg & 15) * 16), 0, AUX_SC1);
  };
  item(wid);
  if (wid == 0) item(8);
  WAITL0(); BAR();
  if (wid == 0) {
    const char* p = smem + K2_SOFF + lane * 72;
    f32x2 s[9];
    float m = 0.f;
#pragma unroll
    for (int k = 0; k < 9; ++k) { s[k] = *(const f32x2*)(p + k * 8); m += s[k][0]; }
    m *= (1.f / 9.f);
    float m2 = 0.f;
#pragma unroll
    for (int k = 0; k < 9; ++k) { const float d = s[k][0] - m; m2 += s[k][1] + 8.f * d * d; }
    const rsrc_t rsS = MKRS(ws + WS_ST, ST_BYTES);
    __builtin_amdgcn_raw_buffer_store_b64(__builtin_bit_cast(u32x2, f32x2{m, m2}), rsS, (unsigned)(((64 * mt + lane) * 8 + nt) * 8), 0, AUX_SC1);
  }
  if (wid >= 1 && wid <= 3) {
    const int c = tid_cs(wid, lane);
    if (c < 144) {
      float a = 0.f;
#pragma unroll
      for (int k = 0; k < 24; ++k) a += *(const float*)(smem + K2_CSO + (k * 144 + c) * 4);
      const rsrc_t rsC = MKRS(ws + WS_CS, CS_BYTES);
      __builtin_amdgcn_raw_buffer_store_b32(__float_as_uint(a), rsC, (unsigned)(cft * 4608 + crg * 1152 + cm * 576 + c * 4), 0, AUX_SC1);
    }
  }
  STAMP(ws, 1, 3);
}

constexpr int K3_SLOT = 16384 + 18432, K3_AUX = 4 * K3_SLOT, K3_TOK = K3_AUX + 16384, K3_CS = K3_TOK + 2048, K3_LDS = K3_CS + 4608;

#define DSR128(dst, addr, off) asm volatile("ds_read_b128 %0, %1 offset:%2" : "=v"(dst) : "v"(addr), "i"(off))
#define DSRTR(dst, addr, off) asm volatile("ds_read_b64_tr_b16 %0, %1 offset:%2" : "=v"(dst) : "v"(addr), "i"(off))

template <int NCH>
__device__ __forceinline__ void k3_body(char* smem, u32x4 rsH, u32x4 rsG, u32x4 rsU, u32x4 rsS, u32x4 rsC, u32x4 rsD, rsrc_t rsO, int tt, int ft,
                                        int wid, int lane, char* ws) {
  STAMP(ws, 2, 0);
  constexpr bool LEAD = NCH == 5;
  const int tg = wid & 3, q = lane >> 4, r16 = lane & 15;
  const int odd = ft & 1;
  const int p0 = odd;
  const int cL = odd ? 0 : 8;
  const unsigned vt = (unsigned)((tt * 16 + wid) * 72 * 256 + lane * 16);
  unsigned vw0, vw1, vw2;
  {
    const int o0 = wid * 1024 + lane * 16, r0 = o0 / 288;
    vw0 = (unsigned)(r0 * 9216 + ft * 288 + (o0 - r0 * 288));
    const int o1 = (wid == 0 ? 8192 : (wid - 1) * 1024) + lane * 16, r1 = o1 / 288;
    vw1 = (unsigned)(r1 * 9216 + ft * 288 + (o1 - r1 * 288));
    const int o2 = 7168 + wid * 256 + lane * 16, r2 = o2 / 288;
    vw2 = (unsigned)(r2 * 9216 + ft * 288 + (o2 - r2 * 288));
  }
  const u32x4 rsW1 = wid == 0 ? rsG : rsU;
  const unsigned lds0 = lds_base(smem);
  const unsigned mA = lds0 + wid * 1024, mP = lds0 + wid * 256;
  auto issue1 = [&](int j, int k) __attribute__((always_inline)) {
    const unsigned so = (unsigned)((j & 3) * K3_SLOT), m0 = mA + so, m1 = mP + so;
    if (k == 0) GLDS(rsH, m0, 0, vt, j * 1024);
    if (k == 1) GLDS(rsH, m0, 8192, vt, j * 1024 + 8 * 72 * 256);
    if (k == 2) GLDS(rsG, m0, 16384, vw0, j * 294912);
    if (k == 3) GLDS(rsW1, m0, 16384 + 8192, vw1, j * 294912);
    if (k == 4) GLDS_N(0xffff, rsU, m1, 16384 + 16384, vw2, j * 294912);
  };
  auto issue = [&](int j) __attribute__((always_inline)) { issue1(j, 0); issue1(j, 1); issue1(j, 2); issue1(j, 3); issue1(j, 4); };
  const unsigned atok = lds0 + (unsigned)(tg * 4096 + lane * 16);
  const int a4 = r16 >> 2, p4 = r16 & 3;
  const int cfirst = LEAD ? p0 : p0 + 4;
  const unsigned aw1 = lds0 + (unsigned)(16384 + (8 * q + 4 * (q & 1) + a4) * 288 + cfirst * 32 + p4 * 8);
  const unsigned aw2 = lds0 + (unsigned)(16384 + (8 * q + 4 * (1 - (q & 1)) + a4) * 288 + cfirst * 32 + p4 * 8);
  const int dL = (cL - cfirst) * 32;

  f32x4 ag[NCH][4], au[NCH][4];
#pragma unroll
  for (int c = 0; c < NCH; ++c)
#pragma unroll
    for (int t = 0; t < 4; ++t) { ag[c][t] = f32x4{0, 0, 0, 0}; au[c][t] = f32x4{0, 0, 0, 0}; }

  bf16x8 tk[2][4];
  s16x4 w[2][4];
  auto rdw = [&](int buf, unsigned b1, unsigned b2, int off, int i) __attribute__((always_inline)) {
    if (i == 0) DSRTR(w[buf][0], b1, off);
    if (i == 1) DSRTR(w[buf][1], b2, off);
    if (i == 2) DSRTR(w[buf][2], b1, off + 9216);
    if (i == 3) DSRTR(w[buf][3], b2, off + 9216);
  };
  auto stage = [&](int j, auto P_) __attribute__((always_inline)) {
    constexpr int P = decltype(P_)::value;
    constexpr bool NXT = true;
    const bool STG = j < 15;
    const unsigned so = (unsigned)((j & 3) * K3_SLOT), sn = (unsigned)(((j + 1) & 3) * K3_SLOT);
    const unsigned b1 = aw1 + so, b2 = aw2 + so, n1 = aw1 + sn, n2 = aw2 + sn, nt = atok + sn;
    const unsigned l1 = b1 + (unsigned)dL, l2 = b2 + (unsigned)dL;
#pragma unroll
    for (int c = 0; c < NCH; ++c) {
      const int cb = LEAD ? ((P + c) & 1) : (c & 1), nb = cb ^ 1;
      const bool last = c == NCH - 1;
      if (c > 0) asm volatile("s_waitcnt lgkmcnt(0)" : "+v"(w[cb][0]), "+v"(w[cb][1]), "+v"(w[cb][2]), "+v"(w[cb][3]));
      if (c == 0) TSX(ts[7], j); else TSX(ts[c - 1], j);
      if (P == 0 && last && j == 0) { WAITV(9); BAR(); }
      const bf16x8 g = __builtin_shufflevector(w[cb][0], w[cb][1], 0, 1, 2, 3, 4, 5, 6, 7);
      const bf16x8 u = __builtin_shufflevector(w[cb][2], w[cb][3], 0, 1, 2, 3, 4, 5, 6, 7);
      SCHED();
#pragma unroll
      for (int t = 0; t < 4; ++t) {
        ag[c][t] = MFMA16(g, tk[P][t], ag[c][t]); au[c][t] = MFMA16(u, tk[P][t], au[c][t]);
        SCHED();
        if (!last) {
          if (t < 2) {
            const bool lc = LEAD && c == 3;
            rdw(nb, lc ? l1 : b1, lc ? l2 : b2, lc ? 0 : (c + 1) * 32, 2 * t);
            rdw(nb, lc ? l1 : b1, lc ? l2 : b2, lc ? 0 : (c + 1) * 32, 2 * t + 1);
          }
        } else if (NXT) {
          if (t == 0) { DSR128(tk[P ^ 1][0], nt, 0); rdw(nb, n1, n2, 0, 0); rdw(nb, n1, n2, 0, 1); }
          if (t == 1) { rdw(nb, n1, n2, 0, 2); rdw(nb, n1, n2, 0, 3); DSR128(tk[P ^ 1][1], nt, 1024); }
          if (t == 2) { DSR128(tk[P ^ 1][2], nt, 2048); DSR128(tk[P ^ 1][3], nt, 3072); }
        }
        if (STG) {
          if (t == 2) issue1(j + 3, LEAD ? c : (c == 0 ? 0 : c + 1));
          if (!LEAD && c == 0 && t == 3) issue1(j + 3, 1);
        }
        SCHED();
      }
    }
    if (NXT)
      asm volatile("s_waitcnt lgkmcnt(0)" : "+v"(tk[P ^ 1][0]), "+v"(tk[P ^ 1][1]), "+v"(tk[P ^ 1][2]), "+v"(tk[P ^ 1][3]),
                   "+v"(w[LEAD ? (P ^ 1) : 0][0]), "+v"(w[LEAD ? (P ^ 1) : 0][1]), "+v"(w[LEAD ? (P ^ 1) : 0][2]), "+v"(w[LEAD ? (P ^ 1) : 0][3]));
    TSX(ts[5], j);
  };

  issue(0);
  GLDS(rsS, mA, K3_AUX, (unsigned)(tt * 16384 + wid * 1024 + lane * 16), 0);
  GLDS(rsS, mA, K3_AUX + 8192, (unsigned)(tt * 16384 + wid * 1024 + lane * 16), 8192);
  GLDS_36(rsC, lds0 + wid * 576, K3_CS, (unsigned)(ft * 4608 + wid * 576 + lane * 16), 0);
  issue(1); issue(2);
  WAITV(10); BAR();
  STAMP(ws, 2, 1);
  DSR128(tk[0][0], atok, 0); DSR128(tk[0][1], atok, 1024); DSR128(tk[0][2], atok, 2048); DSR128(tk[0][3], atok, 3072);
  rdw(0, aw1, aw2, 0, 0); rdw(0, aw1, aw2, 0, 1); rdw(0, aw1, aw2, 0, 2); rdw(0, aw1, aw2, 0, 3);
  if constexpr (!LEAD) {
    const int t = tg * 64 + lane;
    const char* p = smem + K3_AUX + t * 64;
    f32x4 s[4];
    float m = 0.f;
#pragma unroll
    for (int k = 0; k < 4; ++k) { s[k] = *(const f32x4*)(p + k * 16); m += s[k][0] + s[k][2]; }
    m *= 0.125f;
    float m2 = 0.f;
#pragma unroll
    for (int k = 0; k < 4; ++k) { const float d0 = s[k][0] - m, d1 = s[k][2] - m; m2 += s[k][1] + s[k][3] + 72.f * (d0 * d0 + d1 * d1); }
    const float r = __builtin_amdgcn_rsqf(m2 * (1.f / 576.f) + LN_EPS);
    *(f32x2*)(smem + K3_TOK + t * 8) = f32x2{r, m};
  }
  asm volatile("s_waitcnt lgkmcnt(0)" : "+v"(tk[0][0]), "+v"(tk[0][1]), "+v"(tk[0][2]), "+v"(tk[0][3]), "+v"(w[0][0]), "+v"(w[0][1]), "+v"(w[0][2]), "+v"(w[0][3]) :: "memory");
  SCHED();

#pragma clang loop unroll(disable)
  for (int j = 0; j < 18; j += 2) {
    stage(j, IC<0>{});
    if (j < 14) WAITV(5);
    else if (j == 14) {
      int tz = threadIdx.x;
      asm volatile("" : "+v"(tz));
      int l = tz & 63;
      l = wid * 21 + (l < 20 ? l : 20);
      l = l < 161 ? l : 161;
      CODE_TOUCH("k4_down", 3968u, lds0 + K3_AUX, blockIdx.x >> 3, wid);
      TOUCH(rsD, lds0 + K3_AUX, (unsigned)(blockIdx.x * 20736 + l * 128));
      WAITV(7);
    }
    if (j < 16) BAR();
    stage(j + 1, IC<1>{});
    if (j < 14) WAITV(5); else WAITV(2);
    if (j < 16) BAR();
  }

  STAMP(ws, 2, 2);
  int tz = threadIdx.x;
  asm volatile("" : "+v"(tz));
  const int ln = tz & 63, eq = ln >> 4, e16 = ln & 15;
  f32x2 st[4];
#pragma unroll
  for (int t = 0; t < 4; ++t) st[t] = *(const f32x2*)(smem + K3_TOK + (tg * 64 + t * 16 + e16) * 8);
  constexpr float NLOG2E = -1.4426950408889634f;
  auto csum = [&](int ch, int m) __attribute__((always_inline)) -> f32x4 {
    const char* p = smem + K3_CS + m * 576 + ch * 64 + eq * 16;
    return (*(const f32x4*)p + *(const f32x4*)(p + 1152)) + (*(const f32x4*)(p + 2 * 1152) + *(const f32x4*)(p + 3 * 1152));
  };
  auto actv = [&](int c, int t, f32x4 cg, f32x4 cu) __attribute__((always_inline)) -> u32x2 {
    const float r = st[t][0], mu = st[t][1];
    float o[4];
#pragma unroll
    for (int k = 0; k < 4; ++k) {
      const float g = (ag[c][t][k] - mu * cg[k]) * r;
      const float u = (au[c][t][k] - mu * cu[k]) * r;
      const float e = __builtin_amdgcn_exp2f(g * NLOG2E);
      o[k] = g * __builtin_amdgcn_rcpf(1.f + e) * u;
    }
    return u32x2{pk_bf16(o[0], o[1]), pk_bf16(o[2], o[3])};
  };
  const int fbase = ft * 144;
#pragma unroll
  for (int pr = 0; pr < 2; ++pr) {
    const int c0 = cfirst + 2 * pr;
    const int G = (fbase + c0 * 16) >> 5;
    const f32x4 cg0 = csum(c0, 0), cu0 = csum(c0, 1), cg1 = csum(c0 + 1, 0), cu1 = csum(c0 + 1, 1);
#pragma unroll
    for (int t = 0; t < 4; ++t) {
      const u32x2 lo = actv(2 * pr, t, cg0, cu0), hi = actv(2 * pr + 1, t, cg1, cu1);
      const int T16 = tt * 16 + tg * 4 + t;
      __builtin_amdgcn_raw_buffer_store_b128(u32x4{lo[0], lo[1], hi[0], hi[1]}, rsO, (unsigned)((T16 * 144 + G) * 1024 + ln * 16), 0, AUX_SC1);
    }
    SCHED();
  }
  if constexpr (NCH == 5) {
    const int G = (fbase + cL * 16) >> 5, half = odd;
    const f32x4 cg0 = csum(cL, 0), cu0 = csum(cL, 1);
#pragma unroll
    for (int t = 0; t < 4; ++t) {
      const u32x2 v = actv(4, t, cg0, cu0);
      const int T16 = tt * 16 + tg * 4 + t;
      __builtin_amdgcn_raw_buffer_store_b64(v, rsO, (unsigned)((T16 * 144 + G) * 1024 + ln * 16 + half * 8), 0, AUX_SC1);
    }
  }
  WAITV(0);
  STAMP(ws, 2, 3);
}

extern "C" __global__ __launch_bounds__(512, 2) void k3_gu(const bf16_t* wg, const bf16_t* wu, const bf16_t* wd, char* ws) {
  __shared__ __attribute__((aligned(16))) char smem[K3_LDS];
  const int wid = __builtin_amdgcn_readfirstlane(threadIdx.x >> 6), lane = threadIdx.x & 63;
  const int b = blockIdx.x, xcd = b & 7, i = b >> 3;
  const int ft = xcd * 4 + (i & 3), tt = i >> 2;
  const u32x4 rsH = mksrd(ws + WS_HT, HT_BYTES), rsG = mksrd(wg, D_ * F_ * 2), rsU = mksrd(wu, D_ * F_ * 2);
  const u32x4 rsS = mksrd(ws + WS_ST, ST_BYTES), rsC = mksrd(ws + WS_CS, CS_BYTES), rsD = mksrd(wd, F_ * D_ * 2);
  const rsrc_t rsO = MKRS(ws + WS_ACT, ACT_BYTES);
  if (wid < 4) k3_body<5>(smem, rsH, rsG, rsU, rsS, rsC, rsD, rsO, tt, ft, wid, lane, ws);
  else k3_body<4>(smem, rsH, rsG, rsU, rsS, rsC, rsD, rsO, tt, ft, wid, lane, ws);
}

constexpr int K4_SLOT = 16384 + 20480, K4_HOFF = 4 * K4_SLOT, K4_LDS = K4_HOFF + 10240;
constexpr int K4_RSTR = 272, K4_RKQ = 80 * K4_RSTR;

extern "C" __global__ __launch_bounds__(512, 2) void k4_down(const bf16_t* wd, char* ws, bf16_t* out) {
  __shared__ __attribute__((aligned(16))) char smem[K4_LDS];
  const int wid = __builtin_amdgcn_readfirstlane(threadIdx.x >> 6), lane = threadIdx.x & 63;
  const int b = blockIdx.x, xcd = b & 7, i = b >> 3;
  const int id = xcd * 29 + (xcd < 2 ? xcd : 2) + i;
  const int tt = id / 9, et = id - tt * 9;
  const u32x4 rsW = mksrd(wd, F_ * D_ * 2), rsA = mksrd(ws + WS_ACT, ACT_BYTES), rsH = mksrd(ws + WS_HT, HT_BYTES);
  const int q = lane >> 4, r16 = lane & 15, r8 = lane >> 3, c8 = lane & 7;
  const int eh = wid & 1, kq = wid >> 1;

  const unsigned vw = (unsigned)((8 * wid + r8) * 1152 + et * 128 + ((c8 ^ (((r8 >> 1) & 3) << 1)) * 16));
  unsigned va0, va1, va2;
  {
    const int o0 = wid * 1024 + lane * 16, o1 = o0 + 8192, o2 = 16384 + wid * 512 + lane * 16;
    auto src = [&](int o) { int T = tt * 5 + (o >> 12); T = T < 128 ? T : 127; return (unsigned)(T * 144 * 1024 + (o & 4095)); };
    va0 = src(o0); va1 = src(o1); va2 = src(o2);
  }
  const unsigned lds0 = lds_base(smem);
  const unsigned mA = lds0 + wid * 1024, mP = lds0 + wid * 512;
  auto issue1 = [&](int j, int k) __attribute__((always_inline)) {
    const unsigned so = (unsigned)((j & 3) * K4_SLOT), m0 = mA + so, m1 = mP + so;
    const int jj = j;
    if (k == 0) GLDS(rsW, m0, 0, vw, jj * 147456);
    if (k == 1) GLDS(rsW, m0, 8192, vw, jj * 147456 + 73728);
    if (k == 2) GLDS(rsA, m0, 16384, va0, jj * 4096);
    if (k == 3) GLDS(rsA, m0, 16384 + 8192, va1, jj * 4096);
    if (k == 4) GLDS_N(0xffffffff, rsA, m1, 16384 + 16384, va2, jj * 4096);
  };
  auto issue = [&](int j) __attribute__((always_inline)) { issue1(j, 0); issue1(j, 1); issue1(j, 2); issue1(j, 3); issue1(j, 4); };
  const int a4 = r16 >> 2, p4 = r16 & 3;
  const int rowA = 32 * kq + 4 * q + a4, key = (rowA >> 1) & 3;
  const unsigned aw0 = lds0 + (unsigned)(rowA * 128 + (((2 * eh) ^ key) * 32) + p4 * 8);
  const unsigned aw1 = lds0 + (unsigned)(rowA * 128 + (((2 * eh + 1) ^ key) * 32) + p4 * 8);
  const unsigned aa = lds0 + (unsigned)(16384 + kq * 1024 + lane * 16);

  f32x4 acc[2][5];
#pragma unroll
  for (int t = 0; t < 5; ++t) { acc[0][t] = f32x4{0, 0, 0, 0}; acc[1][t] = f32x4{0, 0, 0, 0}; }
  struct Fr { s16x4 w0l, w0h, w1l, w1h; bf16x8 tk[5]; };
  Fr fr[2];
  auto rd = [&](Fr& f, int j, int part) __attribute__((always_inline)) {
    const unsigned so = (unsigned)((j & 3) * K4_SLOT);
    if (part == 0) { const unsigned b0 = aw0 + so; DSRTR(f.w0l, b0, 0); DSRTR(f.w0h, b0, 2048); }
    if (part == 1) { const unsigned b1 = aw1 + so; DSRTR(f.w1l, b1, 0); DSRTR(f.w1h, b1, 2048); }
    if (part == 2) { const unsigned ba = aa + so; DSR128(f.tk[0], ba, 0); DSR128(f.tk[1], ba, 4096); }
    if (part == 3) { const unsigned ba = aa + so; DSR128(f.tk[2], ba, 8192); DSR128(f.tk[3], ba, 12288); DSR128(f.tk[4], ba, 16384); }
  };
  auto landed = [&](Fr& f) __attribute__((always_inline)) {
    asm volatile("s_waitcnt lgkmcnt(0)" : "+v"(f.w0l), "+v"(f.w0h), "+v"(f.w1l), "+v"(f.w1h), "+v"(f.tk[0]), "+v"(f.tk[1]), "+v"(f.tk[2]), "+v"(f.tk[3]), "+v"(f.tk[4]));
  };
  auto stage = [&](int j, auto P_, auto MODE_, auto RD_) __attribute__((always_inline)) {
    constexpr int P = decltype(P_)::value, MODE = decltype(MODE_)::value, RD = decltype(RD_)::value;
    const Fr& c = fr[P];
    Fr& nx = fr[P ^ 1];
    const bf16x8 w0 = __builtin_shufflevector(c.w0l, c.w0h, 0, 1, 2, 3, 4, 5, 6, 7), w1 = __builtin_shufflevector(c.w1l, c.w1h, 0, 1, 2, 3, 4, 5, 6, 7);
#pragma unroll
    for (int t = 0; t < 5; ++t) {
      if (MODE == 1) issue1(j + 4, t);
      if (MODE == 2 && t == 0) {
        const int o0 = wid * 1024 + lane * 16, o1 = 8192 + wid * 256 + lane * 16;
        auto src = [&](int o) { int T = tt * 5 + (o >> 11); T = T < 128 ? T : 127; return (unsigned)((T * 72 + et * 8) * 256 + (o & 2047)); };
        GLDS(rsH, mA, K4_HOFF, src(o0), 0);
        GLDS_N(0xffff, rsH, lds0 + wid * 256, K4_HOFF + 8192, src(o1), 0);
      }
      SCHED();
      acc[0][t] = MFMA16(w0, c.tk[t], acc[0][t]); acc[1][t] = MFMA16(w1, c.tk[t], acc[1][t]);
      SCHED();
      if (RD && t < 4) rd(nx, j + 1, t);
      SCHED();
    }
    if (RD) landed(nx);
  };
  using I0 = IC<0>; using I1 = IC<1>; using I2 = IC<2>;

  STAMP(ws, 3, 0);
  issue(0); issue(1); issue(2); issue(3);
  WAITV(15); BAR();
  STAMP(ws, 3, 1);
  rd(fr[0], 0, 0); rd(fr[0], 0, 1); rd(fr[0], 0, 2); rd(fr[0], 0, 3);
  landed(fr[0]);
  WAITV(10); BAR();
  for (int j = 0; j < 32; j += 2) {
    stage(j, I0{}, I1{}, I1{}); WAITV(10); BAR();
    stage(j + 1, I1{}, I1{}, I1{}); WAITV(10); BAR();
  }
  stage(32, I0{}, I2{}, I1{}); WAITV(7); BAR();
  stage(33, I1{}, I0{}, I1{}); WAITV(2); BAR();
  stage(34, I0{}, I0{}, I1{});
  stage(35, I1{}, I0{}, I0{});
  WAITV(0); BAR();
  STAMP(ws, 3, 2);
#pragma unroll
  for (int ef = 0; ef < 2; ++ef)
#pragma unroll
    for (int t = 0; t < 5; ++t)
      *(f32x4*)(smem + kq * K4_RKQ + (16 * t + r16) * K4_RSTR + (32 * eh + 16 * ef + 4 * q) * 4) = acc[ef][t];
  WAITL0(); BAR();
  const rsrc_t rsO = MKRS(out, S_ * D_ * 2);
  auto item = [&](int idx) {
    const int tok = idx >> 3, g8 = idx & 7;
    f32x4 lo = {0, 0, 0, 0}, hi = {0, 0, 0, 0};
#pragma unroll
    for (int k = 0; k < 4; ++k) {
      const char* p = smem + k * K4_RKQ + tok * K4_RSTR + g8 * 32;
      lo += *(const f32x4*)p; hi += *(const f32x4*)(p + 16);
    }
    u32x4 hv = *(const u32x4*)(smem + K4_HOFF + (tok >> 4) * 2048 + g8 * 256 + (tok & 15) * 16);
    if (g8 & 1) hv = u32x4{hv[2], hv[3], hv[0], hv[1]};
    const u32x4 ov = {pk_bf16(lo[0] + bf_lo(hv[0]), lo[1] + bf_hi(hv[0])), pk_bf16(lo[2] + bf_lo(hv[1]), lo[3] + bf_hi(hv[1])),
                      pk_bf16(hi[0] + bf_lo(hv[2]), hi[1] + bf_hi(hv[2])), pk_bf16(hi[2] + bf_lo(hv[3]), hi[3] + bf_hi(hv[3]))};
    const int tg = tt * 80 + tok;
    if (tg < S_) __builtin_amdgcn_raw_buffer_store_b128(ov, rsO, (unsigned)(tg * 1152 + et * 128 + g8 * 16), 0, 0);
  };
  item(wid * 64 + lane);
  if (wid < 2) item(512 + wid * 64 + lane);
  STAMP(ws, 3, 3);
}

extern "C" void kernel_launch(void* const* d_in, const int* in_sizes, int n_in, void* d_out, int out_size, void* d_ws, size_t ws_size,
                              hipStream_t stream) {
  const bf16_t* x = (const bf16_t*)d_in[0];
  const bf16_t* cre = (const bf16_t*)d_in[1];
  const bf16_t* cim = (const bf16_t*)d_in[2];
  const bf16_t* wre = (const bf16_t*)d_in[3];
  const bf16_t* wim = (const bf16_t*)d_in[4];
  const bf16_t* wg = (const bf16_t*)d_in[6];
  const bf16_t* wu = (const bf16_t*)d_in[7];
  const bf16_t* wd = (const bf16_t*)d_in[8];
  char* ws = (char*)d_ws;
  k1_hid<<<256, 512, 0, stream>>>(x, wre, wim, cre, cim, ws);
  k2_seq<<<256, 512, 0, stream>>>(x, cre, cim, wg, wu, ws);
  k3_gu<<<256, 512, 0, stream>>>(wg, wu, wd, ws);
  k4_down<<<234, 512, 0, stream>>>(wd, ws, (bf16_t*)d_out);
}
```

```cpp
#include <hip/hip_runtime.h>
#include <stdint.h>

typedef unsigned short bf16_t;
typedef short bf16x8 __attribute__((ext_vector_type(8)));
typedef short s16x4 __attribute__((ext_vector_type(4)));
typedef float f32x4 __attribute__((ext_vector_type(4)));
typedef float f32x2 __attribute__((ext_vector_type(2)));
typedef unsigned u32x4 __attribute__((ext_vector_type(4)));
typedef unsigned u32x2 __attribute__((ext_vector_type(2)));
typedef __bf16 bf2_t __attribute__((ext_vector_type(2)));
typedef __amdgpu_buffer_rsrc_t rsrc_t;

constexpr int S_ = 2048, D_ = 576, F_ = 4608;
constexpr float LN_EPS = 1e-5f;

constexpr unsigned WS_BP = 0;
constexpr unsigned BP_BYTES = 8u * 32u * 18432u;
constexpr unsigned WS_HT = WS_BP + BP_BYTES;
constexpr unsigned HT_BYTES = 128u * 72u * 256u;
constexpr unsigned WS_ST = WS_HT + HT_BYTES;
constexpr unsigned ST_BYTES = 2048u * 8u * 8u;
constexpr unsigned WS_P = WS_ST + ST_BYTES;
constexpr unsigned P_BYTES = 9u * HT_BYTES;

#define LDSP(p) ((__attribute__((address_space(3))) void*)(p))
#define MKRS(p, n) __builtin_amdgcn_make_buffer_rsrc((void*)(p), 0, (n), 0x00020000)
#define GLDS(rs, m0base, imm, voff, soff)                                                              \
  asm volatile("s_add_u32 m0, %0, %1\n\ts_nop 0\n\tbuffer_load_dwordx4 %2, %3, %4 offen lds"          \
               ::"s"(m0base), "i"(imm), "v"(voff), "s"(rs), "s"(soff) : "memory", "scc")
#define GLDS_N(mask, rs, m0base, imm, voff, soff)                                                      \
  asm volatile("s_add_u32 m0, %0, %1\n\ts_mov_b64 exec, " #mask "\n\tbuffer_load_dwordx4 %2, %3, %4 offen lds\n\ts_mov_b64 exec, -1" \
               ::"s"(m0base), "i"(imm), "v"(voff), "s"(rs), "s"(soff) : "memory", "scc")
#define GLDS_36(rs, m0base, imm, voff, soff)                                                           \
  asm volatile("s_add_u32 m0, %0, %1\n\ts_mov_b32 exec_hi, 0xf\n\tbuffer_load_dwordx4 %2, %3, %4 offen lds\n\ts_mov_b32 exec_hi, -1" \
               ::"s"(m0base), "i"(imm), "v"(voff), "s"(rs), "s"(soff) : "memory", "scc")
#define TOUCH(rs, mscr, voff)                                                                          \
  asm volatile("s_mov_b32 m0, %0\n\ts_nop 0\n\tbuffer_load_dword %1, %2, 0 offen lds" ::"s"(mscr), "v"(voff), "s"(rs) : "memory")
#define TOUCHS(rs, mscr, voff, soff)                                                                   \
  asm volatile("s_mov_b32 m0, %0\n\ts_nop 0\n\tbuffer_load_dword %1, %2, %3 offen lds" ::"s"(mscr), "v"(voff), "s"(rs), "s"(soff) : "memory")
__device__ __forceinline__ u32x4 mksrd(const void* p, unsigned n) {
  const unsigned long long a = (unsigned long long)p;
  return u32x4{(unsigned)a, (unsigned)(a >> 32) & 0xffffu, n, 0x00020000u};
}
#define CODE_SRD(dst, sym, n)                                                                          \
  do {                                                                                                 \
    unsigned lo_, hi_;                                                                                 \
    asm volatile("s_getpc_b64 s[20:21]\n\ts_add_u32 %0, s20, " sym "@rel32@lo+4\n\ts_addc_u32 %1, s21, " sym "@rel32@hi+12" \
                 : "=s"(lo_), "=s"(hi_)::"s20", "s21", "scc");                                         \
    dst = u32x4{lo_, hi_ & 0xffffu, (n), 0x00020000u};                                                 \
  } while (0)
#define CODE_TOUCH(sym, n, mscr, i, wid)                                                               \
  do { u32x4 rc_; CODE_SRD(rc_, sym, n); TOUCH(rc_, mscr, (unsigned)(((i) * 8 + (wid)) * 128)); } while (0)
__device__ __forceinline__ unsigned lds_base(char* smem) { return (unsigned)(size_t)LDSP(smem); }
#define GLD(dst, rs, voff) asm volatile("buffer_load_dwordx4 %0, %1, %2, 0 offen" : "=v"(dst) : "v"(voff), "s"(rs) : "memory")
#define LANDED(n, x) asm volatile("s_waitcnt vmcnt(" #n ")" : "+v"(x))
#define WAITV(n) asm volatile("s_waitcnt vmcnt(" #n ")" ::: "memory")
#define WAITL0() asm volatile("s_waitcnt lgkmcnt(0)" ::: "memory")
#define PIN(x) asm volatile("" : "+v"(x))
#define BAR() __builtin_amdgcn_s_barrier()
#define SCHED() __builtin_amdgcn_sched_barrier(0)
#define MFMA16(a, b, c) __builtin_amdgcn_mfma_f32_16x16x32_bf16((a), (b), (c), 0, 0, 0)
constexpr int AUX_SC1 = 16;
#define PRIO 1
#define PRIO_HI(wid) do { if (PRIO && (wid) >= 4) __builtin_amdgcn_s_setprio(1); } while (0)
#define PRIO_LO(wid) do { if (PRIO && (wid) >= 4) __builtin_amdgcn_s_setprio(0); } while (0)
template <int V> struct IC { static constexpr int value = V; };
#define CS_T 3
#define CT2_MODE 1
#define CT3_MODE 2
#define K2_VAR 0
#define K4_VAR 0
#define TSX(var, j) do { } while (0)
#define TS_OUT(ws, kid, wid, lane, k, var) do { } while (0)
#define STAMP(ws, kid, slot) do { } while (0)

#define WST(k) do { } while (0)
#define WST_OUT(ws, n) do { } while (0)

__device__ __forceinline__ unsigned pk_bf16(float lo, float hi) {
  f32x2 v = {lo, hi};
  bf2_t b = __builtin_convertvector(v, bf2_t);
  return __builtin_bit_cast(unsigned, b);
}
__device__ __forceinline__ float bf_lo(unsigned w) { return __uint_as_float(w << 16); }
__device__ __forceinline__ float bf_hi(unsigned w) { return __uint_as_float(w & 0xffff0000u); }
__device__ __forceinline__ float dot_ones(unsigned w, float acc) {
  const bf2_t ones = {(__bf16)1.0f, (__bf16)1.0f};
  return __builtin_amdgcn_fdot2_f32_bf16(__builtin_bit_cast(bf2_t, w), ones, acc, false);
}
__device__ __forceinline__ float dot_frag(bf16x8 f, float acc) {
  u32x4 u = __builtin_bit_cast(u32x4, f);
  acc = dot_ones(u[0], acc); acc = dot_ones(u[1], acc); acc = dot_ones(u[2], acc); acc = dot_ones(u[3], acc);
  return acc;
}

constexpr int K1_SLOT = 8192 + 18432, K1_NS = 5, K1_ST = K1_NS * K1_SLOT, K1_WS = K1_ST + 512, K1_SCR = K1_WS + 1152, K1_LDS = K1_SCR + 256;

template <int NF>
__device__ __forceinline__ void k1_body(char* smem, u32x4 rsX, u32x4 rsW, u32x4 rsC, rsrc_t rsO, int st, int et, int wid, int lane, int nf0,
                                        unsigned vhead, bool headx, char* ws) {
  STAMP(ws, 0, 0);
  const int tid = wid * 64 + lane;
  const int sh = wid >> 2, q = lane >> 4, r16 = lane & 15, r8 = lane >> 3, c8 = lane & 7;
  const unsigned vx = (unsigned)(64 * st + 8 * wid + r8) * 1152u + (unsigned)((c8 ^ ((r8 & 3) | ((wid & 1) << 2))) * 16);
  const unsigned vw = (unsigned)((et & 3) * 144 + 8 * wid + r8) * 1152u + (unsigned)((c8 ^ r8) * 16);
  const int r2 = 128 + 2 * wid + r8;
  const unsigned vw2 = (unsigned)((et & 3) * 144 + r2) * 1152u + (unsigned)((c8 ^ (r2 & 7)) * 16);
  const unsigned lds0 = lds_base(smem);
  const unsigned mA = lds0 + wid * 1024, mP = lds0 + wid * 256;
  auto issue1 = [&](int kb, int k) __attribute__((always_inline)) {
    const int so = (kb % K1_NS) * K1_SLOT;
    if (k == 0) GLDS(rsX, mA, so, vx, kb * 128);
    if (k == 1) GLDS(rsW, mA, so + 8192, vw, kb * 128);
    if (k == 2) GLDS(rsW, mA, so + 16384, vw, kb * 128 + 64 * 1152);
    if (k == 3) GLDS_N(0xffff, rsW, mP, so + 24576, vw2, kb * 128);
  };
  auto issue = [&](int kb) __attribute__((always_inline)) { issue1(kb, 0); issue1(kb, 1); issue1(kb, 2); issue1(kb, 3); };
  unsigned ax[2];
#pragma unroll
  for (int m = 0; m < 2; ++m) {
    const int row = 32 * sh + 8 * (r16 >> 2) + 4 * m + (r16 & 3);
    const int key = (row & 3) | ((row >> 1) & 4);
    ax[m] = (unsigned)(row * 128 + ((q ^ key) * 16)) ^ (unsigned)(sh * 64);
  }
  const unsigned aw = (unsigned)(8192 + (nf0 * 16 + r16) * 128 + ((q ^ (r16 & 7)) * 16)) ^ (unsigned)(sh * 64);

  f32x4 acc[2][NF];
  float wsum[NF];
#pragma unroll
  for (int n = 0; n < NF; ++n) { wsum[n] = 0.f; acc[0][n] = f32x4{0, 0, 0, 0}; acc[1][n] = f32x4{0, 0, 0, 0}; }
  float shift = 0.f;
  f32x2 s1 = {0.f, 0.f}, s2 = {0.f, 0.f};

  struct Fr { u32x4 v; bf16x8 a[2][2], b[2][NF]; };
  Fr fr[2];
  auto rd = [&](Fr& f, int kb, int part) __attribute__((always_inline)) {
    const char* sl = smem + (kb % K1_NS) * K1_SLOT;
    if (part == 0) {
      f.a[0][0] = *(const bf16x8*)(sl + ax[0]); f.a[0][1] = *(const bf16x8*)(sl + ax[1]);
#pragma unroll
      for (int n = 0; n < NF; ++n) f.b[0][n] = *(const bf16x8*)(sl + aw + n * 2048);
    } else {
      f.v = *(const u32x4*)(sl + tid * 16);
      f.a[1][0] = *(const bf16x8*)(sl + (ax[0] ^ 64)); f.a[1][1] = *(const bf16x8*)(sl + (ax[1] ^ 64));
#pragma unroll
      for (int n = 0; n < NF; ++n) f.b[1][n] = *(const bf16x8*)(sl + ((aw ^ 64) + n * 2048));
    }
  };
  CODE_TOUCH("k1_hid", 32768u, lds0 + K1_SCR, blockIdx.x >> 3, wid);
  issue(0); issue(1);
  TOUCH(headx ? rsX : rsW, lds0 + K1_SCR, vhead);
  issue(2); issue(3); issue(4);
  WAITV(17); BAR();
  STAMP(ws, 0, 1);
  rd(fr[0], 0, 0); rd(fr[0], 0, 1);
  WAITL0();
  WAITV(13); BAR();
#pragma unroll
  for (int kb = 0; kb < 9; ++kb) {
    const Fr& c = fr[kb & 1];
    Fr& nx = fr[(kb & 1) ^ 1];
    const bool stg = kb + 5 < 9;
    if (stg) issue1(kb + 5, 0);
    if (kb == 4)
      TOUCH(rsC, lds0 + K1_SCR, (unsigned)(blockIdx.x * 32768 + (wid & 3) * 8192 + lane * 128));
    SCHED();
#pragma unroll
    for (int n = 0; n < NF; ++n) { acc[0][n] = MFMA16(c.a[0][0], c.b[0][n], acc[0][n]); acc[1][n] = MFMA16(c.a[0][1], c.b[0][n], acc[1][n]); }
    SCHED();
    if (kb < 8) rd(nx, kb + 1, 0);
    SCHED();
    if (stg) issue1(kb + 5, 1);
    SCHED();
    if (kb == 0) shift = bf_lo(c.v[0]);
#pragma unroll
    for (int i = 0; i < 4; ++i) {
      f32x2 d = {bf_lo(c.v[i]) - shift, bf_hi(c.v[i]) - shift};
      s1 += d; s2 += d * d;
    }
    PIN(s1); PIN(s2);
    SCHED();
    if (stg) issue1(kb + 5, 2);
    SCHED();
#pragma unroll
    for (int n = 0; n < NF; ++n) { acc[0][n] = MFMA16(c.a[1][0], c.b[1][n], acc[0][n]); acc[1][n] = MFMA16(c.a[1][1], c.b[1][n], acc[1][n]); }
    SCHED();
    if (kb < 8) rd(nx, kb + 1, 1);
    SCHED();
#pragma unroll
    for (int n = 0; n < NF; ++n) wsum[n] = dot_frag(c.b[0][n], wsum[n]);
#pragma unroll
    for (int n = 0; n < NF; ++n) PIN(wsum[n]);
    SCHED();
    if (stg) issue1(kb + 5, 3);
    WAITL0();
    if (kb <= 3) WAITV(12); else if (kb == 4) WAITV(9); else if (kb == 5) WAITV(5); else if (kb == 6) WAITV(1);
    if (kb < 7) BAR();
    SCHED();
  }
  STAMP(ws, 0, 2);
  {
    const float a1 = s1[0] + s1[1], a2 = s2[0] + s2[1];
    float mean = shift + a1 * (1.f / 72.f);
    float m2 = a2 - a1 * a1 * (1.f / 72.f);
    float n = 72.f;
#pragma unroll
    for (int o = 1; o < 8; o <<= 1) {
      const float mo = __shfl_xor(mean, o), qo = __shfl_xor(m2, o);
      const float dl = mo - mean;
      m2 = m2 + qo + dl * dl * (n * 0.5f);
      mean = 0.5f * (mean + mo);
      n *= 2.f;
    }
    const float r = __builtin_amdgcn_rsqf(m2 * (1.f / 576.f) + LN_EPS);
    if (c8 == 0) *(f32x2*)(smem + K1_ST + (tid >> 3) * 8) = f32x2{r, mean * r};
#pragma unroll
    for (int n = 0; n < NF; ++n) {
      float w = wsum[n];
      w += __shfl_xor(w, 16); w += __shfl_xor(w, 32);
      if (q == 0) *(float*)(smem + K1_WS + sh * 576 + ((nf0 + n) * 16 + r16) * 4) = w;
    }
  }
  WAITL0();
  BAR();
  f32x2 rs[8];
  {
    const char* p = smem + K1_ST + (32 * sh + 8 * q) * 8;
#pragma unroll
    for (int i = 0; i < 4; ++i) {
      f32x4 v = *(const f32x4*)(p + i * 16);
      rs[2 * i] = f32x2{v[0], v[1]}; rs[2 * i + 1] = f32x2{v[2], v[3]};
    }
  }
  const float sgn = et < 4 ? 1.f : -1.f;
  const int kt = (et < 4 ? 0 : 16) + (st >> 1), ksg = (st & 1) * 2 + sh;
#pragma unroll
  for (int n = 0; n < NF; ++n) {
    const int el = (nf0 + n) * 16 + r16;
    const float ws = *(const float*)(smem + K1_WS + el * 4) + *(const float*)(smem + K1_WS + 576 + el * 4);
    float o[8];
#pragma unroll
    for (int m = 0; m < 2; ++m)
#pragma unroll
      for (int j = 0; j < 4; ++j) o[4 * m + j] = sgn * (acc[m][n][j] * rs[4 * m + j][0] - rs[4 * m + j][1] * ws);
    u32x4 pk = {pk_bf16(o[0], o[1]), pk_bf16(o[2], o[3]), pk_bf16(o[4], o[5]), pk_bf16(o[6], o[7])};
    const int hi = el >= 72 ? 1 : 0, e72 = el - 72 * hi;
    const int etg = (et & 3) * 2 + hi;
    const unsigned inner = e72 < 64 ? (unsigned)((e72 >> 4) * 1024 + q * 256 + (e72 & 15) * 16) : (unsigned)(4096 + q * 128 + (e72 & 7) * 16);
    const unsigned off = (unsigned)((etg * 32 + kt) * 4 + ksg) * 4608u + inner;
    __builtin_amdgcn_raw_buffer_store_b128(pk, rsO, off, 0, AUX_SC1);
  }
  WAITV(0);
  STAMP(ws, 0, 3);
}

extern "C" __global__ __launch_bounds__(512, 2) void k1_hid(const bf16_t* x, const bf16_t* wre, const bf16_t* wim, const bf16_t* cre, const bf16_t* cim, char* ws) {
  __shared__ __attribute__((aligned(16))) char smem[K1_LDS];
  const int wid = __builtin_amdgcn_readfirstlane(threadIdx.x >> 6), lane = threadIdx.x & 63;
  const int b = blockIdx.x, xcd = b & 7, i = b >> 3;
  const int st = xcd * 4 + (i & 3), et = i >> 2;
  const u32x4 rsX = mksrd(x, S_ * D_ * 2);
  const u32x4 rsW = mksrd(et < 4 ? wre : wim, D_ * D_ * 2);
  const u32x4 rsC = mksrd(wid < 4 ? cre : cim, S_ * S_ * 2);
  rsrc_t rsO = MKRS(ws + WS_BP, BP_BYTES);
  unsigned vhead;
  if (wid == 7) {
    const int l = lane < 32 ? lane : 31;
    vhead = (unsigned)((64 * st + 8 * et + (l >> 2)) * 1152 + (5 + (l & 3)) * 128);
  } else {
    int l = wid * 21 + (lane < 21 ? lane : 20);
    l = l < 144 ? l : 143;
    vhead = (unsigned)(((et & 3) * 144 + 36 * (i & 3) + (l >> 2)) * 1152 + (5 + (l & 3)) * 128);
  }
  const int w4 = wid & 3;
  if (wid == 0 || wid == 7) k1_body<3>(smem, rsX, rsW, rsC, rsO, st, et, wid, lane, wid == 0 ? 0 : 6, vhead, wid == 7, ws);
  else k1_body<2>(smem, rsX, rsW, rsC, rsO, st, et, wid, lane, wid < 4 ? 1 + 2 * w4 : 2 * w4, vhead, false, ws);
}

constexpr int K2_SLOT = 16384 + 18432, K2_XOFF = 4 * K2_SLOT, K2_SCR = K2_XOFF + 9216, K2_LDS = K2_SCR + 256;
constexpr unsigned CT2_BYTES = 23680u;
constexpr int K2_RSTR = 336, K2_RKQ = 64 * K2_RSTR, K2_SOFF = 4 * K2_RKQ;

extern "C" __global__ __launch_bounds__(512, 2) void k2_seq(const bf16_t* x, const bf16_t* cre, const bf16_t* cim, const bf16_t* wg, const bf16_t* wu, char* ws) {
  __shared__ __attribute__((aligned(16))) char smem[K2_LDS];
  const int wid = __builtin_amdgcn_readfirstlane(threadIdx.x >> 6), lane = threadIdx.x & 63;
  const int b = blockIdx.x, xcd = b & 7, i = b >> 3;
  const int mt = (xcd >> 1) * 8 + (i & 7), nt = (xcd & 1) * 4 + (i >> 3);
  const u32x4 rsRe = mksrd(cre, S_ * S_ * 2), rsIm = mksrd(cim, S_ * S_ * 2);
  const u32x4 rsB = mksrd(ws + WS_BP, BP_BYTES);
  const u32x4 rsX = mksrd(x, S_ * D_ * 2);
  const int q = lane >> 4, r16 = lane & 15, r8 = lane >> 3, c8 = lane & 7;
  const int mh = wid & 1, kq = wid >> 1;

  const unsigned va = (unsigned)(64 * mt + 8 * wid + r8) * 4096u + (unsigned)((c8 ^ r8) * 16);
  const unsigned vb = (unsigned)(wid * 1024 + lane * 16);
  const unsigned vb2 = (unsigned)(16384 + wid * 256 + lane * 16);
  const unsigned mA = lds_base(smem) + wid * 1024, mP = lds_base(smem) + wid * 256;
  auto issue1 = [&](int j, u32x4 rsA, int k) __attribute__((always_inline)) {
    const unsigned so = (unsigned)((j & 3) * K2_SLOT), m0 = mA + so, m1 = mP + so;
    const int sa = (j & 15) * 256, sb = (nt * 32 + j) * 18432;
    if (k == 0) GLDS(rsA, m0, 0, va, sa);
    if (k == 1) GLDS(rsA, m0, 8192, va, sa + 128);
    if (k == 2) GLDS(rsB, m0, 16384, vb, sb);
    if (k == 3) GLDS(rsB, m0, 16384 + 8192, vb, sb + 8192);
    if (k == 4) GLDS_N(0xffff, rsB, m1, 16384 + 16384, vb2, sb);
  };
  auto issue = [&](int j, u32x4 rsA) __attribute__((always_inline)) {
    issue1(j, rsA, 0); issue1(j, rsA, 1); issue1(j, rsA, 2); issue1(j, rsA, 3); issue1(j, rsA, 4);
  };
  const unsigned at0 = (unsigned)((kq >> 1) * 8192 + (32 * mh + r16) * 128 + ((((kq & 1) * 4 + q) ^ (r16 & 7)) * 16));
  const unsigned ae0 = (unsigned)(16384 + kq * 4608 + lane * 16);
  const unsigned aeh = (unsigned)(16384 + kq * 4608 + 4096 + q * 128 + (lane & 7) * 16);

  f32x4 acc[5][2];
#pragma unroll
  for (int f = 0; f < 5; ++f) { acc[f][0] = f32x4{0, 0, 0, 0}; acc[f][1] = f32x4{0, 0, 0, 0}; }
  struct Fr { bf16x8 t0, t1, e[5]; };
  Fr fr[2];
  auto rd = [&](Fr& f, int j, int part) __attribute__((always_inline)) {
    const char* sl = smem + (j & 3) * K2_SLOT;
    if (part == 0) { f.t0 = *(const bf16x8*)(sl + at0); f.t1 = *(const bf16x8*)(sl + at0 + 2048); }
    if (part == 1) { f.e[0] = *(const bf16x8*)(sl + ae0); f.e[1] = *(const bf16x8*)(sl + ae0 + 1024); }
    if (part == 2) { f.e[2] = *(const bf16x8*)(sl + ae0 + 2048); f.e[3] = *(const bf16x8*)(sl + ae0 + 3072); }
    if (part == 3) f.e[4] = *(const bf16x8*)(sl + aeh);
  };
  auto stage = [&](int j, auto P_, u32x4 rsA, auto MODE_, auto RD_) __attribute__((always_inline)) {
    constexpr int P = decltype(P_)::value, MODE = decltype(MODE_)::value, RD = decltype(RD_)::value;
    const Fr& c = fr[P];
    Fr& nx = fr[P ^ 1];
#pragma unroll
    for (int f = 0; f < 5; ++f) {
      if (MODE == 1) issue1(j + 4, rsA, f);
      if (MODE == 2 && f == 0) {
        const int o0 = wid * 1024 + lane * 16, o1 = 8192 + wid * 128 + lane * 16;
        const int row0 = o0 / 144, row1 = o1 / 144;
        GLDS(rsX, mA, K2_XOFF, (unsigned)((64 * mt + row0) * 1152 + nt * 144 + (o0 - row0 * 144)), 0);
        GLDS_N(0xff, rsX, lds_base(smem) + wid * 128, K2_XOFF + 8192, (unsigned)((64 * mt + row1) * 1152 + nt * 144 + (o1 - row1 * 144)), 0);
      }
      if (MODE == 2 && f == 2) {
        CODE_TOUCH("k2_seq", CT2_BYTES, lds_base(smem) + K2_SCR, i, wid);
        int n = wid * 14 + (lane < 13 ? lane : 13);
        n = i * 108 + (n < 107 ? n : 107);
        const int m = i >> 4, rem = n - m * 1728, row = rem / 9;
        TOUCH(mksrd(m ? wu : wg, D_ * F_ * 2), lds_base(smem) + K2_SCR, (unsigned)(row * 9216 + xcd * 1152 + (rem - row * 9) * 128));
      }
      SCHED();
      acc[f][0] = MFMA16(c.e[f], c.t0, acc[f][0]); acc[f][1] = MFMA16(c.e[f], c.t1, acc[f][1]);
      SCHED();
      if (RD && f < 4) rd(nx, j + 1, f);
      SCHED();
    }
    WAITL0();
  };
  using I0 = IC<0>; using I1 = IC<1>; using I2 = IC<2>;

  STAMP(ws, 1, 0);
  issue(0, rsRe); issue(1, rsRe); issue(2, rsRe); issue(3, rsRe);
  WAITV(15); BAR();
  STAMP(ws, 1, 1);
  rd(fr[0], 0, 0); rd(fr[0], 0, 1); rd(fr[0], 0, 2); rd(fr[0], 0, 3);
  WAITL0();
  WAITV(10); BAR();
  for (int j = 0; j < 12; j += 2) {
    stage(j, I0{}, rsRe, I1{}, I1{}); WAITV(10); BAR();
    stage(j + 1, I1{}, rsRe, I1{}, I1{}); WAITV(10); BAR();
  }
  for (int j = 12; j < 28; j += 2) {
    stage(j, I0{}, rsIm, I1{}, I1{}); WAITV(10); BAR();
    stage(j + 1, I1{}, rsIm, I1{}, I1{}); WAITV(10); BAR();
  }
  stage(28, I0{}, rsIm, I2{}, I1{}); WAITV(9); BAR();
  stage(29, I1{}, rsIm, I0{}, I1{}); WAITV(4); BAR();
  stage(30, I0{}, rsIm, I0{}, I1{});
  stage(31, I1{}, rsIm, I0{}, I0{});
  WAITV(2); BAR();
  STAMP(ws, 1, 2);
#pragma unroll
  for (int f = 0; f < 5; ++f)
#pragma unroll
    for (int tf = 0; tf < 2; ++tf)
      *(f32x4*)(smem + kq * K2_RKQ + (32 * mh + 16 * tf + r16) * K2_RSTR + (16 * f + 4 * q) * 4) = acc[f][tf];
  WAITL0(); BAR();
  const rsrc_t rsH = MKRS(ws + WS_HT, HT_BYTES);
  auto item = [&](int U) {
    const int t = lane;
    f32x4 lo = {0, 0, 0, 0}, hi = {0, 0, 0, 0};
#pragma unroll
    for (int k = 0; k < 4; ++k) {
      const char* p = smem + k * K2_RKQ + t * K2_RSTR + U * 32;
      lo += *(const f32x4*)p; hi += *(const f32x4*)(p + 16);
    }
    const u32x4 xv = *(const u32x4*)(smem + K2_XOFF + t * 144 + U * 16);
    u32x4 hv = {pk_bf16(lo[0] + bf_lo(xv[0]), lo[1] + bf_hi(xv[0])), pk_bf16(lo[2] + bf_lo(xv[1]), lo[3] + bf_hi(xv[1])),
                pk_bf16(hi[0] + bf_lo(xv[2]), hi[1] + bf_hi(xv[2])), pk_bf16(hi[2] + bf_lo(xv[3]), hi[3] + bf_hi(xv[3]))};
    float v[8] = {bf_lo(hv[0]), bf_hi(hv[0]), bf_lo(hv[1]), bf_hi(hv[1]), bf_lo(hv[2]), bf_hi(hv[2]), bf_lo(hv[3]), bf_hi(hv[3])};
    float m = 0.f;
#pragma unroll
    for (int k = 0; k < 8; ++k) m += v[k];
    m *= 0.125f;
    float m2 = 0.f;
#pragma unroll
    for (int k = 0; k < 8; ++k) { const float d = v[k] - m; m2 += d * d; }
    *(f32x2*)(smem + K2_SOFF + (t * 9 + U) * 8) = f32x2{m, m2};
    const int Ug = nt * 9 + U, tg = 64 * mt + t;
    __builtin_amdgcn_raw_buffer_store_b128(hv, rsH, (unsigned)(((tg >> 4) * 72 + Ug) * 256 + (tg & 15) * 16), 0, AUX_SC1);
  };
  item(wid);
  if (wid == 0) item(8);
  WAITL0(); BAR();
  if (wid == 0) {
    const char* p = smem + K2_SOFF + lane * 72;
    f32x2 s[9];
    float m = 0.f;
#pragma unroll
    for (int k = 0; k < 9; ++k) { s[k] = *(const f32x2*)(p + k * 8); m += s[k][0]; }
    m *= (1.f / 9.f);
    float m2 = 0.f;
#pragma unroll
    for (int k = 0; k < 9; ++k) { const float d = s[k][0] - m; m2 += s[k][1] + 8.f * d * d; }
    const rsrc_t rsS = MKRS(ws + WS_ST, ST_BYTES);
    __builtin_amdgcn_raw_buffer_store_b64(__builtin_bit_cast(u32x2, f32x2{m, m2}), rsS, (unsigned)(((64 * mt + lane) * 8 + nt) * 8), 0, AUX_SC1);
  }
  STAMP(ws, 1, 3);
}

constexpr int KF_RING = 92160, KF_ST = KF_RING + 65536, KF_SCR = KF_ST + 640, KF_LDS = 163840;
constexpr unsigned CT4_BYTES = 1024u;

#define DSR128(dst, addr, off) asm volatile("ds_read_b128 %0, %1 offset:%2" : "=v"(dst) : "v"(addr), "i"(off))
#define DSRTR(dst, addr, off) asm volatile("ds_read_b64_tr_b16 %0, %1 offset:%2" : "=v"(dst) : "v"(addr), "i"(off))

extern "C" __global__ __launch_bounds__(512, 2) void k3_ffn(const bf16_t* wg, const bf16_t* wu, const bf16_t* wd, char* ws) {
  __shared__ __attribute__((aligned(16))) char smem[KF_LDS];
  const int wid = __builtin_amdgcn_readfirstlane(threadIdx.x >> 6), lane = threadIdx.x & 63;
  const int b = blockIdx.x, xcd = b & 7, i = b >> 3;
  const int id = xcd * 29 + (xcd < 2 ? xcd : 2) + i;
  const int tt = id < 130 ? id / 5 : (id - 130) >> 2, gp = id < 130 ? id - tt * 5 : 5 + ((id - 130) & 3);
  const u32x4 rsG = mksrd(wg, D_ * F_ * 2), rsU = mksrd(wu, D_ * F_ * 2), rsD = mksrd(wd, F_ * D_ * 2);
  const u32x4 rsH = mksrd(ws + WS_HT, HT_BYTES), rsS = mksrd(ws + WS_ST, ST_BYTES);
  const rsrc_t rsP = MKRS(ws + WS_P, P_BYTES);
  STAMP(ws, 2, 0);
  WST(0);
  const int q = lane >> 4, r16 = lane & 15, r8 = lane >> 3, c8 = lane & 7, a4 = r16 >> 2, p4 = r16 & 3;
  const unsigned lds0 = lds_base(smem);
  const unsigned ring = lds0 + KF_RING + wid * 8192;
  const unsigned vA0 = (unsigned)(r8 * 9216 + gp * 1024 + wid * 128 + ((c8 ^ (r8 & 2)) * 16)), vA1 = vA0 ^ 64u;
  const unsigned vB = (unsigned)(r8 * 1152 + wid * 128 + ((c8 ^ (r8 & 6)) * 16));
  const unsigned vB8 = vB + (unsigned)(1024 - wid * 128);
  auto ldA = [&](u32x4 rs, int slot, int s, int j) __attribute__((always_inline)) {
    GLDS(rs, ring, slot * 4096 + j * 1024, (j & 1) ? vA1 : vA0, s * 294912 + j * 73728);
  };
  auto ldB = [&](unsigned v, int slot, int row0, int j) __attribute__((always_inline)) {
    GLDS(rsD, ring, slot * 4096 + j * 1024, v, (row0 + j * 8) * 1152);
  };
  {
    f32x4 sp;
    const int tok = (wid < 5 ? wid : 4) * 16 + (lane >> 2);
    {
      int tg = tt * 80 + tok;
      tg = tg < S_ ? tg : S_ - 1;
      GLD(sp, rsS, (unsigned)(tg * 64 + (lane & 3) * 16));
    }
    u32x4 hv[12];
    const int r0 = gp < 5 ? gp : gp - 5;
    auto rot = [&](int k) { return k + r0 < 5 ? k + r0 : k + r0 - 5; };
    auto tile = [&](int T) { T += tt * 5; return (unsigned)((T < 128 ? T : 127) * 18432 + lane * 16); };
#pragma unroll
    for (int k = 0; k < 5; ++k) { GLD(hv[2 * k], rsH, tile(rot(k)) + wid * 1024); GLD(hv[2 * k + 1], rsH, tile(rot(k)) + wid * 1024 + 8192); }
    GLD(hv[10], rsH, tile(wid >> 1) + (16 + (wid & 1)) * 1024);
    GLD(hv[11], rsH, tile(4) + (16 + (wid & 1)) * 1024);
#pragma unroll
    for (int j = 0; j < 4; ++j) ldA(rsG, 0, 0, j);
#pragma unroll
    for (int j = 0; j < 4; ++j) ldA(rsU, 1, 0, j);
    LANDED(20, sp);
    {
      float m = sp[0] + sp[2];
      m += __shfl_xor(m, 1); m += __shfl_xor(m, 2);
      m *= 0.125f;
      const float d0 = sp[0] - m, d1 = sp[2] - m;
      float m2 = sp[1] + sp[3] + 72.f * (d0 * d0 + d1 * d1);
      m2 += __shfl_xor(m2, 1); m2 += __shfl_xor(m2, 2);
      const float r = __builtin_amdgcn_rsqf(m2 * (1.f / 576.f) + LN_EPS);
      if ((lane & 3) == 0 && wid < 5) *(f32x2*)(smem + KF_ST + tok * 8) = f32x2{r, -m * r};
    }
    WAITL0(); BAR();
    WST(1);
    f32x2 st[6];
#pragma unroll
    for (int k = 0; k < 5; ++k) st[k] = *(const f32x2*)(smem + KF_ST + (rot(k) * 16 + r16) * 8);
    const f32x2 st4 = *(const f32x2*)(smem + KF_ST + (64 + r16) * 8);
    st[5] = *(const f32x2*)(smem + KF_ST + ((wid >> 1) * 16 + r16) * 8);
    auto put = [&](u32x4 v, f32x2 c, int T, int s) __attribute__((always_inline)) {
      u32x4 o;
#pragma unroll
      for (int k = 0; k < 4; ++k) o[k] = pk_bf16(__builtin_fmaf(bf_lo(v[k]), c[0], c[1]), __builtin_fmaf(bf_hi(v[k]), c[0], c[1]));
      *(u32x4*)(smem + T * 18432 + s * 1024 + lane * 16) = o;
    };
    LANDED(19, hv[0]); put(hv[0], st[0], rot(0), wid); LANDED(18, hv[1]); put(hv[1], st[0], rot(0), wid + 8);
    LANDED(17, hv[2]); put(hv[2], st[1], rot(1), wid); LANDED(16, hv[3]); put(hv[3], st[1], rot(1), wid + 8);
    LANDED(15, hv[4]); put(hv[4], st[2], rot(2), wid); LANDED(14, hv[5]); put(hv[5], st[2], rot(2), wid + 8);
    LANDED(13, hv[6]); put(hv[6], st[3], rot(3), wid); LANDED(12, hv[7]); put(hv[7], st[3], rot(3), wid + 8);
    LANDED(11, hv[8]); put(hv[8], st[4], rot(4), wid); LANDED(10, hv[9]); put(hv[9], st[4], rot(4), wid + 8);
    LANDED(9, hv[10]); put(hv[10], st[5], wid >> 1, 16 + (wid & 1));
    LANDED(8, hv[11]);
    if (wid < 2) put(hv[11], st4, 4, 16 + wid);
    WAITV(4); WAITL0(); BAR();
  }
  STAMP(ws, 2, 1);
  WST(2);

  unsigned aA[4], aB[4];
#pragma unroll
  for (int c = 0; c < 4; ++c) {
    aA[c] = ring + (unsigned)((8 * q + a4) * 128 + ((c ^ ((a4 >> 1) | ((q & 1) << 1))) * 32) + p4 * 8);
    aB[c] = ring + (unsigned)((4 * q + a4) * 128 + ((c ^ ((2 * q + (a4 >> 1)) & 3)) * 32) + p4 * 8);
  }
  unsigned at0 = lds0 + (unsigned)(lane * 16), at1 = at0 + 3 * 18432;
  s16x4 wl[2][4], wh[2][4];
  bf16x8 tk[5];
  auto rdw = [&](int buf, const unsigned(&a)[4], int hi) __attribute__((always_inline)) {
#pragma unroll
    for (int c = 0; c < 4; ++c) { DSRTR(wl[buf][c], a[c], buf * 4096); DSRTR(wh[buf][c], a[c], buf * 4096 + hi); }
  };
  auto rdt = [&](int t, int str, int off) __attribute__((always_inline)) {
    if (t < 3) DSR128(tk[t], at0, t * str + off); else DSR128(tk[t], at1, (t - 3) * str + off);
  };
#define LANDED_W(n, buf) asm volatile("s_waitcnt lgkmcnt(" #n ")" : "+v"(wl[buf][0]), "+v"(wl[buf][1]), "+v"(wl[buf][2]), "+v"(wl[buf][3]), \
    "+v"(wh[buf][0]), "+v"(wh[buf][1]), "+v"(wh[buf][2]), "+v"(wh[buf][3]), "+v"(tk[0]), "+v"(tk[1]), "+v"(tk[2]), "+v"(tk[3]))
#define LANDED_T4(n) asm volatile("s_waitcnt lgkmcnt(" #n ")" : "+v"(tk[4]))
  auto opnd = [&](int buf, int c) __attribute__((always_inline)) -> bf16x8 {
    return __builtin_shufflevector(wl[buf][c], wh[buf][c], 0, 1, 2, 3, 4, 5, 6, 7);
  };

  f32x4 ag[4][5], au[4][5];
#pragma unroll
  for (int c = 0; c < 4; ++c)
#pragma unroll
    for (int t = 0; t < 5; ++t) { ag[c][t] = f32x4{0, 0, 0, 0}; au[c][t] = f32x4{0, 0, 0, 0}; }

  const u32x4 rsT = tt < 13 ? rsG : rsU;
  unsigned vT1, vT2;
  {
    int k = wid * 3 + (lane < 2 ? lane : 2), n = (tt < 13 ? tt : tt - 13) * 20 + (k < 19 ? k : 19);
    n = n < 255 ? n : 255;
    vT1 = (unsigned)((n >> 3) * 9216 + gp * 1024 + (n & 7) * 128);
    k = wid * 4 + (lane < 3 ? lane : 3);
    vT2 = (unsigned)(gp * 589824 + (tt * 177 + (k < 29 ? k : 29)) * 128);
  }

  rdt(0, 18432, 0); rdt(1, 18432, 0); rdt(2, 18432, 0); rdt(3, 18432, 0);
  rdw(0, aA, 512);
  rdt(4, 18432, 0);
  auto kstep = [&](int s, auto TM_, auto LAST_) __attribute__((always_inline)) {
    constexpr int TM = decltype(TM_)::value;
    constexpr bool LAST = decltype(LAST_)::value;
    PRIO_HI(wid);
    LANDED_W(1, 0);
#pragma unroll
    for (int t = 0; t < 5; ++t) {
      if (t == 3) { WAITV(5); rdw(1, aA, 512); }
      if (t == 4) LANDED_T4(8);
      SCHED();
#pragma unroll
      for (int c = 0; c < 4; ++c) {
        ag[c][t] = MFMA16(opnd(0, c), tk[t], ag[c][t]);
        SCHED();
        if (t < 2 && (c & 1) == 0) {
          const int j = 2 * t + (c >> 1);
          if (LAST) ldB(vB8, 0, gp * 512 + wid * 64, j); else ldA(rsG, 0, s + 1, j);
          SCHED();
        }
        if (t == 2 && c == 0) {
          if (TM == 0) TOUCHS(rsT, lds0 + KF_SCR, vT1, (s + 6) * 294912);
          else TOUCHS(rsD, lds0 + KF_SCR, vT2, (s - 12) * 3840);
          SCHED();
        }
      }
    }
    PRIO_LO(wid);
    asm volatile("s_waitcnt lgkmcnt(0)" : "+v"(wl[1][0]), "+v"(wl[1][1]), "+v"(wl[1][2]), "+v"(wl[1][3]), "+v"(wh[1][0]), "+v"(wh[1][1]), "+v"(wh[1][2]), "+v"(wh[1][3]));
#pragma unroll
    for (int t = 0; t < 5; ++t) {
      SCHED();
#pragma unroll
      for (int c = 0; c < 4; ++c) {
        au[c][t] = MFMA16(opnd(1, c), tk[t], au[c][t]);
        SCHED();
        if (t < 2 && (c & 1) == 0) {
          const int j = 2 * t + (c >> 1);
          if (LAST) ldB(vB8, 1, gp * 512 + wid * 64 + 32, j); else ldA(rsU, 1, s + 1, j);
          SCHED();
        }
      }
      if (!LAST) rdt(t, 18432, 1024);
      if (t == 3) { WAITV(5); if (LAST) rdw(0, aB, 2048); else rdw(0, aA, 512); }
    }
    at0 += 1024; at1 += 1024;
  };
#pragma clang loop unroll(disable)
  for (int s = 0; s < 12; ++s) kstep(s, IC<0>{}, IC<0>{});
#pragma clang loop unroll(disable)
  for (int s = 12; s < 17; ++s) kstep(s, IC<1>{}, IC<0>{});
  kstep(17, IC<1>{}, IC<1>{});
  STAMP(ws, 2, 4);
  WST(3);

  constexpr float LOG2E = 1.4426950408889634f;
  auto actv = [&](int c, int t) __attribute__((always_inline)) -> u32x2 {
    unsigned o[2];
#pragma unroll
    for (int k = 0; k < 2; ++k) {
      const f32x2 g = {ag[c][t][2 * k], ag[c][t][2 * k + 1]}, u = {au[c][t][2 * k], au[c][t][2 * k + 1]};
      const f32x2 x = g * LOG2E;
      const f32x2 d = f32x2{__builtin_amdgcn_exp2f(-x[0]), __builtin_amdgcn_exp2f(-x[1])} + 1.f;
      const f32x2 r = (g * u) * f32x2{__builtin_amdgcn_rcpf(d[0]), __builtin_amdgcn_rcpf(d[1])};
      o[k] = pk_bf16(r[0], r[1]);
    }
    return u32x2{o[0], o[1]};
  };
  f32x4 o8[4][5], om[4][5];
  u32x4 act[2][5];
  const f32x4 zero4 = {0, 0, 0, 0};
  asm volatile("s_waitcnt lgkmcnt(0)" : "+v"(wl[0][0]), "+v"(wl[0][1]), "+v"(wl[0][2]), "+v"(wl[0][3]), "+v"(wh[0][0]), "+v"(wh[0][1]), "+v"(wh[0][2]), "+v"(wh[0][3]));
#pragma unroll
  for (int j = 0; j < 4; ++j) ldB(vB, 0, gp * 512, j);
  WAITV(4); rdw(1, aB, 2048);
  CODE_TOUCH("k4_sum", CT4_BYTES, lds0 + KF_SCR, 0, wid);
#pragma unroll
  for (int pr = 0; pr < 2; ++pr) {
    if (pr == 1) {
      asm volatile("s_waitcnt lgkmcnt(0)" : "+v"(wl[1][0]), "+v"(wl[1][1]), "+v"(wl[1][2]), "+v"(wl[1][3]), "+v"(wh[1][0]), "+v"(wh[1][1]), "+v"(wh[1][2]), "+v"(wh[1][3]));
#pragma unroll
      for (int j = 0; j < 4; ++j) ldB(vB, 1, gp * 512 + 32, j);
    }
#pragma unroll
    for (int t = 0; t < 5; ++t) {
      const u32x2 lo = actv(2 * pr, t), hi = actv(2 * pr + 1, t);
      act[pr][t] = u32x4{lo[0], lo[1], hi[0], hi[1]};
      const bf16x8 f = __builtin_bit_cast(bf16x8, act[pr][t]);
#pragma unroll
      for (int c = 0; c < 4; ++c) o8[c][t] = MFMA16(opnd(pr, c), f, pr == 0 ? zero4 : o8[c][t]);
      SCHED();
    }
  }
  WST(4);
  BAR();
#pragma unroll
  for (int pr = 0; pr < 2; ++pr)
#pragma unroll
    for (int t = 0; t < 5; ++t) *(u32x4*)(smem + (t * 16 + 2 * wid + pr) * 1024 + lane * 16) = act[pr][t];
#pragma unroll
  for (int c = 0; c < 4; ++c)
#pragma unroll
    for (int t = 0; t < 5; ++t) om[c][t] = f32x4{0, 0, 0, 0};
  WST(5);
  WAITL0(); BAR();
  WST(6);
  STAMP(ws, 2, 5);

  at0 = lds0 + (unsigned)(lane * 16); at1 = at0 + 3 * 16384;
  rdt(0, 16384, 0); rdt(1, 16384, 0); rdt(2, 16384, 0); rdt(3, 16384, 0);
  WAITV(4);
  rdw(0, aB, 2048);
  rdt(4, 16384, 0);
  auto down = [&](int s, auto P_, auto LD_, auto NX_) __attribute__((always_inline)) {
    constexpr int P = decltype(P_)::value;
    constexpr bool LD = decltype(LD_)::value, NX = decltype(NX_)::value;
    if (P == 0) PRIO_HI(wid); else PRIO_LO(wid);
    LANDED_W(1, P);
#pragma unroll
    for (int t = 0; t < 5; ++t) {
      if (t == 4) { if (NX) LANDED_T4(12); else LANDED_T4(0); }
      SCHED();
#pragma unroll
      for (int c = 0; c < 4; ++c) {
        om[c][t] = MFMA16(opnd(P, c), tk[t], om[c][t]);
        SCHED();
        if (LD && t < 2 && (c & 1) == 0) { ldB(vB, P, gp * 512 + (s + 2) * 32, 2 * t + (c >> 1)); SCHED(); }
      }
      if (NX) {
        rdt(t, 16384, 1024);
        if (t == 3) { if (LD) WAITV(4); else WAITV(0); rdw(P ^ 1, aB, 2048); }
      }
    }
    at0 += 1024; at1 += 1024;
  };
#pragma clang loop unroll(disable)
  for (int s = 0; s < 14; s += 2) { down(s, IC<0>{}, IC<1>{}, IC<1>{}); down(s + 1, IC<1>{}, IC<1>{}, IC<1>{}); }
  down(14, IC<0>{}, IC<0>{}, IC<1>{});
  down(15, IC<1>{}, IC<0>{}, IC<0>{});
  WST(7);
  STAMP(ws, 2, 2);

  const int uo = (q & 1) * 2 + (q >> 1);
  auto emit = [&](f32x4 lo, f32x4 hi, int t, int E) __attribute__((always_inline)) {
    const auto x = __builtin_amdgcn_permlane16_swap(pk_bf16(lo[0], lo[1]), pk_bf16(hi[0], hi[1]), false, false);
    const auto y = __builtin_amdgcn_permlane16_swap(pk_bf16(lo[2], lo[3]), pk_bf16(hi[2], hi[3]), false, false);
    const int T16 = tt * 5 + t;
    if (T16 < 128)
      __builtin_amdgcn_raw_buffer_store_b128(u32x4{x[0], y[0], x[1], y[1]}, rsP, (unsigned)(((gp * 128 + T16) * 72 + E * 4 + uo) * 256 + r16 * 16), 0, AUX_SC1);
  };
#pragma unroll
  for (int t = 0; t < 5; ++t) { emit(om[0][t], om[1][t], t, 2 * wid); emit(om[2][t], om[3][t], t, 2 * wid + 1); }
  SCHED();
  BAR();
#pragma unroll
  for (int t = 0; t < 5; ++t)
#pragma unroll
    for (int c = 0; c < 4; ++c) *(f32x4*)(smem + (wid * 20 + t * 4 + c) * 1024 + lane * 16) = o8[c][t];
  WST(8);
  WAITL0(); BAR();
  WST(9);
  auto unit = [&](int u) __attribute__((always_inline)) {
    const char* p = smem + u * 2048 + lane * 16;
    f32x4 lo = {0, 0, 0, 0}, hi = {0, 0, 0, 0};
#pragma unroll
    for (int k = 0; k < 8; ++k) { lo += *(const f32x4*)(p + k * 20480); hi += *(const f32x4*)(p + k * 20480 + 1024); }
    emit(lo, hi, u >> 1, 16 + (u & 1));
  };
  unit(wid);
  if (wid < 2) unit(8 + wid);
  WST(10);
  WAITV(0);
  WST(11);
  WST_OUT(ws, 12);
  STAMP(ws, 2, 3);
}

extern "C" __global__ __launch_bounds__(576) void k4_sum(char* ws, bf16_t* out) {
  STAMP(ws, 3, 0);
  const int wv = threadIdx.x >> 6, lane = threadIdx.x & 63;
  const int T16 = blockIdx.x >> 1, t = (blockIdx.x & 1) * 8 + (lane >> 3), U = wv * 8 + (lane & 7);
  const rsrc_t rsP = MKRS(ws + WS_P, P_BYTES), rsH = MKRS(ws + WS_HT, HT_BYTES), rsO = MKRS(out, S_ * D_ * 2);
  const unsigned off = (unsigned)((T16 * 72 + U) * 256 + t * 16);
  u32x4 p[9];
#pragma unroll
  for (int g = 0; g < 9; ++g) p[g] = __builtin_amdgcn_raw_buffer_load_b128(rsP, off, g * (int)HT_BYTES, 0);
  u32x4 hv = __builtin_amdgcn_raw_buffer_load_b128(rsH, off, 0, 0);
  STAMP(ws, 3, 1);
  float a[8];
#pragma unroll
  for (int k = 0; k < 4; ++k) { a[2 * k] = bf_lo(p[0][k]); a[2 * k + 1] = bf_hi(p[0][k]); }
#pragma unroll
  for (int g = 1; g < 9; ++g)
#pragma unroll
    for (int k = 0; k < 4; ++k) { a[2 * k] += bf_lo(p[g][k]); a[2 * k + 1] += bf_hi(p[g][k]); }
  u32x4 ov;
#pragma unroll
  for (int k = 0; k < 4; ++k) ov[k] = pk_bf16(a[2 * k] + bf_lo(hv[k]), a[2 * k + 1] + bf_hi(hv[k]));
  STAMP(ws, 3, 2);
  __builtin_amdgcn_raw_buffer_store_b128(ov, rsO, (unsigned)((T16 * 16 + t) * 1152 + U * 16), 0, 0);
  STAMP(ws, 3, 3);
}

extern "C" void kernel_launch(void* const* d_in, const int* in_sizes, int n_in, void* d_out, int out_size, void* d_ws, size_t ws_size,
                              hipStream_t stream) {
  const bf16_t* x = (const bf16_t*)d_in[0];
  const bf16_t* cre = (const bf16_t*)d_in[1];
  const bf16_t* cim = (const bf16_t*)d_in[2];
  const bf16_t* wre = (const bf16_t*)d_in[3];
  const bf16_t* wim = (const bf16_t*)d_in[4];
  const bf16_t* wg = (const bf16_t*)d_in[6];
  const bf16_t* wu = (const bf16_t*)d_in[7];
  const bf16_t* wd = (const bf16_t*)d_in[8];
  char* ws = (char*)d_ws;
  k1_hid<<<256, 512, 0, stream>>>(x, wre, wim, cre, cim, ws);
  k2_seq<<<256, 512, 0, stream>>>(x, cre, cim, wg, wu, ws);
  k3_ffn<<<234, 512, 0, stream>>>(wg, wu, wd, ws);
  k4_sum<<<256, 576, 0, stream>>>(ws, (bf16_t*)d_out);
}
```

```cpp
#define DIAG
#define QSPIN 0
#define NMAIN 234
#include <hip/hip_runtime.h>
#include <stdint.h>

typedef unsigned short bf16_t;
typedef short bf16x8 __attribute__((ext_vector_type(8)));
typedef short s16x4 __attribute__((ext_vector_type(4)));
typedef float f32x4 __attribute__((ext_vector_type(4)));
typedef float f32x2 __attribute__((ext_vector_type(2)));
typedef unsigned u32x4 __attribute__((ext_vector_type(4)));
typedef unsigned u32x2 __attribute__((ext_vector_type(2)));
typedef __bf16 bf2_t __attribute__((ext_vector_type(2)));
typedef __amdgpu_buffer_rsrc_t rsrc_t;

constexpr int S_ = 2048, D_ = 576, F_ = 4608;
constexpr float LN_EPS = 1e-5f;

constexpr unsigned WS_BP = 0;
constexpr unsigned BP_BYTES = 8u * 32u * 18432u;
constexpr unsigned WS_HT = WS_BP + BP_BYTES;
constexpr unsigned HT_BYTES = 128u * 72u * 256u;
constexpr unsigned WS_ST = WS_HT + HT_BYTES;
constexpr unsigned ST_BYTES = 2048u * 8u * 8u;
constexpr unsigned WS_P = WS_ST + ST_BYTES;
constexpr unsigned P_BYTES = 9u * HT_BYTES;

#define LDSP(p) ((__attribute__((address_space(3))) void*)(p))
#define MKRS(p, n) __builtin_amdgcn_make_buffer_rsrc((void*)(p), 0, (n), 0x00020000)
#define GLDS(rs, m0base, imm, voff, soff)                                                              \
  asm volatile("s_add_u32 m0, %0, %1\n\ts_nop 0\n\tbuffer_load_dwordx4 %2, %3, %4 offen lds"          \
               ::"s"(m0base), "i"(imm), "v"(voff), "s"(rs), "s"(soff) : "memory", "scc")
#define GLDS_N(mask, rs, m0base, imm, voff, soff)                                                      \
  asm volatile("s_add_u32 m0, %0, %1\n\ts_mov_b64 exec, " #mask "\n\tbuffer_load_dwordx4 %2, %3, %4 offen lds\n\ts_mov_b64 exec, -1" \
               ::"s"(m0base), "i"(imm), "v"(voff), "s"(rs), "s"(soff) : "memory", "scc")
#define GLDS_36(rs, m0base, imm, voff, soff)                                                           \
  asm volatile("s_add_u32 m0, %0, %1\n\ts_mov_b32 exec_hi, 0xf\n\tbuffer_load_dwordx4 %2, %3, %4 offen lds\n\ts_mov_b32 exec_hi, -1" \
               ::"s"(m0base), "i"(imm), "v"(voff), "s"(rs), "s"(soff) : "memory", "scc")
#define RING_M0(m0base, imm) asm volatile("s_add_u32 m0, %0, %1" ::"s"(m0base), "i"(imm) : "scc")
#define RING_LD(rs, off, voff, soff) asm volatile("buffer_load_dwordx4 %0, %1, %2 offen offset:%3 lds" ::"v"(voff), "s"(rs), "s"(soff), "i"(off) : "memory")
#define TOUCH(rs, mscr, voff)                                                                          \
  asm volatile("s_mov_b32 m0, %0\n\ts_nop 0\n\tbuffer_load_dword %1, %2, 0 offen lds" ::"s"(mscr), "v"(voff), "s"(rs) : "memory")
#define TOUCHS(rs, mscr, voff, soff)                                                                   \
  asm volatile("s_mov_b32 m0, %0\n\ts_nop 0\n\tbuffer_load_dword %1, %2, %3 offen lds" ::"s"(mscr), "v"(voff), "s"(rs), "s"(soff) : "memory")
__device__ __forceinline__ u32x4 mksrd(const void* p, unsigned n) {
  const unsigned long long a = (unsigned long long)p;
  return u32x4{(unsigned)a, (unsigned)(a >> 32) & 0xffffu, n, 0x00020000u};
}
#define CODE_ADDR(lo, hi, sym)                                                                         \
  asm volatile("s_getpc_b64 s[20:21]\n\ts_add_u32 %0, s20, " sym "@rel32@lo+4\n\ts_addc_u32 %1, s21, " sym "@rel32@hi+12" \
               : "=s"(lo), "=s"(hi)::"s20", "s21", "scc")
#define CODE_TOUCH(sym, mscr, i, wid)                                                                  \
  do {                                                                                                 \
    unsigned lo_, hi_, le_, he_;                                                                       \
    CODE_ADDR(lo_, hi_, sym); CODE_ADDR(le_, he_, "k4_sum");                                           \
    const unsigned n_ = le_ - lo_ + 1024u;                                                             \
    TOUCH((u32x4{lo_, hi_ & 0xffffu, n_ < 65536u ? n_ : 0u, 0x00020000u}), mscr, (unsigned)(((i) * 8 + (wid)) * 128)); \
  } while (0)
__device__ __forceinline__ unsigned lds_base(char* smem) { return (unsigned)(size_t)LDSP(smem); }
#define GLD(dst, rs, voff) asm volatile("buffer_load_dwordx4 %0, %1, %2, 0 offen" : "=v"(dst) : "v"(voff), "s"(rs) : "memory")
#define LANDED(n, x) asm volatile("s_waitcnt vmcnt(" #n ")" : "+v"(x))
#define WAITV(n) asm volatile("s_waitcnt vmcnt(" #n ")" ::: "memory")
#define WAITL0() asm volatile("s_waitcnt lgkmcnt(0)" ::: "memory")
#define PIN(x) asm volatile("" : "+v"(x))
#define BAR() __builtin_amdgcn_s_barrier()
#define SCHED() __builtin_amdgcn_sched_barrier(0)
#define MFMA16(a, b, c) __builtin_amdgcn_mfma_f32_16x16x32_bf16((a), (b), (c), 0, 0, 0)
constexpr int AUX_SC1 = 16;
#define PRIO 1
#define PA_T 2
#define PB_T 2
#define PRIO_SET(wid, p) do { if (PRIO) asm volatile("s_cmp_lt_u32 %0, 4\n\ts_cbranch_scc1 1f\n\ts_setprio " #p "\n1:" ::"s"(wid) : "scc"); } while (0)
#define PRIO_HI(wid) PRIO_SET(wid, 1)
#define PRIO_LO(wid) PRIO_SET(wid, 0)
template <int V> struct IC { static constexpr int value = V; };
#define CS_T 3
#define CT2_MODE 1
#define CT3_MODE 2
#define K2_VAR 0
#define K4_VAR 0
#define TSX(var, j) do { } while (0)
#define TS_OUT(ws, kid, wid, lane, k, var) do { } while (0)
#define STAMP(ws, kid, slot) do { if (threadIdx.x == 0) { unsigned long long* p_ = (unsigned long long*)((ws) + (64u << 20) + (kid) * 65536 + blockIdx.x * 128 + (slot) * 16); \
  p_[0] = __builtin_amdgcn_s_memtime(); p_[1] = __builtin_amdgcn_s_memrealtime(); } } while (0)

#define WST(k) do { } while (0)
#define WST_OUT(ws, n) do { } while (0)

#define TS4(j) do { } while (0)
#define TS4_OUT(ws) do { } while (0)

__device__ __forceinline__ unsigned pk_bf16(float lo, float hi) {
  f32x2 v = {lo, hi};
  bf2_t b = __builtin_convertvector(v, bf2_t);
  return __builtin_bit_cast(unsigned, b);
}
__device__ __forceinline__ float bf_lo(unsigned w) { return __uint_as_float(w << 16); }
__device__ __forceinline__ float bf_hi(unsigned w) { return __uint_as_float(w & 0xffff0000u); }
__device__ __forceinline__ float dot_ones(unsigned w, float acc) {
  const bf2_t ones = {(__bf16)1.0f, (__bf16)1.0f};
  return __builtin_amdgcn_fdot2_f32_bf16(__builtin_bit_cast(bf2_t, w), ones, acc, false);
}
__device__ __forceinline__ float dot_frag(bf16x8 f, float acc) {
  u32x4 u = __builtin_bit_cast(u32x4, f);
  acc = dot_ones(u[0], acc); acc = dot_ones(u[1], acc); acc = dot_ones(u[2], acc); acc = dot_ones(u[3], acc);
  return acc;
}

constexpr int K1_SLOT = 8192 + 18432, K1_NS = 5, K1_ST = K1_NS * K1_SLOT, K1_WS = K1_ST + 512, K1_SCR = K1_WS + 1152, K1_LDS = K1_SCR + 256;

template <int NF>
__device__ __forceinline__ void k1_body(char* smem, u32x4 rsX, u32x4 rsW, u32x4 rsC, rsrc_t rsO, int st, int et, int wid, int lane, int nf0,
                                        unsigned vhead, bool headx, char* ws) {
  STAMP(ws, 0, 0);
  const int tid = wid * 64 + lane;
  const int sh = wid >> 2, q = lane >> 4, r16 = lane & 15, r8 = lane >> 3, c8 = lane & 7;
  const unsigned vx = (unsigned)(64 * st + 8 * wid + r8) * 1152u + (unsigned)((c8 ^ ((r8 & 3) | ((wid & 1) << 2))) * 16);
  const unsigned vw = (unsigned)((et & 3) * 144 + 8 * wid + r8) * 1152u + (unsigned)((c8 ^ r8) * 16);
  const int r2 = 128 + 2 * wid + r8;
  const unsigned vw2 = (unsigned)((et & 3) * 144 + r2) * 1152u + (unsigned)((c8 ^ (r2 & 7)) * 16);
  const unsigned lds0 = lds_base(smem);
  const unsigned mA = lds0 + wid * 1024, mP = lds0 + wid * 256;
  auto issue1 = [&](int kb, int k) __attribute__((always_inline)) {
    const int so = (kb % K1_NS) * K1_SLOT;
    if (k == 0) GLDS(rsX, mA, so, vx, kb * 128);
    if (k == 1) GLDS(rsW, mA, so + 8192, vw, kb * 128);
    if (k == 2) GLDS(rsW, mA, so + 16384, vw, kb * 128 + 64 * 1152);
    if (k == 3) GLDS_N(0xffff, rsW, mP, so + 24576, vw2, kb * 128);
  };
  auto issue = [&](int kb) __attribute__((always_inline)) { issue1(kb, 0); issue1(kb, 1); issue1(kb, 2); issue1(kb, 3); };
  unsigned ax[2];
#pragma unroll
  for (int m = 0; m < 2; ++m) {
    const int row = 32 * sh + 8 * (r16 >> 2) + 4 * m + (r16 & 3);
    const int key = (row & 3) | ((row >> 1) & 4);
    ax[m] = (unsigned)(row * 128 + ((q ^ key) * 16)) ^ (unsigned)(sh * 64);
  }
  const unsigned aw = (unsigned)(8192 + (nf0 * 16 + r16) * 128 + ((q ^ (r16 & 7)) * 16)) ^ (unsigned)(sh * 64);

  f32x4 acc[2][NF];
  float wsum[NF];
#pragma unroll
  for (int n = 0; n < NF; ++n) { wsum[n] = 0.f; acc[0][n] = f32x4{0, 0, 0, 0}; acc[1][n] = f32x4{0, 0, 0, 0}; }
  float shift = 0.f;
  f32x2 s1 = {0.f, 0.f}, s2 = {0.f, 0.f};

  struct Fr { u32x4 v; bf16x8 a[2][2], b[2][NF]; };
  Fr fr[2];
  auto rd = [&](Fr& f, int kb, int part) __attribute__((always_inline)) {
    const char* sl = smem + (kb % K1_NS) * K1_SLOT;
    if (part == 0) {
      f.a[0][0] = *(const bf16x8*)(sl + ax[0]); f.a[0][1] = *(const bf16x8*)(sl + ax[1]);
#pragma unroll
      for (int n = 0; n < NF; ++n) f.b[0][n] = *(const bf16x8*)(sl + aw + n * 2048);
    } else {
      f.v = *(const u32x4*)(sl + tid * 16);
      f.a[1][0] = *(const bf16x8*)(sl + (ax[0] ^ 64)); f.a[1][1] = *(const bf16x8*)(sl + (ax[1] ^ 64));
#pragma unroll
      for (int n = 0; n < NF; ++n) f.b[1][n] = *(const bf16x8*)(sl + ((aw ^ 64) + n * 2048));
    }
  };
  CODE_TOUCH("k1_hid", lds0 + K1_SCR, blockIdx.x >> 3, wid);
  issue(0); issue(1);
  TOUCH(headx ? rsX : rsW, lds0 + K1_SCR, vhead);
  issue(2); issue(3); issue(4);
  WAITV(17); BAR();
  STAMP(ws, 0, 1);
  rd(fr[0], 0, 0); rd(fr[0], 0, 1);
  WAITL0();
  WAITV(13); BAR();
#pragma unroll
  for (int kb = 0; kb < 9; ++kb) {
    const Fr& c = fr[kb & 1];
    Fr& nx = fr[(kb & 1) ^ 1];
    const bool stg = kb + 5 < 9;
    if (stg) issue1(kb + 5, 0);
    if (kb == 4)
      TOUCH(rsC, lds0 + K1_SCR, (unsigned)(blockIdx.x * 32768 + (wid & 3) * 8192 + lane * 128));
    SCHED();
#pragma unroll
    for (int n = 0; n < NF; ++n) { acc[0][n] = MFMA16(c.a[0][0], c.b[0][n], acc[0][n]); acc[1][n] = MFMA16(c.a[0][1], c.b[0][n], acc[1][n]); }
    SCHED();
    if (kb < 8) rd(nx, kb + 1, 0);
    SCHED();
    if (stg) issue1(kb + 5, 1);
    SCHED();
    if (kb == 0) shift = bf_lo(c.v[0]);
#pragma unroll
    for (int i = 0; i < 4; ++i) {
      f32x2 d = {bf_lo(c.v[i]) - shift, bf_hi(c.v[i]) - shift};
      s1 += d; s2 += d * d;
    }
    PIN(s1); PIN(s2);
    SCHED();
    if (stg) issue1(kb + 5, 2);
    SCHED();
#pragma unroll
    for (int n = 0; n < NF; ++n) { acc[0][n] = MFMA16(c.a[1][0], c.b[1][n], acc[0][n]); acc[1][n] = MFMA16(c.a[1][1], c.b[1][n], acc[1][n]); }
    SCHED();
    if (kb < 8) rd(nx, kb + 1, 1);
    SCHED();
#pragma unroll
    for (int n = 0; n < NF; ++n) wsum[n] = dot_frag(c.b[0][n], wsum[n]);
#pragma unroll
    for (int n = 0; n < NF; ++n) PIN(wsum[n]);
    SCHED();
    if (stg) issue1(kb + 5, 3);
    WAITL0();
    if (kb <= 3) WAITV(12); else if (kb == 4) WAITV(9); else if (kb == 5) WAITV(5); else if (kb == 6) WAITV(1);
    if (kb < 7) BAR();
    SCHED();
  }
  STAMP(ws, 0, 2);
  {
    const float a1 = s1[0] + s1[1], a2 = s2[0] + s2[1];
    float mean = shift + a1 * (1.f / 72.f);
    float m2 = a2 - a1 * a1 * (1.f / 72.f);
    float n = 72.f;
#pragma unroll
    for (int o = 1; o < 8; o <<= 1) {
      const float mo = __shfl_xor(mean, o), qo = __shfl_xor(m2, o);
      const float dl = mo - mean;
      m2 = m2 + qo + dl * dl * (n * 0.5f);
      mean = 0.5f * (mean + mo);
      n *= 2.f;
    }
    const float r = __builtin_amdgcn_rsqf(m2 * (1.f / 576.f) + LN_EPS);
    if (c8 == 0) *(f32x2*)(smem + K1_ST + (tid >> 3) * 8) = f32x2{r, mean * r};
#pragma unroll
    for (int n = 0; n < NF; ++n) {
      float w = wsum[n];
      w += __shfl_xor(w, 16); w += __shfl_xor(w, 32);
      if (q == 0) *(float*)(smem + K1_WS + sh * 576 + ((nf0 + n) * 16 + r16) * 4) = w;
    }
  }
  WAITL0();
  BAR();
  f32x2 rs[8];
  {
    const char* p = smem + K1_ST + (32 * sh + 8 * q) * 8;
#pragma unroll
    for (int i = 0; i < 4; ++i) {
      f32x4 v = *(const f32x4*)(p + i * 16);
      rs[2 * i] = f32x2{v[0], v[1]}; rs[2 * i + 1] = f32x2{v[2], v[3]};
    }
  }
  const float sgn = et < 4 ? 1.f : -1.f;
  const int kt = (et < 4 ? 0 : 16) + (st >> 1), ksg = (st & 1) * 2 + sh;
#pragma unroll
  for (int n = 0; n < NF; ++n) {
    const int el = (nf0 + n) * 16 + r16;
    const float ws = *(const float*)(smem + K1_WS + el * 4) + *(const float*)(smem + K1_WS + 576 + el * 4);
    float o[8];
#pragma unroll
    for (int m = 0; m < 2; ++m)
#pragma unroll
      for (int j = 0; j < 4; ++j) o[4 * m + j] = sgn * (acc[m][n][j] * rs[4 * m + j][0] - rs[4 * m + j][1] * ws);
    u32x4 pk = {pk_bf16(o[0], o[1]), pk_bf16(o[2], o[3]), pk_bf16(o[4], o[5]), pk_bf16(o[6], o[7])};
    const int hi = el >= 72 ? 1 : 0, e72 = el - 72 * hi;
    const int etg = (et & 3) * 2 + hi;
    const unsigned inner = e72 < 64 ? (unsigned)((e72 >> 4) * 1024 + q * 256 + (e72 & 15) * 16) : (unsigned)(4096 + q * 128 + (e72 & 7) * 16);
    const unsigned off = (unsigned)((etg * 32 + kt) * 4 + ksg) * 4608u + inner;
    __builtin_amdgcn_raw_buffer_store_b128(pk, rsO, off, 0, AUX_SC1);
  }
  WAITV(0);
  STAMP(ws, 0, 3);
}

extern "C" __global__ __launch_bounds__(512, 2) void k1_hid(const bf16_t* x, const bf16_t* wre, const bf16_t* wim, const bf16_t* cre, const bf16_t* cim, char* ws) {
  __shared__ __attribute__((aligned(16))) char smem[K1_LDS];
  const int wid = __builtin_amdgcn_readfirstlane(threadIdx.x >> 6), lane = threadIdx.x & 63;
  const int b = blockIdx.x, xcd = b & 7, i = b >> 3;
  const int st = xcd * 4 + (i & 3), et = i >> 2;
  const u32x4 rsX = mksrd(x, S_ * D_ * 2);
  const u32x4 rsW = mksrd(et < 4 ? wre : wim, D_ * D_ * 2);
  const u32x4 rsC = mksrd(wid < 4 ? cre : cim, S_ * S_ * 2);
  rsrc_t rsO = MKRS(ws + WS_BP, BP_BYTES);
  unsigned vhead;
  if (wid == 7) {
    const int l = lane < 32 ? lane : 31;
    vhead = (unsigned)((64 * st + 8 * et + (l >> 2)) * 1152 + (5 + (l & 3)) * 128);
  } else {
    int l = wid * 21 + (lane < 21 ? lane : 20);
    l = l < 144 ? l : 143;
    vhead = (unsigned)(((et & 3) * 144 + 36 * (i & 3) + (l >> 2)) * 1152 + (5 + (l & 3)) * 128);
  }
  const int w4 = wid & 3;
  if (wid == 0 || wid == 7) k1_body<3>(smem, rsX, rsW, rsC, rsO, st, et, wid, lane, wid == 0 ? 0 : 6, vhead, wid == 7, ws);
  else k1_body<2>(smem, rsX, rsW, rsC, rsO, st, et, wid, lane, wid < 4 ? 1 + 2 * w4 : 2 * w4, vhead, false, ws);
}

constexpr int K2_SLOT = 16384 + 18432, K2_XOFF = 4 * K2_SLOT, K2_SCR = K2_XOFF + 9216, K2_LDS = K2_SCR + 256;
constexpr int K2_RSTR = 336, K2_RKQ = 64 * K2_RSTR, K2_SOFF = 4 * K2_RKQ;

extern "C" __global__ __launch_bounds__(512, 2) void k2_seq(const bf16_t* x, const bf16_t* cre, const bf16_t* cim, const bf16_t* wg, const bf16_t* wu, char* ws) {
  __shared__ __attribute__((aligned(16))) char smem[K2_LDS];
  const int wid = __builtin_amdgcn_readfirstlane(threadIdx.x >> 6), lane = threadIdx.x & 63;
  const int b = blockIdx.x, xcd = b & 7, i = b >> 3;
  const int mt = (xcd >> 1) * 8 + (i & 7), nt = (xcd & 1) * 4 + (i >> 3);
  const u32x4 rsRe = mksrd(cre, S_ * S_ * 2), rsIm = mksrd(cim, S_ * S_ * 2);
  const u32x4 rsB = mksrd(ws + WS_BP, BP_BYTES);
  const u32x4 rsX = mksrd(x, S_ * D_ * 2);
  const int q = lane >> 4, r16 = lane & 15, r8 = lane >> 3, c8 = lane & 7;
  const int mh = wid & 1, kq = wid >> 1;

  const unsigned va = (unsigned)(64 * mt + 8 * wid + r8) * 4096u + (unsigned)((c8 ^ r8) * 16);
  const unsigned vb = (unsigned)(wid * 1024 + lane * 16);
  const unsigned vb2 = (unsigned)(16384 + wid * 256 + lane * 16);
  const unsigned mA = lds_base(smem) + wid * 1024, mP = lds_base(smem) + wid * 256;
  auto issue1 = [&](int j, u32x4 rsA, int k) __attribute__((always_inline)) {
    const unsigned so = (unsigned)((j & 3) * K2_SLOT), m0 = mA + so, m1 = mP + so;
    const int sa = (j & 15) * 256, sb = (nt * 32 + j) * 18432;
    if (k == 0) GLDS(rsA, m0, 0, va, sa);
    if (k == 1) GLDS(rsA, m0, 8192, va, sa + 128);
    if (k == 2) GLDS(rsB, m0, 16384, vb, sb);
    if (k == 3) GLDS(rsB, m0, 16384 + 8192, vb, sb + 8192);
    if (k == 4) GLDS_N(0xffff, rsB, m1, 16384 + 16384, vb2, sb);
  };
  auto issue = [&](int j, u32x4 rsA) __attribute__((always_inline)) {
    issue1(j, rsA, 0); issue1(j, rsA, 1); issue1(j, rsA, 2); issue1(j, rsA, 3); issue1(j, rsA, 4);
  };
  const unsigned at0 = (unsigned)((kq >> 1) * 8192 + (32 * mh + r16) * 128 + ((((kq & 1) * 4 + q) ^ (r16 & 7)) * 16));
  const unsigned ae0 = (unsigned)(16384 + kq * 4608 + lane * 16);
  const unsigned aeh = (unsigned)(16384 + kq * 4608 + 4096 + q * 128 + (lane & 7) * 16);

  f32x4 acc[5][2];
#pragma unroll
  for (int f = 0; f < 5; ++f) { acc[f][0] = f32x4{0, 0, 0, 0}; acc[f][1] = f32x4{0, 0, 0, 0}; }
  struct Fr { bf16x8 t0, t1, e[5]; };
  Fr fr[2];
  auto rd = [&](Fr& f, int j, int part) __attribute__((always_inline)) {
    const char* sl = smem + (j & 3) * K2_SLOT;
    if (part == 0) { f.t0 = *(const bf16x8*)(sl + at0); f.t1 = *(const bf16x8*)(sl + at0 + 2048); }
    if (part == 1) { f.e[0] = *(const bf16x8*)(sl + ae0); f.e[1] = *(const bf16x8*)(sl + ae0 + 1024); }
    if (part == 2) { f.e[2] = *(const bf16x8*)(sl + ae0 + 2048); f.e[3] = *(const bf16x8*)(sl + ae0 + 3072); }
    if (part == 3) f.e[4] = *(const bf16x8*)(sl + aeh);
  };
  auto stage = [&](int j, auto P_, u32x4 rsA, auto MODE_, auto RD_) __attribute__((always_inline)) {
    constexpr int P = decltype(P_)::value, MODE = decltype(MODE_)::value, RD = decltype(RD_)::value;
    const Fr& c = fr[P];
    Fr& nx = fr[P ^ 1];
#pragma unroll
    for (int f = 0; f < 5; ++f) {
      if (MODE == 1) issue1(j + 4, rsA, f);
      if (MODE == 2 && f == 0) {
        const int o0 = wid * 1024 + lane * 16, o1 = 8192 + wid * 128 + lane * 16;
        const int row0 = o0 / 144, row1 = o1 / 144;
        GLDS(rsX, mA, K2_XOFF, (unsigned)((64 * mt + row0) * 1152 + nt * 144 + (o0 - row0 * 144)), 0);
        GLDS_N(0xff, rsX, lds_base(smem) + wid * 128, K2_XOFF + 8192, (unsigned)((64 * mt + row1) * 1152 + nt * 144 + (o1 - row1 * 144)), 0);
      }
      if (MODE == 2 && f == 2) {
        CODE_TOUCH("k2_seq", lds_base(smem) + K2_SCR, i, wid);
        int n = wid * 14 + (lane < 13 ? lane : 13);
        n = i * 108 + (n < 107 ? n : 107);
        const int row = n / 9;
        TOUCH(mksrd(wg, D_ * F_ * 2), lds_base(smem) + K2_SCR, (unsigned)(row * 9216 + xcd * 1152 + (n - row * 9) * 128));
      }
      SCHED();
      acc[f][0] = MFMA16(c.e[f], c.t0, acc[f][0]); acc[f][1] = MFMA16(c.e[f], c.t1, acc[f][1]);
      SCHED();
      if (RD && f < 4) rd(nx, j + 1, f);
      SCHED();
    }
    WAITL0();
  };
  using I0 = IC<0>; using I1 = IC<1>; using I2 = IC<2>;

  STAMP(ws, 1, 0);
  issue(0, rsRe); issue(1, rsRe); issue(2, rsRe); issue(3, rsRe);
  WAITV(15); BAR();
  STAMP(ws, 1, 1);
  rd(fr[0], 0, 0); rd(fr[0], 0, 1); rd(fr[0], 0, 2); rd(fr[0], 0, 3);
  WAITL0();
  WAITV(10); BAR();
  for (int j = 0; j < 12; j += 2) {
    stage(j, I0{}, rsRe, I1{}, I1{}); WAITV(10); BAR();
    stage(j + 1, I1{}, rsRe, I1{}, I1{}); WAITV(10); BAR();
  }
  for (int j = 12; j < 28; j += 2) {
    stage(j, I0{}, rsIm, I1{}, I1{}); WAITV(10); BAR();
    stage(j + 1, I1{}, rsIm, I1{}, I1{}); WAITV(10); BAR();
  }
  stage(28, I0{}, rsIm, I2{}, I1{}); WAITV(9); BAR();
  stage(29, I1{}, rsIm, I0{}, I1{}); WAITV(4); BAR();
  stage(30, I0{}, rsIm, I0{}, I1{});
  stage(31, I1{}, rsIm, I0{}, I0{});
  WAITV(2); BAR();
  STAMP(ws, 1, 2);
#pragma unroll
  for (int f = 0; f < 5; ++f)
#pragma unroll
    for (int tf = 0; tf < 2; ++tf)
      *(f32x4*)(smem + kq * K2_RKQ + (32 * mh + 16 * tf + r16) * K2_RSTR + (16 * f + 4 * q) * 4) = acc[f][tf];
  WAITL0(); BAR();
  const rsrc_t rsH = MKRS(ws + WS_HT, HT_BYTES);
  auto item = [&](int U) {
    const int t = lane;
    f32x4 lo = {0, 0, 0, 0}, hi = {0, 0, 0, 0};
#pragma unroll
    for (int k = 0; k < 4; ++k) {
      const char* p = smem + k * K2_RKQ + t * K2_RSTR + U * 32;
      lo += *(const f32x4*)p; hi += *(const f32x4*)(p + 16);
    }
    const u32x4 xv = *(const u32x4*)(smem + K2_XOFF + t * 144 + U * 16);
    u32x4 hv = {pk_bf16(lo[0] + bf_lo(xv[0]), lo[1] + bf_hi(xv[0])), pk_bf16(lo[2] + bf_lo(xv[1]), lo[3] + bf_hi(xv[1])),
                pk_bf16(hi[0] + bf_lo(xv[2]), hi[1] + bf_hi(xv[2])), pk_bf16(hi[2] + bf_lo(xv[3]), hi[3] + bf_hi(xv[3]))};
    float v[8] = {bf_lo(hv[0]), bf_hi(hv[0]), bf_lo(hv[1]), bf_hi(hv[1]), bf_lo(hv[2]), bf_hi(hv[2]), bf_lo(hv[3]), bf_hi(hv[3])};
    float m = 0.f;
#pragma unroll
    for (int k = 0; k < 8; ++k) m += v[k];
    m *= 0.125f;
    float m2 = 0.f;
#pragma unroll
    for (int k = 0; k < 8; ++k) { const float d = v[k] - m; m2 += d * d; }
    *(f32x2*)(smem + K2_SOFF + (t * 9 + U) * 8) = f32x2{m, m2};
    const int Ug = nt * 9 + U, tg = 64 * mt + t;
    __builtin_amdgcn_raw_buffer_store_b128(hv, rsH, (unsigned)(((tg >> 4) * 72 + Ug) * 256 + (tg & 15) * 16), 0, AUX_SC1);
  };
  item(wid);
  if (wid == 0) item(8);
  WAITL0(); BAR();
  if (wid == 0) {
    const char* p = smem + K2_SOFF + lane * 72;
    f32x2 s[9];
    float m = 0.f;
#pragma unroll
    for (int k = 0; k < 9; ++k) { s[k] = *(const f32x2*)(p + k * 8); m += s[k][0]; }
    m *= (1.f / 9.f);
    float m2 = 0.f;
#pragma unroll
    for (int k = 0; k < 9; ++k) { const float d = s[k][0] - m; m2 += s[k][1] + 8.f * d * d; }
    const rsrc_t rsS = MKRS(ws + WS_ST, ST_BYTES);
    __builtin_amdgcn_raw_buffer_store_b64(__builtin_bit_cast(u32x2, f32x2{m, m2}), rsS, (unsigned)(((64 * mt + lane) * 8 + nt) * 8), 0, AUX_SC1);
  }
  STAMP(ws, 1, 3);
}

constexpr int KF_RING = 92160, KF_ST = KF_RING + 65536, KF_SCR = KF_ST + 640, KF_LDS = 163840;
#define EPI 8
#define PB1 1
#define ROT 0
#define WG0 10
#define WPG 1
#define PFINE 0
#define PG_N 12
#define PU_N 12
#define PB_N 10

#define MFMA_ACC(acc, a, b) asm volatile("v_mfma_f32_16x16x32_bf16 %0, %1, %2, %0" : "+v"(acc) : "v"(a), "v"(b))
#define ACC_READY(A) asm volatile("s_nop 7\n\ts_nop 7" : "+v"(A[0][0]), "+v"(A[0][1]), "+v"(A[0][2]), "+v"(A[0][3]), "+v"(A[0][4]), \
    "+v"(A[1][0]), "+v"(A[1][1]), "+v"(A[1][2]), "+v"(A[1][3]), "+v"(A[1][4]), "+v"(A[2][0]), "+v"(A[2][1]), "+v"(A[2][2]), "+v"(A[2][3]), \
    "+v"(A[2][4]), "+v"(A[3][0]), "+v"(A[3][1]), "+v"(A[3][2]), "+v"(A[3][3]), "+v"(A[3][4]))
#define DSR128(dst, addr, off) asm volatile("ds_read_b128 %0, %1 offset:%2" : "+v"(dst) : "v"(addr), "i"(off))
#define DSRTR(dst, addr, off) asm volatile("ds_read_b64_tr_b16 %0, %1 offset:%2" : "+v"(dst) : "v"(addr), "i"(off))

extern "C" __global__ __launch_bounds__(512, 2) void k3_ffn(const bf16_t* wg, const bf16_t* wu, const bf16_t* wd, char* ws) {
  __shared__ __attribute__((aligned(16))) char smem[KF_LDS];
  const int wid = __builtin_amdgcn_readfirstlane(threadIdx.x >> 6), lane = threadIdx.x & 63;
  const int b = blockIdx.x, xcd = b & 7, i = b >> 3;
  const u32x4 rsG = mksrd(wg, D_ * F_ * 2), rsU = mksrd(wu, D_ * F_ * 2), rsD = mksrd(wd, F_ * D_ * 2);
  const unsigned lds0 = lds_base(smem);
  if (b >= 234) {
    STAMP(ws, 2, 0); STAMP(ws, 2, 1);
    const int n0 = ((b - 234) * 8 + wid) * 64;
#pragma unroll 1
    for (int r = 0; r < 9; ++r) {
      const int n = r * 11264 + n0;
      if (n < 13824) TOUCH(rsG, lds0 + wid * 256, (unsigned)((27648 + n + lane) * 128));
      else if (n < 55296) TOUCH(rsU, lds0 + wid * 256, (unsigned)((n - 13824 + lane) * 128));
      else if (n < 96768) TOUCH(rsD, lds0 + wid * 256, (unsigned)((n - 55296 + lane) * 128));
    }
    WAITV(0);
    STAMP(ws, 2, 2); STAMP(ws, 2, 3); STAMP(ws, 2, 4); STAMP(ws, 2, 5);
    return;
  }
  const int id = xcd * 29 + (xcd < 2 ? xcd : 2) + i;
  const int tt = id < 130 ? id / 5 : (id - 130) >> 2, gp = id < 130 ? id - tt * 5 : 5 + ((id - 130) & 3);
  const u32x4 rsH = mksrd(ws + WS_HT, HT_BYTES), rsS = mksrd(ws + WS_ST, ST_BYTES);
  const rsrc_t rsP = MKRS(ws + WS_P, P_BYTES);
  STAMP(ws, 2, 0);
  WST(0);
  const int q = lane >> 4, r16 = lane & 15, r8 = lane >> 3, c8 = lane & 7, a4 = r16 >> 2, p4 = r16 & 3;
  const unsigned ring = lds0 + KF_RING + wid * 8192;
  unsigned vW[4];
#pragma unroll
  for (int j = 0; j < 4; ++j)
    vW[j] = (unsigned)((r8 + 8 * j) * 9216 + gp * 1024 + wid * 128 + ((c8 ^ (r8 & 2) ^ ((j & 1) * 4)) * 16) - j * 1024);
  auto ld = [&](u32x4 rs, int soff, int j) __attribute__((always_inline)) { RING_LD(rs, j * 1024, vW[j], soff); };
  auto ld4 = [&](u32x4 rs, int slot, int soff) __attribute__((always_inline)) {
    RING_M0(ring, slot * 4096);
    asm volatile("s_nop 0");
#pragma unroll
    for (int j = 0; j < 4; ++j) ld(rs, soff, j);
  };
  {
    f32x4 sp;
    const int tok = (wid < 5 ? wid : 4) * 16 + (lane >> 2);
    {
      int tg = tt * 80 + tok;
      tg = tg < S_ ? tg : S_ - 1;
      GLD(sp, rsS, (unsigned)(tg * 64 + (lane & 3) * 16));
    }
    BAR();
    u32x4 hv[12];
    const int r0 = ROT ? (gp < 5 ? gp : gp - 5) : 0;
    auto rot = [&](int k) { return k + r0 < 5 ? k + r0 : k + r0 - 5; };
    auto tile = [&](int T) { T += tt * 5; return (unsigned)((T < 128 ? T : 127) * 18432 + lane * 16); };
#pragma unroll
    for (int k = 0; k < 5; ++k) { GLD(hv[2 * k], rsH, tile(rot(k)) + wid * 1024); GLD(hv[2 * k + 1], rsH, tile(rot(k)) + wid * 1024 + 8192); }
    GLD(hv[10], rsH, tile(wid >> 1) + (16 + (wid & 1)) * 1024);
    GLD(hv[11], rsH, tile(4) + (16 + (wid & 1)) * 1024);
    ld4(rsG, 0, 0);
    LANDED(16, sp);
    {
      float m = sp[0] + sp[2];
      m += __shfl_xor(m, 1); m += __shfl_xor(m, 2);
      m *= 0.125f;
      const float d0 = sp[0] - m, d1 = sp[2] - m;
      float m2 = sp[1] + sp[3] + 72.f * (d0 * d0 + d1 * d1);
      m2 += __shfl_xor(m2, 1); m2 += __shfl_xor(m2, 2);
      const float r = __builtin_amdgcn_rsqf(m2 * (1.f / 576.f) + LN_EPS);
      if ((lane & 3) == 0 && wid < 5) *(f32x2*)(smem + KF_ST + tok * 8) = f32x2{r, -m * r};
    }
    WAITL0(); BAR();
    WST(1);
    f32x2 st[6];
#pragma unroll
    for (int k = 0; k < 5; ++k) st[k] = *(const f32x2*)(smem + KF_ST + (rot(k) * 16 + r16) * 8);
    const f32x2 st4 = *(const f32x2*)(smem + KF_ST + (64 + r16) * 8);
    st[5] = *(const f32x2*)(smem + KF_ST + ((wid >> 1) * 16 + r16) * 8);
    auto put = [&](u32x4 v, f32x2 c, int T, int s) __attribute__((always_inline)) {
      u32x4 o;
#pragma unroll
      for (int k = 0; k < 4; ++k) o[k] = pk_bf16(__builtin_fmaf(bf_lo(v[k]), c[0], c[1]), __builtin_fmaf(bf_hi(v[k]), c[0], c[1]));
      *(u32x4*)(smem + T * 18432 + s * 1024 + lane * 16) = o;
    };
    LANDED(15, hv[0]); put(hv[0], st[0], rot(0), wid); LANDED(14, hv[1]); put(hv[1], st[0], rot(0), wid + 8);
    LANDED(13, hv[2]); put(hv[2], st[1], rot(1), wid); LANDED(12, hv[3]); put(hv[3], st[1], rot(1), wid + 8);
    LANDED(11, hv[4]); put(hv[4], st[2], rot(2), wid); LANDED(10, hv[5]); put(hv[5], st[2], rot(2), wid + 8);
    LANDED(9, hv[6]); put(hv[6], st[3], rot(3), wid); LANDED(8, hv[7]); put(hv[7], st[3], rot(3), wid + 8);
    LANDED(7, hv[8]); put(hv[8], st[4], rot(4), wid); LANDED(6, hv[9]); put(hv[9], st[4], rot(4), wid + 8);
    LANDED(5, hv[10]);
    LANDED(4, hv[11]);
    ld4(rsG, 1, 294912);
    put(hv[10], st[5], wid >> 1, 16 + (wid & 1));
    if (wid < 2) put(hv[11], st4, 4, 16 + wid);
    WAITV(4); WAITL0(); BAR();
  }
  STAMP(ws, 2, 1);
  WST(2);

  unsigned aA[4], aB[4];
#pragma unroll
  for (int c = 0; c < 4; ++c) {
    aA[c] = ring + (unsigned)((8 * q + a4) * 128 + ((c ^ ((a4 >> 1) | ((q & 1) << 1))) * 32) + p4 * 8);
  }
  unsigned at0 = lds0 + (unsigned)(lane * 16), at1 = at0 + 3 * 18432;
  s16x4 wl[2][4], wh[2][4];
  bf16x8 tk[5];
  auto fresh = [&]() __attribute__((always_inline)) {
#pragma unroll
    for (int c = 0; c < 4; ++c) asm volatile("" : "=v"(wl[0][c]), "=v"(wh[0][c]), "=v"(wl[1][c]), "=v"(wh[1][c]));
#pragma unroll
    for (int t = 0; t < 5; ++t) asm volatile("" : "=v"(tk[t]));
  };
  fresh();
  auto rdw = [&](int buf, const unsigned(&a)[4], int hi) __attribute__((always_inline)) {
#pragma unroll
    for (int c = 0; c < 4; ++c) { DSRTR(wl[buf][c], a[c], buf * 4096); DSRTR(wh[buf][c], a[c], buf * 4096 + hi); }
  };
  auto rdt = [&](int t, int str, int off) __attribute__((always_inline)) {
    if (t < 3) DSR128(tk[t], at0, t * str + off); else DSR128(tk[t], at1, (t - 3) * str + off);
  };
#define LANDED_L(n) asm volatile("s_waitcnt lgkmcnt(%0)" ::"n"(n))
#define LANDED_W(n, buf) asm volatile("s_waitcnt lgkmcnt(%12)" : "+v"(wl[buf][0]), "+v"(wl[buf][1]), "+v"(wl[buf][2]), "+v"(wl[buf][3]), \
    "+v"(wh[buf][0]), "+v"(wh[buf][1]), "+v"(wh[buf][2]), "+v"(wh[buf][3]), "+v"(tk[0]), "+v"(tk[1]), "+v"(tk[2]), "+v"(tk[3]) : "n"(n))
#define LANDED_WB(buf) asm volatile("s_waitcnt lgkmcnt(0)" : "+v"(wl[buf][0]), "+v"(wl[buf][1]), "+v"(wl[buf][2]), "+v"(wl[buf][3]), \
    "+v"(wh[buf][0]), "+v"(wh[buf][1]), "+v"(wh[buf][2]), "+v"(wh[buf][3]))
#define LANDED_T(n, t) asm volatile("s_waitcnt lgkmcnt(%1)" : "+v"(tk[t]) : "n"(n))
  auto opnd = [&](int buf, int c) __attribute__((always_inline)) -> bf16x8 {
    return __builtin_shufflevector(wl[buf][c], wh[buf][c], 0, 1, 2, 3, 4, 5, 6, 7);
  };
  auto step = [&](f32x4(&acc)[4][5], auto P_, auto LD_, auto NW_, auto NT_, int str, int toff, u32x4 rs, int soff, auto gap) __attribute__((always_inline)) {
    constexpr int P = decltype(P_)::value, NW = decltype(NW_)::value;
    constexpr bool LD = decltype(LD_)::value, NT = decltype(NT_)::value;
    constexpr int W16 = (16 - WG0) * WPG < 8 ? (16 - WG0) * WPG : 8;
    if (LD) RING_M0(ring, P * 4096);
    LANDED_L(1);
#pragma unroll
    for (int t = 0; t < 5; ++t) {
      if (t == 4) LANDED_L((NT ? 4 : 0) + (NW ? W16 : 0));
      SCHED();
#pragma unroll
      for (int c = 0; c < 4; ++c) {
        const int g = t * 4 + c;
        MFMA_ACC(acc[c][t], opnd(P, c), tk[t]);
        SCHED();
        if (LD && t < 2 && (c & 1) == 0) { ld(rs, soff, 2 * t + (c >> 1)); SCHED(); }
        if (NW && g >= WG0 && g < WG0 + 8 / WPG) {
          if (g == WG0) { if (LD) WAITV(4); else WAITV(0); }
#pragma unroll
          for (int k = (g - WG0) * WPG; k < (g - WG0 + 1) * WPG; ++k) {
            const unsigned a = NW == 1 ? aA[k >> 1] : aB[k >> 1];
            if (k & 1) DSRTR(wh[P ^ 1][k >> 1], a, (P ^ 1) * 4096 + (NW == 1 ? 512 : 2048)); else DSRTR(wl[P ^ 1][k >> 1], a, (P ^ 1) * 4096);
          }
        }
        gap(g);
      }
      if (NT) rdt(t, str, toff);
    }
  };
  auto nogap = [&](int) __attribute__((always_inline)) {};
  using I0 = IC<0>; using I1 = IC<1>; using I2 = IC<2>;

  f32x4 ag[4][5], au[4][5];
#pragma unroll
  for (int c = 0; c < 4; ++c)
#pragma unroll
    for (int t = 0; t < 5; ++t) { ag[c][t] = f32x4{0, 0, 0, 0}; au[c][t] = f32x4{0, 0, 0, 0}; }
  ACC_READY(ag);

#pragma unroll
  for (int t = 0; t < 5; ++t) { rdt(t, 18432, 0); if (t == 3) rdw(0, aA, 512); }
  auto coarse = [&](int s, int n) __attribute__((always_inline)) {
    if (PRIO) asm volatile("s_cmp_lt_u32 %0, 4\n\ts_cbranch_scc1 2f\n\ts_cmp_lt_u32 %1, %2\n\ts_cbranch_scc1 1f\n\ts_setprio 0\n\ts_branch 2f\n1:\n\ts_setprio 1\n2:" ::"s"(wid), "s"(s), "s"(n) : "scc");
  };
#pragma clang loop unroll(disable)
  for (int s = 0; s < 16; s += 2) {
    coarse(s, PG_N);
    step(ag, I0{}, I1{}, I1{}, I1{}, 18432, 1024, rsG, (s + 2) * 294912, nogap);
    step(ag, I1{}, I1{}, I1{}, I1{}, 18432, 2048, rsG, (s + 3) * 294912, nogap);
    at0 += 2048; at1 += 2048;
  }
  at0 -= 16 * 1024; at1 -= 16 * 1024;
  step(ag, I0{}, I1{}, I1{}, I1{}, 18432, 17 * 1024, rsU, 0, nogap);
  step(ag, I1{}, I1{}, I1{}, I1{}, 18432, 0, rsU, 294912, nogap);
  WST(3);
  ACC_READY(ag); ACC_READY(au);
  float sx[5] = {0.f, 0.f, 0.f, 0.f, 0.f}, gs[80];
#pragma unroll
  for (int v = 0; v < 80; ++v) gs[v] = ag[(v >> 2) & 3][v >> 4][v & 3];
  auto ustep = [&](auto S_c) __attribute__((always_inline)) {
    constexpr int S = decltype(S_c)::value, NV = S < 8 ? 5 : 4, V0 = S < 8 ? 5 * S : 40 + 4 * (S - 8), NOP = 5 * NV;
    auto gap = [&](int g) __attribute__((always_inline)) {
      const int gi = g < 8 ? ((g & 1) ? g >> 1 : -1) : g - 4;
      if (gi < 0) return;
#pragma unroll
      for (int k = 0; k < NOP; ++k) {
        if (k * 16 / NOP != gi) continue;
        const int op = k / NV, e = k - op * NV, v = V0 + e;
        if (op == 0) asm volatile("v_mul_f32 %0, 0xbfb8aa3b, %1" : "+v"(sx[e]) : "v"(gs[v]));
        if (op == 1) asm volatile("v_exp_f32 %0, %0" : "+v"(sx[e]));
        if (op == 2) asm volatile("v_add_f32 %0, 1.0, %0" : "+v"(sx[e]));
        if (op == 3) asm volatile("v_rcp_f32 %0, %0" : "+v"(sx[e]));
        if (op == 4) asm volatile("v_mul_f32 %0, %0, %1" : "+v"(gs[v]) : "v"(sx[e]));
      }
      SCHED();
    };
    if (S == 0) PRIO_HI(wid);
    if (S == PU_N) PRIO_LO(wid);
    step(au, IC<S & 1>{}, I1{}, IC<(S < 17 ? 1 : 2)>{}, IC<(S < 17 ? 1 : 0)>{}, 18432, (S + 1) * 1024, S < 16 ? rsU : rsD,
         S < 16 ? (S + 2) * 294912 : (gp * 512 + wid * 64 + (S & 1) * 32) * 1152 + 1024 - wid * 128, gap);
  };
  ustep(IC<0>{}); ustep(IC<1>{}); ustep(IC<2>{}); ustep(IC<3>{}); ustep(IC<4>{}); ustep(IC<5>{});
  ustep(IC<6>{}); ustep(IC<7>{}); ustep(IC<8>{}); ustep(IC<9>{}); ustep(IC<10>{}); ustep(IC<11>{});
  ustep(IC<12>{}); ustep(IC<13>{}); ustep(IC<14>{}); ustep(IC<15>{});
  {
    int l = lane;
    asm volatile("" : "+v"(l));
    const int q_ = l >> 4, a4_ = (l >> 2) & 3, r8_ = l >> 3;
#pragma unroll
    for (int j = 0; j < 4; ++j) vW[j] = (unsigned)((r8_ + 8 * j) * 1152 + wid * 128 + (((l & 7) ^ (r8_ & 6)) * 16) - j * 1024);
#pragma unroll
    for (int c = 0; c < 4; ++c) aB[c] = ring + (unsigned)((4 * q_ + a4_) * 128 + ((c ^ ((2 * q_ + (a4_ >> 1)) & 3)) * 32) + (l & 3) * 8);
  }
  ustep(IC<16>{}); ustep(IC<17>{});
  ACC_READY(au);
  STAMP(ws, 2, 4);

  f32x4 o8[4][5], om[4][5];
  u32x4 act[2][5];
  const f32x4 zero4 = {0, 0, 0, 0};
  LANDED_WB(0);
  ld4(rsD, 0, gp * 512 * 1152);
  WAITV(4); rdw(1, aB, 2048);
  CODE_TOUCH("k4_sum", lds0 + KF_SCR, 0, wid);
#pragma unroll
  for (int pr = 0; pr < 2; ++pr) {
    if (pr == 1) {
      LANDED_WB(1);
      ld4(rsD, 1, (gp * 512 + 32) * 1152);
    }
#pragma unroll
    for (int t = 0; t < 5; ++t) {
      const float* g0 = gs + (t * 4 + 2 * pr) * 4;
      const f32x4 lo = f32x4{g0[0], g0[1], g0[2], g0[3]} * au[2 * pr][t], hi = f32x4{g0[4], g0[5], g0[6], g0[7]} * au[2 * pr + 1][t];
      act[pr][t] = u32x4{pk_bf16(lo[0], lo[1]), pk_bf16(lo[2], lo[3]), pk_bf16(hi[0], hi[1]), pk_bf16(hi[2], hi[3])};
      const bf16x8 f = __builtin_bit_cast(bf16x8, act[pr][t]);
#pragma unroll
      for (int c = 0; c < 4; ++c) o8[c][t] = MFMA16(opnd(pr, c), f, pr == 0 ? zero4 : o8[c][t]);
      SCHED();
    }
  }
  WST(4);
  BAR();
#pragma unroll
  for (int pr = 0; pr < 2; ++pr)
#pragma unroll
    for (int t = 0; t < 5; ++t) *(u32x4*)(smem + (t * 16 + 2 * wid + pr) * 1024 + lane * 16) = act[pr][t];
#pragma unroll
  for (int c = 0; c < 4; ++c)
#pragma unroll
    for (int t = 0; t < 5; ++t) om[c][t] = f32x4{0, 0, 0, 0};
  ACC_READY(om);
  WST(5);
  WAITL0(); BAR();
  WST(6);
  STAMP(ws, 2, 5);

  at0 = lds0 + (unsigned)(lane * 16); at1 = at0 + 3 * 16384;
  fresh();
  WAITV(4);
#pragma unroll
  for (int t = 0; t < 5; ++t) { rdt(t, 16384, 0); if (t == 3) rdw(0, aB, 2048); }
#pragma clang loop unroll(disable)
  for (int s = 0; s < 14; s += 2) {
    coarse(s, PB_N);
    step(om, I0{}, I1{}, I2{}, I1{}, 16384, 1024, rsD, (gp * 512 + (s + 2) * 32) * 1152, nogap);
    step(om, I1{}, I1{}, I2{}, I1{}, 16384, 2048, rsD, (gp * 512 + (s + 3) * 32) * 1152, nogap);
    at0 += 2048; at1 += 2048;
  }
  step(om, I0{}, I0{}, I2{}, I1{}, 16384, 1024, rsD, 0, nogap);
  step(om, I1{}, I0{}, I0{}, I0{}, 16384, 0, rsD, 0, nogap);
  PRIO_LO(wid);
  ACC_READY(om);
  WST(7);
  STAMP(ws, 2, 2);

  const int uo = (q & 1) * 2 + (q >> 1);
  auto emit = [&](f32x4 lo, f32x4 hi, int t, int E) __attribute__((always_inline)) {
    const auto x = __builtin_amdgcn_permlane16_swap(pk_bf16(lo[0], lo[1]), pk_bf16(hi[0], hi[1]), false, false);
    const auto y = __builtin_amdgcn_permlane16_swap(pk_bf16(lo[2], lo[3]), pk_bf16(hi[2], hi[3]), false, false);
    const int T16 = tt * 5 + t;
    if (T16 < 128)
      __builtin_amdgcn_raw_buffer_store_b128(u32x4{x[0], y[0], x[1], y[1]}, rsP, (unsigned)(((gp * 128 + T16) * 72 + E * 4 + uo) * 256 + r16 * 16), 0, AUX_SC1);
  };
#pragma unroll
  for (int k = 0; k < EPI - 1; ++k) emit(om[(k & 1) * 2][k >> 1], om[(k & 1) * 2 + 1][k >> 1], k >> 1, 2 * wid + (k & 1));
  SCHED();
  BAR();
#pragma unroll
  for (int t = 0; t < 5; ++t) {
#pragma unroll
    for (int c = 0; c < 4; ++c) *(f32x4*)(smem + (wid * 20 + t * 4 + c) * 1024 + lane * 16) = o8[c][t];
    SCHED();
  }
  WAITL0(); BAR();
#pragma unroll
  for (int k = EPI - 1; k < 10; ++k) emit(om[(k & 1) * 2][k >> 1], om[(k & 1) * 2 + 1][k >> 1], k >> 1, 2 * wid + (k & 1));
  SCHED();
  WST(8);
  WST(9);
  auto unit = [&](int u) __attribute__((always_inline)) {
    const char* p = smem + u * 2048 + lane * 16;
    f32x4 lo = {0, 0, 0, 0}, hi = {0, 0, 0, 0};
#pragma unroll
    for (int k = 0; k < 8; ++k) { lo += *(const f32x4*)(p + k * 20480); hi += *(const f32x4*)(p + k * 20480 + 1024); }
    emit(lo, hi, u >> 1, 16 + (u & 1));
  };
  unit(wid);
  if (wid < 2) unit(8 + wid);
  WST(10);
  WAITV(0);
  WST(11);
  WST_OUT(ws, 12);
  STAMP(ws, 2, 3);
}

extern "C" __global__ __launch_bounds__(576) void k4_sum(char* ws, bf16_t* out) {
  STAMP(ws, 3, 0);
  const int wv = threadIdx.x >> 6, lane = threadIdx.x & 63;
  const int T16 = blockIdx.x >> 1, t = (blockIdx.x & 1) * 8 + (lane >> 3), U = wv * 8 + (lane & 7);
  const rsrc_t rsP = MKRS(ws + WS_P, P_BYTES), rsH = MKRS(ws + WS_HT, HT_BYTES), rsO = MKRS(out, S_ * D_ * 2);
  const unsigned off = (unsigned)((T16 * 72 + U) * 256 + t * 16);
  u32x4 p[9];
#pragma unroll
  for (int g = 0; g < 9; ++g) p[g] = __builtin_amdgcn_raw_buffer_load_b128(rsP, off, g * (int)HT_BYTES, 0);
  u32x4 hv = __builtin_amdgcn_raw_buffer_load_b128(rsH, off, 0, 0);
  STAMP(ws, 3, 1);
  float a[8];
#pragma unroll
  for (int k = 0; k < 4; ++k) { a[2 * k] = bf_lo(p[0][k]); a[2 * k + 1] = bf_hi(p[0][k]); }
#pragma unroll
  for (int g = 1; g < 9; ++g)
#pragma unroll
    for (int k = 0; k < 4; ++k) { a[2 * k] += bf_lo(p[g][k]); a[2 * k + 1] += bf_hi(p[g][k]); }
  u32x4 ov;
#pragma unroll
  for (int k = 0; k < 4; ++k) ov[k] = pk_bf16(a[2 * k] + bf_lo(hv[k]), a[2 * k + 1] + bf_hi(hv[k]));
  STAMP(ws, 3, 2);
  __builtin_amdgcn_raw_buffer_store_b128(ov, rsO, (unsigned)((T16 * 16 + t) * 1152 + U * 16), 0, 0);
  STAMP(ws, 3, 3);
  if (blockIdx.x == 0) {
    __shared__ long long red[9][4];
    const int tb = threadIdx.x;
    long long v = 0; int mode = 0;
    auto rt = [&](int k, int b, int sl) -> long long { return (long long)*(const unsigned long long*)(ws + (64u << 20) + k * 65536 + b * 128 + sl * 16 + 8); };
    auto cy = [&](int k, int b, int sl) -> long long { return (long long)*(const unsigned long long*)(ws + (64u << 20) + k * 65536 + b * 128 + sl * 16); };
    auto wred = [&](long long x, int op) -> long long {
      for (int o = 1; o < 64; o <<= 1) { long long y = __shfl_xor(x, o); x = op == 0 ? (y < x ? y : x) : op == 1 ? (y > x ? y : x) : x + y; }
      if ((tb & 63) == 0) red[tb >> 6][0] = x;
      __syncthreads();
      long long r = red[0][0];
      for (int w = 1; w < 4; ++w) { long long y = red[w][0]; r = op == 0 ? (y < r ? y : r) : op == 1 ? (y > r ? y : r) : r + y; }
      __syncthreads();
      return r;
    };
    const int b = tb < 256 ? tb : 255;
    const bool mainb = b < NMAIN;
    const long long BIG = 1ll << 62;
    const long long s1 = wred(rt(0, b, 0), 0), s2 = wred(rt(1, b, 0), 0), s3 = wred(mainb ? rt(2, b, 0) : BIG, 0), s4 = wred(rt(3, b, 0) > 0 && b == 0 ? rt(3, b, 0) : BIG, 0);
    const long long e1 = wred(rt(0, b, 3), 1), e2 = wred(rt(1, b, 3), 1), e3 = wred(mainb ? rt(2, b, 3) : 0, 1), ep = wred(mainb ? 0 : rt(2, b, 3), 1);
    auto avg = [&](int k, int a, int c) { return wred(mainb && tb < 256 ? rt(k, b, c) - rt(k, b, a) : 0, 2) / NMAIN; };
    long long qq[19];
    qq[0] = 0;
    qq[1] = s2 - s1;
    qq[2] = s3 - s2;
    qq[3] = e3 - s3;
    qq[4] = avg(2, 0, 1);
    qq[5] = ep - s3;
    qq[6] = avg(2, 1, 4);
    qq[7] = avg(2, 4, 5);
    qq[8] = avg(2, 5, 2);
    qq[9] = avg(2, 2, 3);
    qq[10] = wred(mainb && tb < 256 ? cy(2, b, 3) - cy(2, b, 0) : 0, 2) / NMAIN / 24;
    qq[11] = s4 - e3;
    qq[12] = e1 - s1;
    qq[13] = e2 - s2;
    qq[14] = wred(tb < 256 ? rt(1, b, 1) - rt(1, b, 0) : 0, 2) / 256;
    qq[15] = wred(tb < 256 ? rt(0, b, 1) - rt(0, b, 0) : 0, 2) / 256;
    qq[16] = wred(tb < 256 ? cy(1, b, 3) - cy(1, b, 0) : 0, 2) / 256 / 24;
    qq[17] = wred(tb < 256 ? rt(1, b, 3) - rt(1, b, 0) : 0, 2) / 256;
    qq[18] = wred(mainb && tb < 256 ? rt(2, b, 3) - rt(2, b, 0) : 0, 2) / NMAIN;
    for (int k = 0; k < 19; ++k) asm volatile("" ::"v"(qq[k]));
    long long q = qq[QSPIN];
    q *= 4;
    q = q < 0 ? 0 : (q > 30000 ? 30000 : q);
    if (tb == 0) { const long long t0 = (long long)__builtin_amdgcn_s_memrealtime(); while ((long long)__builtin_amdgcn_s_memrealtime() - t0 < q) __builtin_amdgcn_s_sleep(2); }
  }

}

extern "C" void kernel_launch(void* const* d_in, const int* in_sizes, int n_in, void* d_out, int out_size, void* d_ws, size_t ws_size,
                              hipStream_t stream) {
  const bf16_t* x = (const bf16_t*)d_in[0];
  const bf16_t* cre = (const bf16_t*)d_in[1];
  const bf16_t* cim = (const bf16_t*)d_in[2];
  const bf16_t* wre = (const bf16_t*)d_in[3];
  const bf16_t* wim = (const bf16_t*)d_in[4];
  const bf16_t* wg = (const bf16_t*)d_in[6];
  const bf16_t* wu = (const bf16_t*)d_in[7];
  const bf16_t* wd = (const bf16_t*)d_in[8];
  char* ws = (char*)d_ws;
  k1_hid<<<256, 512, 0, stream>>>(x, wre, wim, cre, cim, ws);
  k2_seq<<<256, 512, 0, stream>>>(x, cre, cim, wg, wu, ws);
  k3_ffn<<<256, 512, 0, stream>>>(wg, wu, wd, ws);
  k4_sum<<<256, 576, 0, stream>>>(ws, (bf16_t*)d_out);
}
```

```cpp
#include <hip/hip_runtime.h>
#include <stdint.h>

typedef unsigned short bf16_t;
typedef short bf16x8 __attribute__((ext_vector_type(8)));
typedef short s16x4 __attribute__((ext_vector_type(4)));
typedef float f32x4 __attribute__((ext_vector_type(4)));
typedef float f32x2 __attribute__((ext_vector_type(2)));
typedef unsigned u32x4 __attribute__((ext_vector_type(4)));
typedef unsigned u32x2 __attribute__((ext_vector_type(2)));
typedef __bf16 bf2_t __attribute__((ext_vector_type(2)));
typedef __amdgpu_buffer_rsrc_t rsrc_t;

constexpr int S_ = 2048, D_ = 576, F_ = 4608;
constexpr float LN_EPS = 1e-5f;

constexpr unsigned WS_BP = 0;
constexpr unsigned BP_BYTES = 8u * 32u * 18432u;
constexpr unsigned WS_HT = WS_BP + BP_BYTES;
constexpr unsigned HT_BYTES = 128u * 72u * 256u;
constexpr unsigned WS_ST = WS_HT + HT_BYTES;
constexpr unsigned ST_BYTES = 2048u * 8u * 8u;
constexpr unsigned WS_P = WS_ST + ST_BYTES;
constexpr unsigned P_BYTES = 9u * HT_BYTES;

#define LDSP(p) ((__attribute__((address_space(3))) void*)(p))
#define MKRS(p, n) __builtin_amdgcn_make_buffer_rsrc((void*)(p), 0, (n), 0x00020000)
#define GLDS(rs, m0base, imm, voff, soff)                                                              \
  asm volatile("s_add_u32 m0, %0, %1\n\ts_nop 0\n\tbuffer_load_dwordx4 %2, %3, %4 offen lds"          \
               ::"s"(m0base), "i"(imm), "v"(voff), "s"(rs), "s"(soff) : "memory", "scc")
#define GLDS_N(mask, rs, m0base, imm, voff, soff)                                                      \
  asm volatile("s_add_u32 m0, %0, %1\n\ts_mov_b64 exec, " #mask "\n\tbuffer_load_dwordx4 %2, %3, %4 offen lds\n\ts_mov_b64 exec, -1" \
               ::"s"(m0base), "i"(imm), "v"(voff), "s"(rs), "s"(soff) : "memory", "scc")
#define GLDS_36(rs, m0base, imm, voff, soff)                                                           \
  asm volatile("s_add_u32 m0, %0, %1\n\ts_mov_b32 exec_hi, 0xf\n\tbuffer_load_dwordx4 %2, %3, %4 offen lds\n\ts_mov_b32 exec_hi, -1" \
               ::"s"(m0base), "i"(imm), "v"(voff), "s"(rs), "s"(soff) : "memory", "scc")
#define RING_M0(m0base, imm) asm volatile("s_add_u32 m0, %0, %1" ::"s"(m0base), "i"(imm) : "scc")
#define RING_LD(rs, off, voff, soff) asm volatile("buffer_load_dwordx4 %0, %1, %2 offen offset:%3 lds" ::"v"(voff), "s"(rs), "s"(soff), "i"(off) : "memory")
#define TOUCH(rs, mscr, voff)                                                                          \
  asm volatile("s_mov_b32 m0, %0\n\ts_nop 0\n\tbuffer_load_dword %1, %2, 0 offen lds" ::"s"(mscr), "v"(voff), "s"(rs) : "memory")
#define TOUCHS(rs, mscr, voff, soff)                                                                   \
  asm volatile("s_mov_b32 m0, %0\n\ts_nop 0\n\tbuffer_load_dword %1, %2, %3 offen lds" ::"s"(mscr), "v"(voff), "s"(rs), "s"(soff) : "memory")
__device__ __forceinline__ u32x4 mksrd(const void* p, unsigned n) {
  const unsigned long long a = (unsigned long long)p;
  return u32x4{(unsigned)a, (unsigned)(a >> 32) & 0xffffu, n, 0x00020000u};
}
#define CODE_ADDR(lo, hi, sym)                                                                         \
  asm volatile("s_getpc_b64 s[20:21]\n\ts_add_u32 %0, s20, " sym "@rel32@lo+4\n\ts_addc_u32 %1, s21, " sym "@rel32@hi+12" \
               : "=s"(lo), "=s"(hi)::"s20", "s21", "scc")
#define CODE_TOUCH(sym, mscr, i, wid)                                                                  \
  do {                                                                                                 \
    unsigned lo_, hi_, le_, he_;                                                                       \
    CODE_ADDR(lo_, hi_, sym); CODE_ADDR(le_, he_, "k4_sum");                                           \
    const unsigned n_ = le_ - lo_ + 1024u;                                                             \
    TOUCH((u32x4{lo_, hi_ & 0xffffu, n_ < 65536u ? n_ : 0u, 0x00020000u}), mscr, (unsigned)(((i) * 8 + (wid)) * 128)); \
  } while (0)
__device__ __forceinline__ unsigned lds_base(char* smem) { return (unsigned)(size_t)LDSP(smem); }
#define GLD(dst, rs, voff) asm volatile("buffer_load_dwordx4 %0, %1, %2, 0 offen" : "=v"(dst) : "v"(voff), "s"(rs) : "memory")
#define LANDED(n, x) asm volatile("s_waitcnt vmcnt(" #n ")" : "+v"(x))
#define WAITV(n) asm volatile("s_waitcnt vmcnt(" #n ")" ::: "memory")
#define WAITL0() asm volatile("s_waitcnt lgkmcnt(0)" ::: "memory")
#define PIN(x) asm volatile("" : "+v"(x))
#define BAR() __builtin_amdgcn_s_barrier()
#define SCHED() __builtin_amdgcn_sched_barrier(0)
#define MFMA16(a, b, c) __builtin_amdgcn_mfma_f32_16x16x32_bf16((a), (b), (c), 0, 0, 0)
constexpr int AUX_SC1 = 16;
#define PRIO 1
#define PA_T 2
#define PB_T 2
#define PRIO_SET(wid, p) do { if (PRIO) asm volatile("s_cmp_lt_u32 %0, 4\n\ts_cbranch_scc1 1f\n\ts_setprio " #p "\n1:" ::"s"(wid) : "scc"); } while (0)
#define PRIO_HI(wid) PRIO_SET(wid, 1)
#define PRIO_LO(wid) PRIO_SET(wid, 0)
template <int V> struct IC { static constexpr int value = V; };
#define CS_T 3
#define CT2_MODE 1
#define CT3_MODE 2
#define K2_VAR 0
#define K4_AUX 0
#define TSX(var, j) do { } while (0)
#define TS_OUT(ws, kid, wid, lane, k, var) do { } while (0)
#define STAMP(ws, kid, slot) do { } while (0)

#define WST(k) do { } while (0)
#define WST_OUT(ws, n) do { } while (0)

#define TS4(j) do { } while (0)
#define TS4_OUT(ws) do { } while (0)

__device__ __forceinline__ unsigned pk_bf16(float lo, float hi) {
  f32x2 v = {lo, hi};
  bf2_t b = __builtin_convertvector(v, bf2_t);
  return __builtin_bit_cast(unsigned, b);
}
__device__ __forceinline__ float bf_lo(unsigned w) { return __uint_as_float(w << 16); }
__device__ __forceinline__ float bf_hi(unsigned w) { return __uint_as_float(w & 0xffff0000u); }
__device__ __forceinline__ float dot_ones(unsigned w, float acc) {
  const bf2_t ones = {(__bf16)1.0f, (__bf16)1.0f};
  return __builtin_amdgcn_fdot2_f32_bf16(__builtin_bit_cast(bf2_t, w), ones, acc, false);
}
__device__ __forceinline__ float dot_frag(bf16x8 f, float acc) {
  u32x4 u = __builtin_bit_cast(u32x4, f);
  acc = dot_ones(u[0], acc); acc = dot_ones(u[1], acc); acc = dot_ones(u[2], acc); acc = dot_ones(u[3], acc);
  return acc;
}

constexpr int K1_SLOT = 8192 + 18432, K1_NS = 5, K1_ST = K1_NS * K1_SLOT, K1_WS = K1_ST + 512, K1_SCR = K1_WS + 1152, K1_LDS = K1_SCR + 256;

template <int NF>
__device__ __forceinline__ void k1_body(char* smem, u32x4 rsX, u32x4 rsW, u32x4 rsC, rsrc_t rsO, int st, int et, int wid, int lane, int nf0,
                                        unsigned vhead, bool headx, char* ws) {
  STAMP(ws, 0, 0);
  const int tid = wid * 64 + lane;
  const int sh = wid >> 2, q = lane >> 4, r16 = lane & 15, r8 = lane >> 3, c8 = lane & 7;
  const unsigned vx = (unsigned)(64 * st + 8 * wid + r8) * 1152u + (unsigned)((c8 ^ ((r8 & 3) | ((wid & 1) << 2))) * 16);
  const unsigned vw = (unsigned)((et & 3) * 144 + 8 * wid + r8) * 1152u + (unsigned)((c8 ^ r8) * 16);
  const int r2 = 128 + 2 * wid + r8;
  const unsigned vw2 = (unsigned)((et & 3) * 144 + r2) * 1152u + (unsigned)((c8 ^ (r2 & 7)) * 16);
  const unsigned lds0 = lds_base(smem);
  const unsigned mA = lds0 + wid * 1024, mP = lds0 + wid * 256;
  auto issue1 = [&](int kb, int k) __attribute__((always_inline)) {
    const int so = (kb % K1_NS) * K1_SLOT;
    if (k == 0) GLDS(rsX, mA, so, vx, kb * 128);
    if (k == 1) GLDS(rsW, mA, so + 8192, vw, kb * 128);
    if (k == 2) GLDS(rsW, mA, so + 16384, vw, kb * 128 + 64 * 1152);
    if (k == 3) GLDS_N(0xffff, rsW, mP, so + 24576, vw2, kb * 128);
  };
  auto issue = [&](int kb) __attribute__((always_inline)) { issue1(kb, 0); issue1(kb, 1); issue1(kb, 2); issue1(kb, 3); };
  unsigned ax[2];
#pragma unroll
  for (int m = 0; m < 2; ++m) {
    const int row = 32 * sh + 8 * (r16 >> 2) + 4 * m + (r16 & 3);
    const int key = (row & 3) | ((row >> 1) & 4);
    ax[m] = (unsigned)(row * 128 + ((q ^ key) * 16)) ^ (unsigned)(sh * 64);
  }
  const unsigned aw = (unsigned)(8192 + (nf0 * 16 + r16) * 128 + ((q ^ (r16 & 7)) * 16)) ^ (unsigned)(sh * 64);

  f32x4 acc[2][NF];
  float wsum[NF];
#pragma unroll
  for (int n = 0; n < NF; ++n) { wsum[n] = 0.f; acc[0][n] = f32x4{0, 0, 0, 0}; acc[1][n] = f32x4{0, 0, 0, 0}; }
  float shift = 0.f;
  f32x2 s1 = {0.f, 0.f}, s2 = {0.f, 0.f};

  struct Fr { u32x4 v; bf16x8 a[2][2], b[2][NF]; };
  Fr fr[2];
  auto rd = [&](Fr& f, int kb, int part) __attribute__((always_inline)) {
    const char* sl = smem + (kb % K1_NS) * K1_SLOT;
    if (part == 0) {
      f.a[0][0] = *(const bf16x8*)(sl + ax[0]); f.a[0][1] = *(const bf16x8*)(sl + ax[1]);
#pragma unroll
      for (int n = 0; n < NF; ++n) f.b[0][n] = *(const bf16x8*)(sl + aw + n * 2048);
    } else {
      f.v = *(const u32x4*)(sl + tid * 16);
      f.a[1][0] = *(const bf16x8*)(sl + (ax[0] ^ 64)); f.a[1][1] = *(const bf16x8*)(sl + (ax[1] ^ 64));
#pragma unroll
      for (int n = 0; n < NF; ++n) f.b[1][n] = *(const bf16x8*)(sl + ((aw ^ 64) + n * 2048));
    }
  };
  CODE_TOUCH("k1_hid", lds0 + K1_SCR, blockIdx.x >> 3, wid);
  issue(0); issue(1);
  TOUCH(headx ? rsX : rsW, lds0 + K1_SCR, vhead);
  issue(2); issue(3); issue(4);
  WAITV(17); BAR();
  STAMP(ws, 0, 1);
  rd(fr[0], 0, 0); rd(fr[0], 0, 1);
  WAITL0();
  WAITV(13); BAR();
#pragma unroll
  for (int kb = 0; kb < 9; ++kb) {
    const Fr& c = fr[kb & 1];
    Fr& nx = fr[(kb & 1) ^ 1];
    const bool stg = kb + 5 < 9;
    if (stg) issue1(kb + 5, 0);
    if (kb == 4)
      TOUCH(rsC, lds0 + K1_SCR, (unsigned)(blockIdx.x * 32768 + (wid & 3) * 8192 + lane * 128));
    SCHED();
#pragma unroll
    for (int n = 0; n < NF; ++n) { acc[0][n] = MFMA16(c.a[0][0], c.b[0][n], acc[0][n]); acc[1][n] = MFMA16(c.a[0][1], c.b[0][n], acc[1][n]); }
    SCHED();
    if (kb < 8) rd(nx, kb + 1, 0);
    SCHED();
    if (stg) issue1(kb + 5, 1);
    SCHED();
    if (kb == 0) shift = bf_lo(c.v[0]);
#pragma unroll
    for (int i = 0; i < 4; ++i) {
      f32x2 d = {bf_lo(c.v[i]) - shift, bf_hi(c.v[i]) - shift};
      s1 += d; s2 += d * d;
    }
    PIN(s1); PIN(s2);
    SCHED();
    if (stg) issue1(kb + 5, 2);
    SCHED();
#pragma unroll
    for (int n = 0; n < NF; ++n) { acc[0][n] = MFMA16(c.a[1][0], c.b[1][n], acc[0][n]); acc[1][n] = MFMA16(c.a[1][1], c.b[1][n], acc[1][n]); }
    SCHED();
    if (kb < 8) rd(nx, kb + 1, 1);
    SCHED();
#pragma unroll
    for (int n = 0; n < NF; ++n) wsum[n] = dot_frag(c.b[0][n], wsum[n]);
#pragma unroll
    for (int n = 0; n < NF; ++n) PIN(wsum[n]);
    SCHED();
    if (stg) issue1(kb + 5, 3);
    WAITL0();
    if (kb <= 3) WAITV(12); else if (kb == 4) WAITV(9); else if (kb == 5) WAITV(5); else if (kb == 6) WAITV(1);
    if (kb < 7) BAR();
    SCHED();
  }
  STAMP(ws, 0, 2);
  {
    const float a1 = s1[0] + s1[1], a2 = s2[0] + s2[1];
    float mean = shift + a1 * (1.f / 72.f);
    float m2 = a2 - a1 * a1 * (1.f / 72.f);
    float n = 72.f;
#pragma unroll
    for (int o = 1; o < 8; o <<= 1) {
      const float mo = __shfl_xor(mean, o), qo = __shfl_xor(m2, o);
      const float dl = mo - mean;
      m2 = m2 + qo + dl * dl * (n * 0.5f);
      mean = 0.5f * (mean + mo);
      n *= 2.f;
    }
    const float r = __builtin_amdgcn_rsqf(m2 * (1.f / 576.f) + LN_EPS);
    if (c8 == 0) *(f32x2*)(smem + K1_ST + (tid >> 3) * 8) = f32x2{r, mean * r};
#pragma unroll
    for (int n = 0; n < NF; ++n) {
      float w = wsum[n];
      w += __shfl_xor(w, 16); w += __shfl_xor(w, 32);
      if (q == 0) *(float*)(smem + K1_WS + sh * 576 + ((nf0 + n) * 16 + r16) * 4) = w;
    }
  }
  WAITL0();
  BAR();
  f32x2 rs[8];
  {
    const char* p = smem + K1_ST + (32 * sh + 8 * q) * 8;
#pragma unroll
    for (int i = 0; i < 4; ++i) {
      f32x4 v = *(const f32x4*)(p + i * 16);
      rs[2 * i] = f32x2{v[0], v[1]}; rs[2 * i + 1] = f32x2{v[2], v[3]};
    }
  }
  const float sgn = et < 4 ? 1.f : -1.f;
  const int kt = (et < 4 ? 0 : 16) + (st >> 1), ksg = (st & 1) * 2 + sh;
#pragma unroll
  for (int n = 0; n < NF; ++n) {
    const int el = (nf0 + n) * 16 + r16;
    const float ws = *(const float*)(smem + K1_WS + el * 4) + *(const float*)(smem + K1_WS + 576 + el * 4);
    float o[8];
#pragma unroll
    for (int m = 0; m < 2; ++m)
#pragma unroll
      for (int j = 0; j < 4; ++j) o[4 * m + j] = sgn * (acc[m][n][j] * rs[4 * m + j][0] - rs[4 * m + j][1] * ws);
    u32x4 pk = {pk_bf16(o[0], o[1]), pk_bf16(o[2], o[3]), pk_bf16(o[4], o[5]), pk_bf16(o[6], o[7])};
    const int hi = el >= 72 ? 1 : 0, e72 = el - 72 * hi;
    const int etg = (et & 3) * 2 + hi;
    const unsigned inner = e72 < 64 ? (unsigned)((e72 >> 4) * 1024 + q * 256 + (e72 & 15) * 16) : (unsigned)(4096 + q * 128 + (e72 & 7) * 16);
    const unsigned off = (unsigned)((etg * 32 + kt) * 4 + ksg) * 4608u + inner;
    __builtin_amdgcn_raw_buffer_store_b128(pk, rsO, off, 0, AUX_SC1);
  }
  WAITV(0);
  STAMP(ws, 0, 3);
}

extern "C" __global__ __launch_bounds__(512, 2) void k1_hid(const bf16_t* x, const bf16_t* wre, const bf16_t* wim, const bf16_t* cre, const bf16_t* cim, char* ws) {
  __shared__ __attribute__((aligned(16))) char smem[K1_LDS];
  const int wid = __builtin_amdgcn_readfirstlane(threadIdx.x >> 6), lane = threadIdx.x & 63;
  const int b = blockIdx.x, xcd = b & 7, i = b >> 3;
  const int st = xcd * 4 + (i & 3), et = i >> 2;
  const u32x4 rsX = mksrd(x, S_ * D_ * 2);
  const u32x4 rsW = mksrd(et < 4 ? wre : wim, D_ * D_ * 2);
  const u32x4 rsC = mksrd(wid < 4 ? cre : cim, S_ * S_ * 2);
  rsrc_t rsO = MKRS(ws + WS_BP, BP_BYTES);
  unsigned vhead;
  if (wid == 7) {
    const int l = lane < 32 ? lane : 31;
    vhead = (unsigned)((64 * st + 8 * et + (l >> 2)) * 1152 + (5 + (l & 3)) * 128);
  } else {
    int l = wid * 21 + (lane < 21 ? lane : 20);
    l = l < 144 ? l : 143;
    vhead = (unsigned)(((et & 3) * 144 + 36 * (i & 3) + (l >> 2)) * 1152 + (5 + (l & 3)) * 128);
  }
  const int w4 = wid & 3;
  if (wid == 0 || wid == 7) k1_body<3>(smem, rsX, rsW, rsC, rsO, st, et, wid, lane, wid == 0 ? 0 : 6, vhead, wid == 7, ws);
  else k1_body<2>(smem, rsX, rsW, rsC, rsO, st, et, wid, lane, wid < 4 ? 1 + 2 * w4 : 2 * w4, vhead, false, ws);
}

constexpr int K2_SLOT = 16384 + 18432, K2_XOFF = 4 * K2_SLOT, K2_SCR = K2_XOFF + 9216, K2_LDS = K2_SCR + 256;
constexpr int K2_RSTR = 336, K2_RKQ = 64 * K2_RSTR, K2_SOFF = 4 * K2_RKQ;

extern "C" __global__ __launch_bounds__(512, 2) void k2_seq(const bf16_t* x, const bf16_t* cre, const bf16_t* cim, const bf16_t* wg, const bf16_t* wu, char* ws) {
  __shared__ __attribute__((aligned(16))) char smem[K2_LDS];
  const int wid = __builtin_amdgcn_readfirstlane(threadIdx.x >> 6), lane = threadIdx.x & 63;
  const int b = blockIdx.x, xcd = b & 7, i = b >> 3;
  const int mt = (xcd >> 1) * 8 + (i & 7), nt = (xcd & 1) * 4 + (i >> 3);
  const u32x4 rsRe = mksrd(cre, S_ * S_ * 2), rsIm = mksrd(cim, S_ * S_ * 2);
  const u32x4 rsB = mksrd(ws + WS_BP, BP_BYTES);
  const u32x4 rsX = mksrd(x, S_ * D_ * 2);
  const int q = lane >> 4, r16 = lane & 15, r8 = lane >> 3, c8 = lane & 7;
  const int mh = wid & 1, kq = wid >> 1;

  const unsigned va = (unsigned)(64 * mt + 8 * wid + r8) * 4096u + (unsigned)((c8 ^ r8) * 16);
  const unsigned vb = (unsigned)(wid * 1024 + lane * 16);
  const unsigned vb2 = (unsigned)(16384 + wid * 256 + lane * 16);
  const unsigned mA = lds_base(smem) + wid * 1024, mP = lds_base(smem) + wid * 256;
  auto issue1 = [&](int j, u32x4 rsA, int k) __attribute__((always_inline)) {
    const unsigned so = (unsigned)((j & 3) * K2_SLOT), m0 = mA + so, m1 = mP + so;
    const int sa = (j & 15) * 256, sb = (nt * 32 + j) * 18432;
    if (k == 0) GLDS(rsA, m0, 0, va, sa);
    if (k == 1) GLDS(rsA, m0, 8192, va, sa + 128);
    if (k == 2) GLDS(rsB, m0, 16384, vb, sb);
    if (k == 3) GLDS(rsB, m0, 16384 + 8192, vb, sb + 8192);
    if (k == 4) GLDS_N(0xffff, rsB, m1, 16384 + 16384, vb2, sb);
  };
  auto issue = [&](int j, u32x4 rsA) __attribute__((always_inline)) {
    issue1(j, rsA, 0); issue1(j, rsA, 1); issue1(j, rsA, 2); issue1(j, rsA, 3); issue1(j, rsA, 4);
  };
  const unsigned at0 = (unsigned)((kq >> 1) * 8192 + (32 * mh + r16) * 128 + ((((kq & 1) * 4 + q) ^ (r16 & 7)) * 16));
  const unsigned ae0 = (unsigned)(16384 + kq * 4608 + lane * 16);
  const unsigned aeh = (unsigned)(16384 + kq * 4608 + 4096 + q * 128 + (lane & 7) * 16);

  f32x4 acc[5][2];
#pragma unroll
  for (int f = 0; f < 5; ++f) { acc[f][0] = f32x4{0, 0, 0, 0}; acc[f][1] = f32x4{0, 0, 0, 0}; }
  struct Fr { bf16x8 t0, t1, e[5]; };
  Fr fr[2];
  auto rd = [&](Fr& f, int j, int part) __attribute__((always_inline)) {
    const char* sl = smem + (j & 3) * K2_SLOT;
    if (part == 0) { f.t0 = *(const bf16x8*)(sl + at0); f.t1 = *(const bf16x8*)(sl + at0 + 2048); }
    if (part == 1) { f.e[0] = *(const bf16x8*)(sl + ae0); f.e[1] = *(const bf16x8*)(sl + ae0 + 1024); }
    if (part == 2) { f.e[2] = *(const bf16x8*)(sl + ae0 + 2048); f.e[3] = *(const bf16x8*)(sl + ae0 + 3072); }
    if (part == 3) f.e[4] = *(const bf16x8*)(sl + aeh);
  };
  auto stage = [&](int j, auto P_, u32x4 rsA, auto MODE_, auto RD_) __attribute__((always_inline)) {
    constexpr int P = decltype(P_)::value, MODE = decltype(MODE_)::value, RD = decltype(RD_)::value;
    const Fr& c = fr[P];
    Fr& nx = fr[P ^ 1];
#pragma unroll
    for (int f = 0; f < 5; ++f) {
      if (MODE == 1) issue1(j + 4, rsA, f);
      if (MODE == 2 && f == 0) {
        const int o0 = wid * 1024 + lane * 16, o1 = 8192 + wid * 128 + lane * 16;
        const int row0 = o0 / 144, row1 = o1 / 144;
        GLDS(rsX, mA, K2_XOFF, (unsigned)((64 * mt + row0) * 1152 + nt * 144 + (o0 - row0 * 144)), 0);
        GLDS_N(0xff, rsX, lds_base(smem) + wid * 128, K2_XOFF + 8192, (unsigned)((64 * mt + row1) * 1152 + nt * 144 + (o1 - row1 * 144)), 0);
      }
      if (MODE == 2 && f == 2) {
        CODE_TOUCH("k2_seq", lds_base(smem) + K2_SCR, i, wid);
        int n = wid * 14 + (lane < 13 ? lane : 13);
        n = i * 108 + (n < 107 ? n : 107);
        const int row = n / 9;
        TOUCH(mksrd(wg, D_ * F_ * 2), lds_base(smem) + K2_SCR, (unsigned)(row * 9216 + xcd * 1152 + (n - row * 9) * 128));
      }
      SCHED();
      acc[f][0] = MFMA16(c.e[f], c.t0, acc[f][0]); acc[f][1] = MFMA16(c.e[f], c.t1, acc[f][1]);
      SCHED();
      if (RD && f < 4) rd(nx, j + 1, f);
      SCHED();
    }
    WAITL0();
  };
  using I0 = IC<0>; using I1 = IC<1>; using I2 = IC<2>;

  STAMP(ws, 1, 0);
  issue(0, rsRe); issue(1, rsRe); issue(2, rsRe); issue(3, rsRe);
  WAITV(15); BAR();
  STAMP(ws, 1, 1);
  rd(fr[0], 0, 0); rd(fr[0], 0, 1); rd(fr[0], 0, 2); rd(fr[0], 0, 3);
  WAITL0();
  WAITV(10); BAR();
  for (int j = 0; j < 12; j += 2) {
    stage(j, I0{}, rsRe, I1{}, I1{}); WAITV(10); BAR();
    stage(j + 1, I1{}, rsRe, I1{}, I1{}); WAITV(10); BAR();
  }
  for (int j = 12; j < 28; j += 2) {
    stage(j, I0{}, rsIm, I1{}, I1{}); WAITV(10); BAR();
    stage(j + 1, I1{}, rsIm, I1{}, I1{}); WAITV(10); BAR();
  }
  stage(28, I0{}, rsIm, I2{}, I1{}); WAITV(9); BAR();
  stage(29, I1{}, rsIm, I0{}, I1{}); WAITV(4); BAR();
  stage(30, I0{}, rsIm, I0{}, I1{});
  stage(31, I1{}, rsIm, I0{}, I0{});
  WAITV(2); BAR();
  STAMP(ws, 1, 2);
#pragma unroll
  for (int f = 0; f < 5; ++f)
#pragma unroll
    for (int tf = 0; tf < 2; ++tf)
      *(f32x4*)(smem + kq * K2_RKQ + (32 * mh + 16 * tf + r16) * K2_RSTR + (16 * f + 4 * q) * 4) = acc[f][tf];
  WAITL0(); BAR();
  const rsrc_t rsH = MKRS(ws + WS_HT, HT_BYTES);
  auto item = [&](int U) {
    const int t = lane;
    f32x4 lo = {0, 0, 0, 0}, hi = {0, 0, 0, 0};
#pragma unroll
    for (int k = 0; k < 4; ++k) {
      const char* p = smem + k * K2_RKQ + t * K2_RSTR + U * 32;
      lo += *(const f32x4*)p; hi += *(const f32x4*)(p + 16);
    }
    const u32x4 xv = *(const u32x4*)(smem + K2_XOFF + t * 144 + U * 16);
    u32x4 hv = {pk_bf16(lo[0] + bf_lo(xv[0]), lo[1] + bf_hi(xv[0])), pk_bf16(lo[2] + bf_lo(xv[1]), lo[3] + bf_hi(xv[1])),
                pk_bf16(hi[0] + bf_lo(xv[2]), hi[1] + bf_hi(xv[2])), pk_bf16(hi[2] + bf_lo(xv[3]), hi[3] + bf_hi(xv[3]))};
    float v[8] = {bf_lo(hv[0]), bf_hi(hv[0]), bf_lo(hv[1]), bf_hi(hv[1]), bf_lo(hv[2]), bf_hi(hv[2]), bf_lo(hv[3]), bf_hi(hv[3])};
    float m = 0.f;
#pragma unroll
    for (int k = 0; k < 8; ++k) m += v[k];
    m *= 0.125f;
    float m2 = 0.f;
#pragma unroll
    for (int k = 0; k < 8; ++k) { const float d = v[k] - m; m2 += d * d; }
    *(f32x2*)(smem + K2_SOFF + (t * 9 + U) * 8) = f32x2{m, m2};
    const int Ug = nt * 9 + U, tg = 64 * mt + t;
    __builtin_amdgcn_raw_buffer_store_b128(hv, rsH, (unsigned)(((tg >> 4) * 72 + Ug) * 256 + (tg & 15) * 16), 0, AUX_SC1);
  };
  item(wid);
  if (wid == 0) item(8);
  WAITL0(); BAR();
  if (wid == 0) {
    const char* p = smem + K2_SOFF + lane * 72;
    f32x2 s[9];
    float m = 0.f;
#pragma unroll
    for (int k = 0; k < 9; ++k) { s[k] = *(const f32x2*)(p + k * 8); m += s[k][0]; }
    m *= (1.f / 9.f);
    float m2 = 0.f;
#pragma unroll
    for (int k = 0; k < 9; ++k) { const float d = s[k][0] - m; m2 += s[k][1] + 8.f * d * d; }
    const rsrc_t rsS = MKRS(ws + WS_ST, ST_BYTES);
    __builtin_amdgcn_raw_buffer_store_b64(__builtin_bit_cast(u32x2, f32x2{m, m2}), rsS, (unsigned)(((64 * mt + lane) * 8 + nt) * 8), 0, AUX_SC1);
  }
  STAMP(ws, 1, 3);
}

constexpr int KF_RING = 92160, KF_ST = KF_RING + 65536, KF_SCR = KF_ST + 640, KF_LDS = 163840;
#define EPI 8
#define PB1 1
#define ROT 0
#define WG0 10
#define WPG 1
#define PFINE 0
#define PG_N 12
#define PU_N 12
#define PB_N 10

#define MFMA_ACC(acc, a, b) asm volatile("v_mfma_f32_16x16x32_bf16 %0, %1, %2, %0" : "+v"(acc) : "v"(a), "v"(b))
#define ACC_READY(A) asm volatile("s_nop 7\n\ts_nop 7" : "+v"(A[0][0]), "+v"(A[0][1]), "+v"(A[0][2]), "+v"(A[0][3]), "+v"(A[0][4]), \
    "+v"(A[1][0]), "+v"(A[1][1]), "+v"(A[1][2]), "+v"(A[1][3]), "+v"(A[1][4]), "+v"(A[2][0]), "+v"(A[2][1]), "+v"(A[2][2]), "+v"(A[2][3]), \
    "+v"(A[2][4]), "+v"(A[3][0]), "+v"(A[3][1]), "+v"(A[3][2]), "+v"(A[3][3]), "+v"(A[3][4]))
#define DSR128(dst, addr, off) asm volatile("ds_read_b128 %0, %1 offset:%2" : "+v"(dst) : "v"(addr), "i"(off))
#define DSRTR(dst, addr, off) asm volatile("ds_read_b64_tr_b16 %0, %1 offset:%2" : "+v"(dst) : "v"(addr), "i"(off))

extern "C" __global__ __launch_bounds__(512, 2) void k3_ffn(const bf16_t* wg, const bf16_t* wu, const bf16_t* wd, char* ws) {
  __shared__ __attribute__((aligned(16))) char smem[KF_LDS];
  const int wid = __builtin_amdgcn_readfirstlane(threadIdx.x >> 6), lane = threadIdx.x & 63;
  const int b = blockIdx.x, xcd = b & 7, i = b >> 3;
  const u32x4 rsG = mksrd(wg, D_ * F_ * 2), rsU = mksrd(wu, D_ * F_ * 2), rsD = mksrd(wd, F_ * D_ * 2);
  const unsigned lds0 = lds_base(smem);
  if (b >= 234) {
    STAMP(ws, 2, 0); STAMP(ws, 2, 1);
    const int n0 = ((b - 234) * 8 + wid) * 64;
#pragma unroll 1
    for (int r = 0; r < 9; ++r) {
      const int n = r * 11264 + n0;
      if (n < 13824) TOUCH(rsG, lds0 + wid * 256, (unsigned)((27648 + n + lane) * 128));
      else if (n < 55296) TOUCH(rsU, lds0 + wid * 256, (unsigned)((n - 13824 + lane) * 128));
      else if (n < 96768) TOUCH(rsD, lds0 + wid * 256, (unsigned)((n - 55296 + lane) * 128));
    }
    WAITV(0);
    STAMP(ws, 2, 2); STAMP(ws, 2, 3); STAMP(ws, 2, 4); STAMP(ws, 2, 5);
    return;
  }
  const int id = xcd * 29 + (xcd < 2 ? xcd : 2) + i;
  const int tt = id < 130 ? id / 5 : (id - 130) >> 2, gp = id < 130 ? id - tt * 5 : 5 + ((id - 130) & 3);
  const u32x4 rsH = mksrd(ws + WS_HT, HT_BYTES), rsS = mksrd(ws + WS_ST, ST_BYTES);
  const rsrc_t rsP = MKRS(ws + WS_P, P_BYTES);
  STAMP(ws, 2, 0);
  WST(0);
  const int q = lane >> 4, r16 = lane & 15, r8 = lane >> 3, c8 = lane & 7, a4 = r16 >> 2, p4 = r16 & 3;
  const unsigned ring = lds0 + KF_RING + wid * 8192;
  unsigned vW[4];
#pragma unroll
  for (int j = 0; j < 4; ++j)
    vW[j] = (unsigned)((r8 + 8 * j) * 9216 + gp * 1024 + wid * 128 + ((c8 ^ (r8 & 2) ^ ((j & 1) * 4)) * 16) - j * 1024);
  auto ld = [&](u32x4 rs, int soff, int j) __attribute__((always_inline)) { RING_LD(rs, j * 1024, vW[j], soff); };
  auto ld4 = [&](u32x4 rs, int slot, int soff) __attribute__((always_inline)) {
    RING_M0(ring, slot * 4096);
    asm volatile("s_nop 0");
#pragma unroll
    for (int j = 0; j < 4; ++j) ld(rs, soff, j);
  };
  {
    f32x4 sp;
    const int tok = (wid < 5 ? wid : 4) * 16 + (lane >> 2);
    {
      int tg = tt * 80 + tok;
      tg = tg < S_ ? tg : S_ - 1;
      GLD(sp, rsS, (unsigned)(tg * 64 + (lane & 3) * 16));
    }
    BAR();
    u32x4 hv[12];
    const int r0 = ROT ? (gp < 5 ? gp : gp - 5) : 0;
    auto rot = [&](int k) { return k + r0 < 5 ? k + r0 : k + r0 - 5; };
    auto tile = [&](int T) { T += tt * 5; return (unsigned)((T < 128 ? T : 127) * 18432 + lane * 16); };
#pragma unroll
    for (int k = 0; k < 5; ++k) { GLD(hv[2 * k], rsH, tile(rot(k)) + wid * 1024); GLD(hv[2 * k + 1], rsH, tile(rot(k)) + wid * 1024 + 8192); }
    GLD(hv[10], rsH, tile(wid >> 1) + (16 + (wid & 1)) * 1024);
    GLD(hv[11], rsH, tile(4) + (16 + (wid & 1)) * 1024);
    ld4(rsG, 0, 0);
    LANDED(16, sp);
    {
      float m = sp[0] + sp[2];
      m += __shfl_xor(m, 1); m += __shfl_xor(m, 2);
      m *= 0.125f;
      const float d0 = sp[0] - m, d1 = sp[2] - m;
      float m2 = sp[1] + sp[3] + 72.f * (d0 * d0 + d1 * d1);
      m2 += __shfl_xor(m2, 1); m2 += __shfl_xor(m2, 2);
      const float r = __builtin_amdgcn_rsqf(m2 * (1.f / 576.f) + LN_EPS);
      if ((lane & 3) == 0 && wid < 5) *(f32x2*)(smem + KF_ST + tok * 8) = f32x2{r, -m * r};
    }
    WAITL0(); BAR();
    WST(1);
    f32x2 st[6];
#pragma unroll
    for (int k = 0; k < 5; ++k) st[k] = *(const f32x2*)(smem + KF_ST + (rot(k) * 16 + r16) * 8);
    const f32x2 st4 = *(const f32x2*)(smem + KF_ST + (64 + r16) * 8);
    st[5] = *(const f32x2*)(smem + KF_ST + ((wid >> 1) * 16 + r16) * 8);
    auto put = [&](u32x4 v, f32x2 c, int T, int s) __attribute__((always_inline)) {
      u32x4 o;
#pragma unroll
      for (int k = 0; k < 4; ++k) o[k] = pk_bf16(__builtin_fmaf(bf_lo(v[k]), c[0], c[1]), __builtin_fmaf(bf_hi(v[k]), c[0], c[1]));
      *(u32x4*)(smem + T * 18432 + s * 1024 + lane * 16) = o;
    };
    LANDED(15, hv[0]); put(hv[0], st[0], rot(0), wid); LANDED(14, hv[1]); put(hv[1], st[0], rot(0), wid + 8);
    LANDED(13, hv[2]); put(hv[2], st[1], rot(1), wid); LANDED(12, hv[3]); put(hv[3], st[1], rot(1), wid + 8);
    LANDED(11, hv[4]); put(hv[4], st[2], rot(2), wid); LANDED(10, hv[5]); put(hv[5], st[2], rot(2), wid + 8);
    LANDED(9, hv[6]); put(hv[6], st[3], rot(3), wid); LANDED(8, hv[7]); put(hv[7], st[3], rot(3), wid + 8);
    LANDED(7, hv[8]); put(hv[8], st[4], rot(4), wid); LANDED(6, hv[9]); put(hv[9], st[4], rot(4), wid + 8);
    LANDED(5, hv[10]);
    LANDED(4, hv[11]);
    ld4(rsG, 1, 294912);
    put(hv[10], st[5], wid >> 1, 16 + (wid & 1));
    if (wid < 2) put(hv[11], st4, 4, 16 + wid);
    WAITV(4); WAITL0(); BAR();
  }
  STAMP(ws, 2, 1);
  WST(2);

  unsigned aA[4], aB[4];
#pragma unroll
  for (int c = 0; c < 4; ++c) {
    aA[c] = ring + (unsigned)((8 * q + a4) * 128 + ((c ^ ((a4 >> 1) | ((q & 1) << 1))) * 32) + p4 * 8);
  }
  unsigned at0 = lds0 + (unsigned)(lane * 16), at1 = at0 + 3 * 18432;
  s16x4 wl[2][4], wh[2][4];
  bf16x8 tk[5];
  auto fresh = [&]() __attribute__((always_inline)) {
#pragma unroll
    for (int c = 0; c < 4; ++c) asm volatile("" : "=v"(wl[0][c]), "=v"(wh[0][c]), "=v"(wl[1][c]), "=v"(wh[1][c]));
#pragma unroll
    for (int t = 0; t < 5; ++t) asm volatile("" : "=v"(tk[t]));
  };
  fresh();
  auto rdw = [&](int buf, const unsigned(&a)[4], int hi) __attribute__((always_inline)) {
#pragma unroll
    for (int c = 0; c < 4; ++c) { DSRTR(wl[buf][c], a[c], buf * 4096); DSRTR(wh[buf][c], a[c], buf * 4096 + hi); }
  };
  auto rdt = [&](int t, int str, int off) __attribute__((always_inline)) {
    if (t < 3) DSR128(tk[t], at0, t * str + off); else DSR128(tk[t], at1, (t - 3) * str + off);
  };
#define LANDED_L(n) asm volatile("s_waitcnt lgkmcnt(%0)" ::"n"(n))
#define LANDED_W(n, buf) asm volatile("s_waitcnt lgkmcnt(%12)" : "+v"(wl[buf][0]), "+v"(wl[buf][1]), "+v"(wl[buf][2]), "+v"(wl[buf][3]), \
    "+v"(wh[buf][0]), "+v"(wh[buf][1]), "+v"(wh[buf][2]), "+v"(wh[buf][3]), "+v"(tk[0]), "+v"(tk[1]), "+v"(tk[2]), "+v"(tk[3]) : "n"(n))
#define LANDED_WB(buf) asm volatile("s_waitcnt lgkmcnt(0)" : "+v"(wl[buf][0]), "+v"(wl[buf][1]), "+v"(wl[buf][2]), "+v"(wl[buf][3]), \
    "+v"(wh[buf][0]), "+v"(wh[buf][1]), "+v"(wh[buf][2]), "+v"(wh[buf][3]))
#define LANDED_T(n, t) asm volatile("s_waitcnt lgkmcnt(%1)" : "+v"(tk[t]) : "n"(n))
  auto opnd = [&](int buf, int c) __attribute__((always_inline)) -> bf16x8 {
    return __builtin_shufflevector(wl[buf][c], wh[buf][c], 0, 1, 2, 3, 4, 5, 6, 7);
  };
  auto step = [&](f32x4(&acc)[4][5], auto P_, auto LD_, auto NW_, auto NT_, int str, int toff, u32x4 rs, int soff, auto gap) __attribute__((always_inline)) {
    constexpr int P = decltype(P_)::value, NW = decltype(NW_)::value;
    constexpr bool LD = decltype(LD_)::value, NT = decltype(NT_)::value;
    constexpr int W16 = (16 - WG0) * WPG < 8 ? (16 - WG0) * WPG : 8;
    if (LD) RING_M0(ring, P * 4096);
    LANDED_L(1);
#pragma unroll
    for (int t = 0; t < 5; ++t) {
      if (t == 4) LANDED_L((NT ? 4 : 0) + (NW ? W16 : 0));
      SCHED();
#pragma unroll
      for (int c = 0; c < 4; ++c) {
        const int g = t * 4 + c;
        MFMA_ACC(acc[c][t], opnd(P, c), tk[t]);
        SCHED();
        if (LD && t < 2 && (c & 1) == 0) { ld(rs, soff, 2 * t + (c >> 1)); SCHED(); }
        if (NW && g >= WG0 && g < WG0 + 8 / WPG) {
          if (g == WG0) { if (LD) WAITV(4); else WAITV(0); }
#pragma unroll
          for (int k = (g - WG0) * WPG; k < (g - WG0 + 1) * WPG; ++k) {
            const unsigned a = NW == 1 ? aA[k >> 1] : aB[k >> 1];
            if (k & 1) DSRTR(wh[P ^ 1][k >> 1], a, (P ^ 1) * 4096 + (NW == 1 ? 512 : 2048)); else DSRTR(wl[P ^ 1][k >> 1], a, (P ^ 1) * 4096);
          }
        }
        gap(g);
      }
      if (NT) rdt(t, str, toff);
    }
  };
  auto nogap = [&](int) __attribute__((always_inline)) {};
  using I0 = IC<0>; using I1 = IC<1>; using I2 = IC<2>;

  f32x4 ag[4][5], au[4][5];
#pragma unroll
  for (int c = 0; c < 4; ++c)
#pragma unroll
    for (int t = 0; t < 5; ++t) { ag[c][t] = f32x4{0, 0, 0, 0}; au[c][t] = f32x4{0, 0, 0, 0}; }
  ACC_READY(ag);

#pragma unroll
  for (int t = 0; t < 5; ++t) { rdt(t, 18432, 0); if (t == 3) rdw(0, aA, 512); }
  auto coarse = [&](int s, int n) __attribute__((always_inline)) {
    if (PRIO) asm volatile("s_cmp_lt_u32 %0, 4\n\ts_cbranch_scc1 2f\n\ts_cmp_lt_u32 %1, %2\n\ts_cbranch_scc1 1f\n\ts_setprio 0\n\ts_branch 2f\n1:\n\ts_setprio 1\n2:" ::"s"(wid), "s"(s), "s"(n) : "scc");
  };
#pragma clang loop unroll(disable)
  for (int s = 0; s < 16; s += 2) {
    coarse(s, PG_N);
    step(ag, I0{}, I1{}, I1{}, I1{}, 18432, 1024, rsG, (s + 2) * 294912, nogap);
    step(ag, I1{}, I1{}, I1{}, I1{}, 18432, 2048, rsG, (s + 3) * 294912, nogap);
    at0 += 2048; at1 += 2048;
  }
  at0 -= 16 * 1024; at1 -= 16 * 1024;
  step(ag, I0{}, I1{}, I1{}, I1{}, 18432, 17 * 1024, rsU, 0, nogap);
  step(ag, I1{}, I1{}, I1{}, I1{}, 18432, 0, rsU, 294912, nogap);
  WST(3);
  ACC_READY(ag); ACC_READY(au);
  float sx[5] = {0.f, 0.f, 0.f, 0.f, 0.f}, gs[80];
#pragma unroll
  for (int v = 0; v < 80; ++v) gs[v] = ag[(v >> 2) & 3][v >> 4][v & 3];
  auto ustep = [&](auto S_c) __attribute__((always_inline)) {
    constexpr int S = decltype(S_c)::value, NV = S < 8 ? 5 : 4, V0 = S < 8 ? 5 * S : 40 + 4 * (S - 8), NOP = 5 * NV;
    auto gap = [&](int g) __attribute__((always_inline)) {
      const int gi = g < 8 ? ((g & 1) ? g >> 1 : -1) : g - 4;
      if (gi < 0) return;
#pragma unroll
      for (int k = 0; k < NOP; ++k) {
        if (k * 16 / NOP != gi) continue;
        const int op = k / NV, e = k - op * NV, v = V0 + e;
        if (op == 0) asm volatile("v_mul_f32 %0, 0xbfb8aa3b, %1" : "+v"(sx[e]) : "v"(gs[v]));
        if (op == 1) asm volatile("v_exp_f32 %0, %0" : "+v"(sx[e]));
        if (op == 2) asm volatile("v_add_f32 %0, 1.0, %0" : "+v"(sx[e]));
        if (op == 3) asm volatile("v_rcp_f32 %0, %0" : "+v"(sx[e]));
        if (op == 4) asm volatile("v_mul_f32 %0, %0, %1" : "+v"(gs[v]) : "v"(sx[e]));
      }
      SCHED();
    };
    if (S == 0) PRIO_HI(wid);
    if (S == PU_N) PRIO_LO(wid);
    step(au, IC<S & 1>{}, I1{}, IC<(S < 17 ? 1 : 2)>{}, IC<(S < 17 ? 1 : 0)>{}, 18432, (S + 1) * 1024, S < 16 ? rsU : rsD,
         S < 16 ? (S + 2) * 294912 : (gp * 512 + wid * 64 + (S & 1) * 32) * 1152 + 1024 - wid * 128, gap);
  };
  ustep(IC<0>{}); ustep(IC<1>{}); ustep(IC<2>{}); ustep(IC<3>{}); ustep(IC<4>{}); ustep(IC<5>{});
  ustep(IC<6>{}); ustep(IC<7>{}); ustep(IC<8>{}); ustep(IC<9>{}); ustep(IC<10>{}); ustep(IC<11>{});
  ustep(IC<12>{}); ustep(IC<13>{}); ustep(IC<14>{}); ustep(IC<15>{});
  {
    int l = lane;
    asm volatile("" : "+v"(l));
    const int q_ = l >> 4, a4_ = (l >> 2) & 3, r8_ = l >> 3;
#pragma unroll
    for (int j = 0; j < 4; ++j) vW[j] = (unsigned)((r8_ + 8 * j) * 1152 + wid * 128 + (((l & 7) ^ (r8_ & 6)) * 16) - j * 1024);
#pragma unroll
    for (int c = 0; c < 4; ++c) aB[c] = ring + (unsigned)((4 * q_ + a4_) * 128 + ((c ^ ((2 * q_ + (a4_ >> 1)) & 3)) * 32) + (l & 3) * 8);
  }
  ustep(IC<16>{}); ustep(IC<17>{});
  ACC_READY(au);
  STAMP(ws, 2, 4);

  f32x4 o8[4][5], om[4][5];
  u32x4 act[2][5];
  const f32x4 zero4 = {0, 0, 0, 0};
  LANDED_WB(0);
  ld4(rsD, 0, gp * 512 * 1152);
  WAITV(4); rdw(1, aB, 2048);
  CODE_TOUCH("k4_sum", lds0 + KF_SCR, 0, wid);
#pragma unroll
  for (int pr = 0; pr < 2; ++pr) {
    if (pr == 1) {
      LANDED_WB(1);
      ld4(rsD, 1, (gp * 512 + 32) * 1152);
    }
#pragma unroll
    for (int t = 0; t < 5; ++t) {
      const float* g0 = gs + (t * 4 + 2 * pr) * 4;
      const f32x4 lo = f32x4{g0[0], g0[1], g0[2], g0[3]} * au[2 * pr][t], hi = f32x4{g0[4], g0[5], g0[6], g0[7]} * au[2 * pr + 1][t];
      act[pr][t] = u32x4{pk_bf16(lo[0], lo[1]), pk_bf16(lo[2], lo[3]), pk_bf16(hi[0], hi[1]), pk_bf16(hi[2], hi[3])};
      const bf16x8 f = __builtin_bit_cast(bf16x8, act[pr][t]);
#pragma unroll
      for (int c = 0; c < 4; ++c) o8[c][t] = MFMA16(opnd(pr, c), f, pr == 0 ? zero4 : o8[c][t]);
      SCHED();
    }
  }
  WST(4);
  BAR();
#pragma unroll
  for (int pr = 0; pr < 2; ++pr)
#pragma unroll
    for (int t = 0; t < 5; ++t) *(u32x4*)(smem + (t * 16 + 2 * wid + pr) * 1024 + lane * 16) = act[pr][t];
#pragma unroll
  for (int c = 0; c < 4; ++c)
#pragma unroll
    for (int t = 0; t < 5; ++t) om[c][t] = f32x4{0, 0, 0, 0};
  ACC_READY(om);
  WST(5);
  WAITL0(); BAR();
  WST(6);
  STAMP(ws, 2, 5);

  at0 = lds0 + (unsigned)(lane * 16); at1 = at0 + 3 * 16384;
  fresh();
  WAITV(4);
#pragma unroll
  for (int t = 0; t < 5; ++t) { rdt(t, 16384, 0); if (t == 3) rdw(0, aB, 2048); }
#pragma clang loop unroll(disable)
  for (int s = 0; s < 14; s += 2) {
    coarse(s, PB_N);
    step(om, I0{}, I1{}, I2{}, I1{}, 16384, 1024, rsD, (gp * 512 + (s + 2) * 32) * 1152, nogap);
    step(om, I1{}, I1{}, I2{}, I1{}, 16384, 2048, rsD, (gp * 512 + (s + 3) * 32) * 1152, nogap);
    at0 += 2048; at1 += 2048;
  }
  step(om, I0{}, I0{}, I2{}, I1{}, 16384, 1024, rsD, 0, nogap);
  step(om, I1{}, I0{}, I0{}, I0{}, 16384, 0, rsD, 0, nogap);
  PRIO_LO(wid);
  ACC_READY(om);
  WST(7);
  STAMP(ws, 2, 2);

  const int uo = (q & 1) * 2 + (q >> 1);
  auto emit = [&](f32x4 lo, f32x4 hi, int t, int E) __attribute__((always_inline)) {
    const auto x = __builtin_amdgcn_permlane16_swap(pk_bf16(lo[0], lo[1]), pk_bf16(hi[0], hi[1]), false, false);
    const auto y = __builtin_amdgcn_permlane16_swap(pk_bf16(lo[2], lo[3]), pk_bf16(hi[2], hi[3]), false, false);
    const int T16 = tt * 5 + t;
    if (T16 < 128)
      __builtin_amdgcn_raw_buffer_store_b128(u32x4{x[0], y[0], x[1], y[1]}, rsP, (unsigned)(((gp * 128 + T16) * 72 + E * 4 + uo) * 256 + r16 * 16), 0, AUX_SC1);
  };
#pragma unroll
  for (int k = 0; k < EPI - 1; ++k) emit(om[(k & 1) * 2][k >> 1], om[(k & 1) * 2 + 1][k >> 1], k >> 1, 2 * wid + (k & 1));
  SCHED();
  BAR();
#pragma unroll
  for (int t = 0; t < 5; ++t) {
#pragma unroll
    for (int c = 0; c < 4; ++c) *(f32x4*)(smem + (wid * 20 + t * 4 + c) * 1024 + lane * 16) = o8[c][t];
    SCHED();
  }
  WAITL0(); BAR();
#pragma unroll
  for (int k = EPI - 1; k < 10; ++k) emit(om[(k & 1) * 2][k >> 1], om[(k & 1) * 2 + 1][k >> 1], k >> 1, 2 * wid + (k & 1));
  SCHED();
  WST(8);
  WST(9);
  auto unit = [&](int u) __attribute__((always_inline)) {
    const char* p = smem + u * 2048 + lane * 16;
    f32x4 lo = {0, 0, 0, 0}, hi = {0, 0, 0, 0};
#pragma unroll
    for (int k = 0; k < 8; ++k) { lo += *(const f32x4*)(p + k * 20480); hi += *(const f32x4*)(p + k * 20480 + 1024); }
    emit(lo, hi, u >> 1, 16 + (u & 1));
  };
  unit(wid);
  if (wid < 2) unit(8 + wid);
  WST(10);
  WAITV(0);
  WST(11);
  WST_OUT(ws, 12);
  STAMP(ws, 2, 3);
}

extern "C" __global__ __launch_bounds__(576) void k4_sum(char* ws, bf16_t* out) {
  STAMP(ws, 3, 0);
  const int wv = threadIdx.x >> 6, lane = threadIdx.x & 63;
  const int T16 = blockIdx.x >> 1, t = (blockIdx.x & 1) * 8 + (lane >> 3), U = wv * 8 + (lane & 7);
  const rsrc_t rsP = MKRS(ws + WS_P, P_BYTES), rsH = MKRS(ws + WS_HT, HT_BYTES), rsO = MKRS(out, S_ * D_ * 2);
  const unsigned off = (unsigned)((T16 * 72 + U) * 256 + t * 16);
  u32x4 p[9];
#pragma unroll
  for (int g = 0; g < 9; ++g) p[g] = __builtin_amdgcn_raw_buffer_load_b128(rsP, off, g * (int)HT_BYTES, 0);
  u32x4 hv = __builtin_amdgcn_raw_buffer_load_b128(rsH, off, 0, 0);
  STAMP(ws, 3, 1);
  float a[8];
#pragma unroll
  for (int k = 0; k < 4; ++k) { a[2 * k] = bf_lo(p[0][k]); a[2 * k + 1] = bf_hi(p[0][k]); }
#pragma unroll
  for (int g = 1; g < 9; ++g)
#pragma unroll
    for (int k = 0; k < 4; ++k) { a[2 * k] += bf_lo(p[g][k]); a[2 * k + 1] += bf_hi(p[g][k]); }
  u32x4 ov;
#pragma unroll
  for (int k = 0; k < 4; ++k) ov[k] = pk_bf16(a[2 * k] + bf_lo(hv[k]), a[2 * k + 1] + bf_hi(hv[k]));
  STAMP(ws, 3, 2);
  __builtin_amdgcn_raw_buffer_store_b128(ov, rsO, (unsigned)((T16 * 16 + t) * 1152 + U * 16), 0, K4_AUX);
  STAMP(ws, 3, 3);
}

extern "C" void kernel_launch(void* const* d_in, const int* in_sizes, int n_in, void* d_out, int out_size, void* d_ws, size_t ws_size,
                              hipStream_t stream) {
  const bf16_t* x = (const bf16_t*)d_in[0];
  const bf16_t* cre = (const bf16_t*)d_in[1];
  const bf16_t* cim = (const bf16_t*)d_in[2];
  const bf16_t* wre = (const bf16_t*)d_in[3];
  const bf16_t* wim = (const bf16_t*)d_in[4];
  const bf16_t* wg = (const bf16_t*)d_in[6];
  const bf16_t* wu = (const bf16_t*)d_in[7];
  const bf16_t* wd = (const bf16_t*)d_in[8];
  char* ws = (char*)d_ws;
  k1_hid<<<256, 512, 0, stream>>>(x, wre, wim, cre, cim, ws);
  k2_seq<<<256, 512, 0, stream>>>(x, cre, cim, wg, wu, ws);
  k3_ffn<<<256, 512, 0, stream>>>(wg, wu, wd, ws);
  k4_sum<<<256, 576, 0, stream>>>(ws, (bf16_t*)d_out);
}
```

```cpp
#define DIAG
#define QSPIN 0
#define K1_TOP 0
#define QABS 7
#define QAVG 0
#define NMAIN 234
#include <hip/hip_runtime.h>
#include <stdint.h>

typedef unsigned short bf16_t;
typedef short bf16x8 __attribute__((ext_vector_type(8)));
typedef short s16x4 __attribute__((ext_vector_type(4)));
typedef float f32x4 __attribute__((ext_vector_type(4)));
typedef float f32x2 __attribute__((ext_vector_type(2)));
typedef unsigned u32x4 __attribute__((ext_vector_type(4)));
typedef unsigned u32x2 __attribute__((ext_vector_type(2)));
typedef __bf16 bf2_t __attribute__((ext_vector_type(2)));
typedef __amdgpu_buffer_rsrc_t rsrc_t;

constexpr int S_ = 2048, D_ = 576, F_ = 4608;
constexpr float LN_EPS = 1e-5f;

constexpr unsigned WS_BP = 0;
constexpr unsigned BP_BYTES = 8u * 32u * 18432u;
constexpr unsigned WS_HT = WS_BP + BP_BYTES;
constexpr unsigned HT_BYTES = 128u * 72u * 256u;
constexpr unsigned WS_ST = WS_HT + HT_BYTES;
constexpr unsigned ST_BYTES = 2048u * 8u * 8u;
constexpr unsigned WS_P = WS_ST + ST_BYTES;
constexpr unsigned P_BYTES = 9u * HT_BYTES;

#define LDSP(p) ((__attribute__((address_space(3))) void*)(p))
#define MKRS(p, n) __builtin_amdgcn_make_buffer_rsrc((void*)(p), 0, (n), 0x00020000)
#define GLDS(rs, m0base, imm, voff, soff)                                                              \
  asm volatile("s_add_u32 m0, %0, %1\n\ts_nop 0\n\tbuffer_load_dwordx4 %2, %3, %4 offen lds"          \
               ::"s"(m0base), "i"(imm), "v"(voff), "s"(rs), "s"(soff) : "memory", "scc")
#define GLDS_N(mask, rs, m0base, imm, voff, soff)                                                      \
  asm volatile("s_add_u32 m0, %0, %1\n\ts_mov_b64 exec, " #mask "\n\tbuffer_load_dwordx4 %2, %3, %4 offen lds\n\ts_mov_b64 exec, -1" \
               ::"s"(m0base), "i"(imm), "v"(voff), "s"(rs), "s"(soff) : "memory", "scc")
#define GLDS_36(rs, m0base, imm, voff, soff)                                                           \
  asm volatile("s_add_u32 m0, %0, %1\n\ts_mov_b32 exec_hi, 0xf\n\tbuffer_load_dwordx4 %2, %3, %4 offen lds\n\ts_mov_b32 exec_hi, -1" \
               ::"s"(m0base), "i"(imm), "v"(voff), "s"(rs), "s"(soff) : "memory", "scc")
#define RING_M0(m0base, imm) asm volatile("s_add_u32 m0, %0, %1" ::"s"(m0base), "i"(imm) : "scc")
#define RING_LD(rs, off, voff, soff) asm volatile("buffer_load_dwordx4 %0, %1, %2 offen offset:%3 lds" ::"v"(voff), "s"(rs), "s"(soff), "i"(off) : "memory")
#define TOUCH(rs, mscr, voff)                                                                          \
  asm volatile("s_mov_b32 m0, %0\n\ts_nop 0\n\tbuffer_load_dword %1, %2, 0 offen lds" ::"s"(mscr), "v"(voff), "s"(rs) : "memory")
#define TOUCHS(rs, mscr, voff, soff)                                                                   \
  asm volatile("s_mov_b32 m0, %0\n\ts_nop 0\n\tbuffer_load_dword %1, %2, %3 offen lds" ::"s"(mscr), "v"(voff), "s"(rs), "s"(soff) : "memory")
__device__ __forceinline__ u32x4 mksrd(const void* p, unsigned n) {
  const unsigned long long a = (unsigned long long)p;
  return u32x4{(unsigned)a, (unsigned)(a >> 32) & 0xffffu, n, 0x00020000u};
}
#define CODE_ADDR(lo, hi, sym)                                                                         \
  asm volatile("s_getpc_b64 s[20:21]\n\ts_add_u32 %0, s20, " sym "@rel32@lo+4\n\ts_addc_u32 %1, s21, " sym "@rel32@hi+12" \
               : "=s"(lo), "=s"(hi)::"s20", "s21", "scc")
#define CODE_TOUCH(sym, mscr, i, wid)                                                                  \
  do {                                                                                                 \
    unsigned lo_, hi_, le_, he_;                                                                       \
    CODE_ADDR(lo_, hi_, sym); CODE_ADDR(le_, he_, "k4_sum");                                           \
    const unsigned n_ = le_ - lo_ + 1024u;                                                             \
    TOUCH((u32x4{lo_, hi_ & 0xffffu, n_ < 65536u ? n_ : 0u, 0x00020000u}), mscr, (unsigned)(((i) * 8 + (wid)) * 128)); \
  } while (0)
#define CODE_TOUCH_TOP(sym, dst)                                                                       \
  asm volatile("s_getpc_b64 s[40:41]\n\ts_add_u32 s36, s40, " sym "@rel32@lo+4\n\ts_addc_u32 s37, s41, " sym "@rel32@hi+12\n\t"          \
               "s_getpc_b64 s[40:41]\n\ts_add_u32 s38, s40, k4_sum@rel32@lo+4\n\ts_sub_u32 s38, s38, s36\n\ts_add_u32 s38, s38, 0x400\n\t"  \
               "s_and_b32 s37, s37, 0xffff\n\ts_mov_b32 s39, 0x20000\n\ts_cmp_lt_u32 s38, 0x10000\n\ts_cselect_b32 s38, s38, 0\n\t"            \
               "s_and_b32 s40, %1, -8\n\tv_lshrrev_b32 %0, 6, %2\n\tv_add_u32 %0, s40, %0\n\tv_lshlrev_b32 %0, 7, %0\n\t"                    \
               "buffer_load_dword %0, %0, s[36:39], 0 offen"                                           \
               : "=&v"(dst) : "s"(blockIdx.x), "v"(threadIdx.x) : "s36", "s37", "s38", "s39", "s40", "s41", "scc", "memory")
__device__ __forceinline__ unsigned lds_base(char* smem) { return (unsigned)(size_t)LDSP(smem); }
#define GLD(dst, rs, voff) asm volatile("buffer_load_dwordx4 %0, %1, %2, 0 offen" : "=v"(dst) : "v"(voff), "s"(rs) : "memory")
#define LANDED(n, x) asm volatile("s_waitcnt vmcnt(" #n ")" : "+v"(x))
#define WAITV(n) asm volatile("s_waitcnt vmcnt(" #n ")" ::: "memory")
#define WAITL0() asm volatile("s_waitcnt lgkmcnt(0)" ::: "memory")
#define PIN(x) asm volatile("" : "+v"(x))
#define BAR() __builtin_amdgcn_s_barrier()
#define SCHED() __builtin_amdgcn_sched_barrier(0)
#define MFMA16(a, b, c) __builtin_amdgcn_mfma_f32_16x16x32_bf16((a), (b), (c), 0, 0, 0)
constexpr int AUX_SC1 = 16;
#define PRIO 1
#define PA_T 2
#define PB_T 2
#define PRIO_SET(wid, p) do { if (PRIO) asm volatile("s_cmp_lt_u32 %0, 4\n\ts_cbranch_scc1 1f\n\ts_setprio " #p "\n1:" ::"s"(wid) : "scc"); } while (0)
#define PRIO_HI(wid) PRIO_SET(wid, 1)
#define PRIO_LO(wid) PRIO_SET(wid, 0)
template <int V> struct IC { static constexpr int value = V; };
#define CS_T 3
#define CT2_MODE 1
#define CT3_MODE 2
#define K2_VAR 0
#define K4_AUX 0
#define TSX(var, j) do { } while (0)
#define TS_OUT(ws, kid, wid, lane, k, var) do { } while (0)
#define STAMP(ws, kid, slot) do { if (threadIdx.x == 0) { unsigned long long* p_ = (unsigned long long*)((ws) + (64u << 20) + (kid) * 65536 + blockIdx.x * 128 + (slot) * 16); \
  p_[0] = __builtin_amdgcn_s_memtime(); p_[1] = __builtin_amdgcn_s_memrealtime(); } } while (0)

#define WST(k) do { } while (0)
#define WST_OUT(ws, n) do { } while (0)

#define TS4(j) do { } while (0)
#define TS4_OUT(ws) do { } while (0)

__device__ __forceinline__ unsigned pk_bf16(float lo, float hi) {
  f32x2 v = {lo, hi};
  bf2_t b = __builtin_convertvector(v, bf2_t);
  return __builtin_bit_cast(unsigned, b);
}
__device__ __forceinline__ float bf_lo(unsigned w) { return __uint_as_float(w << 16); }
__device__ __forceinline__ float bf_hi(unsigned w) { return __uint_as_float(w & 0xffff0000u); }
__device__ __forceinline__ float dot_ones(unsigned w, float acc) {
  const bf2_t ones = {(__bf16)1.0f, (__bf16)1.0f};
  return __builtin_amdgcn_fdot2_f32_bf16(__builtin_bit_cast(bf2_t, w), ones, acc, false);
}
__device__ __forceinline__ float dot_frag(bf16x8 f, float acc) {
  u32x4 u = __builtin_bit_cast(u32x4, f);
  acc = dot_ones(u[0], acc); acc = dot_ones(u[1], acc); acc = dot_ones(u[2], acc); acc = dot_ones(u[3], acc);
  return acc;
}

constexpr int K1_SLOT = 8192 + 18432, K1_NS = 5, K1_ST = K1_NS * K1_SLOT, K1_WS = K1_ST + 512, K1_SCR = K1_WS + 1152, K1_LDS = K1_SCR + 256;

template <int NF>
__device__ __forceinline__ void k1_body(char* smem, u32x4 rsX, u32x4 rsW, u32x4 rsC, rsrc_t rsO, int st, int et, int wid, int lane, int nf0,
                                        unsigned vhead, bool headx, char* ws, unsigned ctop) {
  STAMP(ws, 0, 0);
  const int tid = wid * 64 + lane;
  const int sh = wid >> 2, q = lane >> 4, r16 = lane & 15, r8 = lane >> 3, c8 = lane & 7;
  const unsigned vx = (unsigned)(64 * st + 8 * wid + r8) * 1152u + (unsigned)((c8 ^ ((r8 & 3) | ((wid & 1) << 2))) * 16);
  const unsigned vw = (unsigned)((et & 3) * 144 + 8 * wid + r8) * 1152u + (unsigned)((c8 ^ r8) * 16);
  const int r2 = 128 + 2 * wid + r8;
  const unsigned vw2 = (unsigned)((et & 3) * 144 + r2) * 1152u + (unsigned)((c8 ^ (r2 & 7)) * 16);
  const unsigned lds0 = lds_base(smem);
  const unsigned mA = lds0 + wid * 1024, mP = lds0 + wid * 256;
  auto issue1 = [&](int kb, int k) __attribute__((always_inline)) {
    const int so = (kb % K1_NS) * K1_SLOT;
    if (k == 0) GLDS(rsX, mA, so, vx, kb * 128);
    if (k == 1) GLDS(rsW, mA, so + 8192, vw, kb * 128);
    if (k == 2) GLDS(rsW, mA, so + 16384, vw, kb * 128 + 64 * 1152);
    if (k == 3) GLDS_N(0xffff, rsW, mP, so + 24576, vw2, kb * 128);
  };
  auto issue = [&](int kb) __attribute__((always_inline)) { issue1(kb, 0); issue1(kb, 1); issue1(kb, 2); issue1(kb, 3); };
  unsigned ax[2];
#pragma unroll
  for (int m = 0; m < 2; ++m) {
    const int row = 32 * sh + 8 * (r16 >> 2) + 4 * m + (r16 & 3);
    const int key = (row & 3) | ((row >> 1) & 4);
    ax[m] = (unsigned)(row * 128 + ((q ^ key) * 16)) ^ (unsigned)(sh * 64);
  }
  const unsigned aw = (unsigned)(8192 + (nf0 * 16 + r16) * 128 + ((q ^ (r16 & 7)) * 16)) ^ (unsigned)(sh * 64);

  f32x4 acc[2][NF];
  float wsum[NF];
#pragma unroll
  for (int n = 0; n < NF; ++n) { wsum[n] = 0.f; acc[0][n] = f32x4{0, 0, 0, 0}; acc[1][n] = f32x4{0, 0, 0, 0}; }
  float shift = 0.f;
  f32x2 s1 = {0.f, 0.f}, s2 = {0.f, 0.f};

  struct Fr { u32x4 v; bf16x8 a[2][2], b[2][NF]; };
  Fr fr[2];
  auto rd = [&](Fr& f, int kb, int part) __attribute__((always_inline)) {
    const char* sl = smem + (kb % K1_NS) * K1_SLOT;
    if (part == 0) {
      f.a[0][0] = *(const bf16x8*)(sl + ax[0]); f.a[0][1] = *(const bf16x8*)(sl + ax[1]);
#pragma unroll
      for (int n = 0; n < NF; ++n) f.b[0][n] = *(const bf16x8*)(sl + aw + n * 2048);
    } else {
      f.v = *(const u32x4*)(sl + tid * 16);
      f.a[1][0] = *(const bf16x8*)(sl + (ax[0] ^ 64)); f.a[1][1] = *(const bf16x8*)(sl + (ax[1] ^ 64));
#pragma unroll
      for (int n = 0; n < NF; ++n) f.b[1][n] = *(const bf16x8*)(sl + ((aw ^ 64) + n * 2048));
    }
  };
  CODE_TOUCH("k1_hid", lds0 + K1_SCR, blockIdx.x >> 3, wid);
  issue(0);
  STAMP(ws, 0, 7);
  issue(1);
  TOUCH(headx ? rsX : rsW, lds0 + K1_SCR, vhead);
  issue(2); issue(3); issue(4);
  WAITV(17); BAR();
  asm volatile("" ::"v"(ctop));
  STAMP(ws, 0, 1);
  rd(fr[0], 0, 0); rd(fr[0], 0, 1);
  WAITL0();
  WAITV(13); BAR();
#pragma unroll
  for (int kb = 0; kb < 9; ++kb) {
    const Fr& c = fr[kb & 1];
    Fr& nx = fr[(kb & 1) ^ 1];
    const bool stg = kb + 5 < 9;
    if (stg) issue1(kb + 5, 0);
    if (kb == 4)
      TOUCH(rsC, lds0 + K1_SCR, (unsigned)(blockIdx.x * 32768 + (wid & 3) * 8192 + lane * 128));
    SCHED();
#pragma unroll
    for (int n = 0; n < NF; ++n) { acc[0][n] = MFMA16(c.a[0][0], c.b[0][n], acc[0][n]); acc[1][n] = MFMA16(c.a[0][1], c.b[0][n], acc[1][n]); }
    SCHED();
    if (kb < 8) rd(nx, kb + 1, 0);
    SCHED();
    if (stg) issue1(kb + 5, 1);
    SCHED();
    if (kb == 0) shift = bf_lo(c.v[0]);
#pragma unroll
    for (int i = 0; i < 4; ++i) {
      f32x2 d = {bf_lo(c.v[i]) - shift, bf_hi(c.v[i]) - shift};
      s1 += d; s2 += d * d;
    }
    PIN(s1); PIN(s2);
    SCHED();
    if (stg) issue1(kb + 5, 2);
    SCHED();
#pragma unroll
    for (int n = 0; n < NF; ++n) { acc[0][n] = MFMA16(c.a[1][0], c.b[1][n], acc[0][n]); acc[1][n] = MFMA16(c.a[1][1], c.b[1][n], acc[1][n]); }
    SCHED();
    if (kb < 8) rd(nx, kb + 1, 1);
    SCHED();
#pragma unroll
    for (int n = 0; n < NF; ++n) wsum[n] = dot_frag(c.b[0][n], wsum[n]);
#pragma unroll
    for (int n = 0; n < NF; ++n) PIN(wsum[n]);
    SCHED();
    if (stg) issue1(kb + 5, 3);
    WAITL0();
    if (kb <= 3) WAITV(12); else if (kb == 4) WAITV(9); else if (kb == 5) WAITV(5); else if (kb == 6) WAITV(1);
    if (kb < 7) BAR();
    SCHED();
  }
  STAMP(ws, 0, 2);
  {
    const float a1 = s1[0] + s1[1], a2 = s2[0] + s2[1];
    float mean = shift + a1 * (1.f / 72.f);
    float m2 = a2 - a1 * a1 * (1.f / 72.f);
    float n = 72.f;
#pragma unroll
    for (int o = 1; o < 8; o <<= 1) {
      const float mo = __shfl_xor(mean, o), qo = __shfl_xor(m2, o);
      const float dl = mo - mean;
      m2 = m2 + qo + dl * dl * (n * 0.5f);
      mean = 0.5f * (mean + mo);
      n *= 2.f;
    }
    const float r = __builtin_amdgcn_rsqf(m2 * (1.f / 576.f) + LN_EPS);
    if (c8 == 0) *(f32x2*)(smem + K1_ST + (tid >> 3) * 8) = f32x2{r, mean * r};
#pragma unroll
    for (int n = 0; n < NF; ++n) {
      float w = wsum[n];
      w += __shfl_xor(w, 16); w += __shfl_xor(w, 32);
      if (q == 0) *(float*)(smem + K1_WS + sh * 576 + ((nf0 + n) * 16 + r16) * 4) = w;
    }
  }
  WAITL0();
  BAR();
  f32x2 rs[8];
  {
    const char* p = smem + K1_ST + (32 * sh + 8 * q) * 8;
#pragma unroll
    for (int i = 0; i < 4; ++i) {
      f32x4 v = *(const f32x4*)(p + i * 16);
      rs[2 * i] = f32x2{v[0], v[1]}; rs[2 * i + 1] = f32x2{v[2], v[3]};
    }
  }
  const float sgn = et < 4 ? 1.f : -1.f;
  const int kt = (et < 4 ? 0 : 16) + (st >> 1), ksg = (st & 1) * 2 + sh;
#pragma unroll
  for (int n = 0; n < NF; ++n) {
    const int el = (nf0 + n) * 16 + r16;
    const float ws = *(const float*)(smem + K1_WS + el * 4) + *(const float*)(smem + K1_WS + 576 + el * 4);
    float o[8];
#pragma unroll
    for (int m = 0; m < 2; ++m)
#pragma unroll
      for (int j = 0; j < 4; ++j) o[4 * m + j] = sgn * (acc[m][n][j] * rs[4 * m + j][0] - rs[4 * m + j][1] * ws);
    u32x4 pk = {pk_bf16(o[0], o[1]), pk_bf16(o[2], o[3]), pk_bf16(o[4], o[5]), pk_bf16(o[6], o[7])};
    const int hi = el >= 72 ? 1 : 0, e72 = el - 72 * hi;
    const int etg = (et & 3) * 2 + hi;
    const unsigned inner = e72 < 64 ? (unsigned)((e72 >> 4) * 1024 + q * 256 + (e72 & 15) * 16) : (unsigned)(4096 + q * 128 + (e72 & 7) * 16);
    const unsigned off = (unsigned)((etg * 32 + kt) * 4 + ksg) * 4608u + inner;
    __builtin_amdgcn_raw_buffer_store_b128(pk, rsO, off, 0, AUX_SC1);
  }
  WAITV(0);
  STAMP(ws, 0, 3);
}

extern "C" __global__ __launch_bounds__(512, 2) void k1_hid(const bf16_t* x, const bf16_t* wre, const bf16_t* wim, const bf16_t* cre, const bf16_t* cim, char* ws) {
  __shared__ __attribute__((aligned(16))) char smem[K1_LDS];
  const int wid = __builtin_amdgcn_readfirstlane(threadIdx.x >> 6), lane = threadIdx.x & 63;
  const int b = blockIdx.x, xcd = b & 7, i = b >> 3;
  unsigned ctop = 0;
  const int st = xcd * 4 + (i & 3), et = i >> 2;
  const u32x4 rsX = mksrd(x, S_ * D_ * 2);
  const u32x4 rsW = mksrd(et < 4 ? wre : wim, D_ * D_ * 2);
  const u32x4 rsC = mksrd(wid < 4 ? cre : cim, S_ * S_ * 2);
  rsrc_t rsO = MKRS(ws + WS_BP, BP_BYTES);
  unsigned vhead;
  if (wid == 7) {
    const int l = lane < 32 ? lane : 31;
    vhead = (unsigned)((64 * st + 8 * et + (l >> 2)) * 1152 + (5 + (l & 3)) * 128);
  } else {
    int l = wid * 21 + (lane < 21 ? lane : 20);
    l = l < 144 ? l : 143;
    vhead = (unsigned)(((et & 3) * 144 + 36 * (i & 3) + (l >> 2)) * 1152 + (5 + (l & 3)) * 128);
  }
  const int w4 = wid & 3;
  if (wid == 0 || wid == 7) k1_body<3>(smem, rsX, rsW, rsC, rsO, st, et, wid, lane, wid == 0 ? 0 : 6, vhead, wid == 7, ws, ctop);
  else k1_body<2>(smem, rsX, rsW, rsC, rsO, st, et, wid, lane, wid < 4 ? 1 + 2 * w4 : 2 * w4, vhead, false, ws, ctop);
}

constexpr int K2_SLOT = 16384 + 18432, K2_XOFF = 4 * K2_SLOT, K2_SCR = K2_XOFF + 9216, K2_LDS = K2_SCR + 256;
constexpr int K2_RSTR = 336, K2_RKQ = 64 * K2_RSTR, K2_SOFF = 4 * K2_RKQ;

extern "C" __global__ __launch_bounds__(512, 2) void k2_seq(const bf16_t* x, const bf16_t* cre, const bf16_t* cim, const bf16_t* wg, const bf16_t* wu, char* ws) {
  __shared__ __attribute__((aligned(16))) char smem[K2_LDS];
  const int wid = __builtin_amdgcn_readfirstlane(threadIdx.x >> 6), lane = threadIdx.x & 63;
  const int b = blockIdx.x, xcd = b & 7, i = b >> 3;
  const int mt = (xcd >> 1) * 8 + (i & 7), nt = (xcd & 1) * 4 + (i >> 3);
  const u32x4 rsRe = mksrd(cre, S_ * S_ * 2), rsIm = mksrd(cim, S_ * S_ * 2);
  const u32x4 rsB = mksrd(ws + WS_BP, BP_BYTES);
  const u32x4 rsX = mksrd(x, S_ * D_ * 2);
  const int q = lane >> 4, r16 = lane & 15, r8 = lane >> 3, c8 = lane & 7;
  const int mh = wid & 1, kq = wid >> 1;

  const unsigned va = (unsigned)(64 * mt + 8 * wid + r8) * 4096u + (unsigned)((c8 ^ r8) * 16);
  const unsigned vb = (unsigned)(wid * 1024 + lane * 16);
  const unsigned vb2 = (unsigned)(16384 + wid * 256 + lane * 16);
  const unsigned mA = lds_base(smem) + wid * 1024, mP = lds_base(smem) + wid * 256;
  auto issue1 = [&](int j, u32x4 rsA, int k) __attribute__((always_inline)) {
    const unsigned so = (unsigned)((j & 3) * K2_SLOT), m0 = mA + so, m1 = mP + so;
    const int sa = (j & 15) * 256, sb = (nt * 32 + j) * 18432;
    if (k == 0) GLDS(rsA, m0, 0, va, sa);
    if (k == 1) GLDS(rsA, m0, 8192, va, sa + 128);
    if (k == 2) GLDS(rsB, m0, 16384, vb, sb);
    if (k == 3) GLDS(rsB, m0, 16384 + 8192, vb, sb + 8192);
    if (k == 4) GLDS_N(0xffff, rsB, m1, 16384 + 16384, vb2, sb);
  };
  auto issue = [&](int j, u32x4 rsA) __attribute__((always_inline)) {
    issue1(j, rsA, 0); issue1(j, rsA, 1); issue1(j, rsA, 2); issue1(j, rsA, 3); issue1(j, rsA, 4);
  };
  const unsigned at0 = (unsigned)((kq >> 1) * 8192 + (32 * mh + r16) * 128 + ((((kq & 1) * 4 + q) ^ (r16 & 7)) * 16));
  const unsigned ae0 = (unsigned)(16384 + kq * 4608 + lane * 16);
  const unsigned aeh = (unsigned)(16384 + kq * 4608 + 4096 + q * 128 + (lane & 7) * 16);

  f32x4 acc[5][2];
#pragma unroll
  for (int f = 0; f < 5; ++f) { acc[f][0] = f32x4{0, 0, 0, 0}; acc[f][1] = f32x4{0, 0, 0, 0}; }
  struct Fr { bf16x8 t0, t1, e[5]; };
  Fr fr[2];
  auto rd = [&](Fr& f, int j, int part) __attribute__((always_inline)) {
    const char* sl = smem + (j & 3) * K2_SLOT;
    if (part == 0) { f.t0 = *(const bf16x8*)(sl + at0); f.t1 = *(const bf16x8*)(sl + at0 + 2048); }
    if (part == 1) { f.e[0] = *(const bf16x8*)(sl + ae0); f.e[1] = *(const bf16x8*)(sl + ae0 + 1024); }
    if (part == 2) { f.e[2] = *(const bf16x8*)(sl + ae0 + 2048); f.e[3] = *(const bf16x8*)(sl + ae0 + 3072); }
    if (part == 3) f.e[4] = *(const bf16x8*)(sl + aeh);
  };
  auto stage = [&](int j, auto P_, u32x4 rsA, auto MODE_, auto RD_) __attribute__((always_inline)) {
    constexpr int P = decltype(P_)::value, MODE = decltype(MODE_)::value, RD = decltype(RD_)::value;
    const Fr& c = fr[P];
    Fr& nx = fr[P ^ 1];
#pragma unroll
    for (int f = 0; f < 5; ++f) {
      if (MODE == 1) issue1(j + 4, rsA, f);
      if (MODE == 2 && f == 0) {
        const int o0 = wid * 1024 + lane * 16, o1 = 8192 + wid * 128 + lane * 16;
        const int row0 = o0 / 144, row1 = o1 / 144;
        GLDS(rsX, mA, K2_XOFF, (unsigned)((64 * mt + row0) * 1152 + nt * 144 + (o0 - row0 * 144)), 0);
        GLDS_N(0xff, rsX, lds_base(smem) + wid * 128, K2_XOFF + 8192, (unsigned)((64 * mt + row1) * 1152 + nt * 144 + (o1 - row1 * 144)), 0);
      }
      if (MODE == 2 && f == 2) {
        CODE_TOUCH("k2_seq", lds_base(smem) + K2_SCR, i, wid);
        int n = wid * 14 + (lane < 13 ? lane : 13);
        n = i * 108 + (n < 107 ? n : 107);
        const int row = n / 9;
        TOUCH(mksrd(wg, D_ * F_ * 2), lds_base(smem) + K2_SCR, (unsigned)(row * 9216 + xcd * 1152 + (n - row * 9) * 128));
      }
      SCHED();
      acc[f][0] = MFMA16(c.e[f], c.t0, acc[f][0]); acc[f][1] = MFMA16(c.e[f], c.t1, acc[f][1]);
      SCHED();
      if (RD && f < 4) rd(nx, j + 1, f);
      SCHED();
    }
    WAITL0();
  };
  using I0 = IC<0>; using I1 = IC<1>; using I2 = IC<2>;

  STAMP(ws, 1, 0);
  issue(0, rsRe); issue(1, rsRe); issue(2, rsRe); issue(3, rsRe);
  WAITV(15); BAR();
  STAMP(ws, 1, 1);
  rd(fr[0], 0, 0); rd(fr[0], 0, 1); rd(fr[0], 0, 2); rd(fr[0], 0, 3);
  WAITL0();
  WAITV(10); BAR();
  for (int j = 0; j < 12; j += 2) {
    stage(j, I0{}, rsRe, I1{}, I1{}); WAITV(10); BAR();
    stage(j + 1, I1{}, rsRe, I1{}, I1{}); WAITV(10); BAR();
  }
  for (int j = 12; j < 28; j += 2) {
    stage(j, I0{}, rsIm, I1{}, I1{}); WAITV(10); BAR();
    stage(j + 1, I1{}, rsIm, I1{}, I1{}); WAITV(10); BAR();
  }
  stage(28, I0{}, rsIm, I2{}, I1{}); WAITV(9); BAR();
  stage(29, I1{}, rsIm, I0{}, I1{}); WAITV(4); BAR();
  stage(30, I0{}, rsIm, I0{}, I1{});
  stage(31, I1{}, rsIm, I0{}, I0{});
  WAITV(2); BAR();
  STAMP(ws, 1, 2);
#pragma unroll
  for (int f = 0; f < 5; ++f)
#pragma unroll
    for (int tf = 0; tf < 2; ++tf)
      *(f32x4*)(smem + kq * K2_RKQ + (32 * mh + 16 * tf + r16) * K2_RSTR + (16 * f + 4 * q) * 4) = acc[f][tf];
  WAITL0(); BAR();
  const rsrc_t rsH = MKRS(ws + WS_HT, HT_BYTES);
  auto item = [&](int U) {
    const int t = lane;
    f32x4 lo = {0, 0, 0, 0}, hi = {0, 0, 0, 0};
#pragma unroll
    for (int k = 0; k < 4; ++k) {
      const char* p = smem + k * K2_RKQ + t * K2_RSTR + U * 32;
      lo += *(const f32x4*)p; hi += *(const f32x4*)(p + 16);
    }
    const u32x4 xv = *(const u32x4*)(smem + K2_XOFF + t * 144 + U * 16);
    u32x4 hv = {pk_bf16(lo[0] + bf_lo(xv[0]), lo[1] + bf_hi(xv[0])), pk_bf16(lo[2] + bf_lo(xv[1]), lo[3] + bf_hi(xv[1])),
                pk_bf16(hi[0] + bf_lo(xv[2]), hi[1] + bf_hi(xv[2])), pk_bf16(hi[2] + bf_lo(xv[3]), hi[3] + bf_hi(xv[3]))};
    float v[8] = {bf_lo(hv[0]), bf_hi(hv[0]), bf_lo(hv[1]), bf_hi(hv[1]), bf_lo(hv[2]), bf_hi(hv[2]), bf_lo(hv[3]), bf_hi(hv[3])};
    float m = 0.f;
#pragma unroll
    for (int k = 0; k < 8; ++k) m += v[k];
    m *= 0.125f;
    float m2 = 0.f;
#pragma unroll
    for (int k = 0; k < 8; ++k) { const float d = v[k] - m; m2 += d * d; }
    *(f32x2*)(smem + K2_SOFF + (t * 9 + U) * 8) = f32x2{m, m2};
    const int Ug = nt * 9 + U, tg = 64 * mt + t;
    __builtin_amdgcn_raw_buffer_store_b128(hv, rsH, (unsigned)(((tg >> 4) * 72 + Ug) * 256 + (tg & 15) * 16), 0, AUX_SC1);
  };
  item(wid);
  if (wid == 0) item(8);
  WAITL0(); BAR();
  if (wid == 0) {
    const char* p = smem + K2_SOFF + lane * 72;
    f32x2 s[9];
    float m = 0.f;
#pragma unroll
    for (int k = 0; k < 9; ++k) { s[k] = *(const f32x2*)(p + k * 8); m += s[k][0]; }
    m *= (1.f / 9.f);
    float m2 = 0.f;
#pragma unroll
    for (int k = 0; k < 9; ++k) { const float d = s[k][0] - m; m2 += s[k][1] + 8.f * d * d; }
    const rsrc_t rsS = MKRS(ws + WS_ST, ST_BYTES);
    __builtin_amdgcn_raw_buffer_store_b64(__builtin_bit_cast(u32x2, f32x2{m, m2}), rsS, (unsigned)(((64 * mt + lane) * 8 + nt) * 8), 0, AUX_SC1);
  }
  STAMP(ws, 1, 3);
}

constexpr int KF_RING = 92160, KF_ST = KF_RING + 65536, KF_SCR = KF_ST + 640, KF_LDS = 163840;
#define EPI 8
#define PB1 1
#define ROT 0
#define WG0 10
#define WPG 1
#define PFINE 0
#define PG_N 12
#define PU_N 12
#define PB_N 10

#define MFMA_ACC(acc, a, b) asm volatile("v_mfma_f32_16x16x32_bf16 %0, %1, %2, %0" : "+v"(acc) : "v"(a), "v"(b))
#define ACC_READY(A) asm volatile("s_nop 7\n\ts_nop 7" : "+v"(A[0][0]), "+v"(A[0][1]), "+v"(A[0][2]), "+v"(A[0][3]), "+v"(A[0][4]), \
    "+v"(A[1][0]), "+v"(A[1][1]), "+v"(A[1][2]), "+v"(A[1][3]), "+v"(A[1][4]), "+v"(A[2][0]), "+v"(A[2][1]), "+v"(A[2][2]), "+v"(A[2][3]), \
    "+v"(A[2][4]), "+v"(A[3][0]), "+v"(A[3][1]), "+v"(A[3][2]), "+v"(A[3][3]), "+v"(A[3][4]))
#define DSR128(dst, addr, off) asm volatile("ds_read_b128 %0, %1 offset:%2" : "+v"(dst) : "v"(addr), "i"(off))
#define DSRTR(dst, addr, off) asm volatile("ds_read_b64_tr_b16 %0, %1 offset:%2" : "+v"(dst) : "v"(addr), "i"(off))

extern "C" __global__ __launch_bounds__(512, 2) void k3_ffn(const bf16_t* wg, const bf16_t* wu, const bf16_t* wd, char* ws) {
  __shared__ __attribute__((aligned(16))) char smem[KF_LDS];
  const int wid = __builtin_amdgcn_readfirstlane(threadIdx.x >> 6), lane = threadIdx.x & 63;
  const int b = blockIdx.x, xcd = b & 7, i = b >> 3;
  const u32x4 rsG = mksrd(wg, D_ * F_ * 2), rsU = mksrd(wu, D_ * F_ * 2), rsD = mksrd(wd, F_ * D_ * 2);
  const unsigned lds0 = lds_base(smem);
  if (b >= 234) {
    STAMP(ws, 2, 0); STAMP(ws, 2, 1);
    const int n0 = ((b - 234) * 8 + wid) * 64;
#pragma unroll 1
    for (int r = 0; r < 9; ++r) {
      const int n = r * 11264 + n0;
      if (n < 13824) TOUCH(rsG, lds0 + wid * 256, (unsigned)((27648 + n + lane) * 128));
      else if (n < 55296) TOUCH(rsU, lds0 + wid * 256, (unsigned)((n - 13824 + lane) * 128));
      else if (n < 96768) TOUCH(rsD, lds0 + wid * 256, (unsigned)((n - 55296 + lane) * 128));
    }
    WAITV(0);
    STAMP(ws, 2, 2); STAMP(ws, 2, 3); STAMP(ws, 2, 4); STAMP(ws, 2, 5);
    return;
  }
  const int id = xcd * 29 + (xcd < 2 ? xcd : 2) + i;
  const int tt = id < 130 ? id / 5 : (id - 130) >> 2, gp = id < 130 ? id - tt * 5 : 5 + ((id - 130) & 3);
  const u32x4 rsH = mksrd(ws + WS_HT, HT_BYTES), rsS = mksrd(ws + WS_ST, ST_BYTES);
  const rsrc_t rsP = MKRS(ws + WS_P, P_BYTES);
  STAMP(ws, 2, 0);
  WST(0);
  const int q = lane >> 4, r16 = lane & 15, r8 = lane >> 3, c8 = lane & 7, a4 = r16 >> 2, p4 = r16 & 3;
  const unsigned ring = lds0 + KF_RING + wid * 8192;
  unsigned vW[4];
#pragma unroll
  for (int j = 0; j < 4; ++j)
    vW[j] = (unsigned)((r8 + 8 * j) * 9216 + gp * 1024 + wid * 128 + ((c8 ^ (r8 & 2) ^ ((j & 1) * 4)) * 16) - j * 1024);
  auto ld = [&](u32x4 rs, int soff, int j) __attribute__((always_inline)) { RING_LD(rs, j * 1024, vW[j], soff); };
  auto ld4 = [&](u32x4 rs, int slot, int soff) __attribute__((always_inline)) {
    RING_M0(ring, slot * 4096);
    asm volatile("s_nop 0");
#pragma unroll
    for (int j = 0; j < 4; ++j) ld(rs, soff, j);
  };
  {
    f32x4 sp;
    const int tok = (wid < 5 ? wid : 4) * 16 + (lane >> 2);
    {
      int tg = tt * 80 + tok;
      tg = tg < S_ ? tg : S_ - 1;
      GLD(sp, rsS, (unsigned)(tg * 64 + (lane & 3) * 16));
    }
    BAR();
    u32x4 hv[12];
    const int r0 = ROT ? (gp < 5 ? gp : gp - 5) : 0;
    auto rot = [&](int k) { return k + r0 < 5 ? k + r0 : k + r0 - 5; };
    auto tile = [&](int T) { T += tt * 5; return (unsigned)((T < 128 ? T : 127) * 18432 + lane * 16); };
#pragma unroll
    for (int k = 0; k < 5; ++k) { GLD(hv[2 * k], rsH, tile(rot(k)) + wid * 1024); GLD(hv[2 * k + 1], rsH, tile(rot(k)) + wid * 1024 + 8192); }
    GLD(hv[10], rsH, tile(wid >> 1) + (16 + (wid & 1)) * 1024);
    GLD(hv[11], rsH, tile(4) + (16 + (wid & 1)) * 1024);
    ld4(rsG, 0, 0);
    LANDED(16, sp);
    {
      float m = sp[0] + sp[2];
      m += __shfl_xor(m, 1); m += __shfl_xor(m, 2);
      m *= 0.125f;
      const float d0 = sp[0] - m, d1 = sp[2] - m;
      float m2 = sp[1] + sp[3] + 72.f * (d0 * d0 + d1 * d1);
      m2 += __shfl_xor(m2, 1); m2 += __shfl_xor(m2, 2);
      const float r = __builtin_amdgcn_rsqf(m2 * (1.f / 576.f) + LN_EPS);
      if ((lane & 3) == 0 && wid < 5) *(f32x2*)(smem + KF_ST + tok * 8) = f32x2{r, -m * r};
    }
    WAITL0(); BAR();
    WST(1);
    f32x2 st[6];
#pragma unroll
    for (int k = 0; k < 5; ++k) st[k] = *(const f32x2*)(smem + KF_ST + (rot(k) * 16 + r16) * 8);
    const f32x2 st4 = *(const f32x2*)(smem + KF_ST + (64 + r16) * 8);
    st[5] = *(const f32x2*)(smem + KF_ST + ((wid >> 1) * 16 + r16) * 8);
    auto put = [&](u32x4 v, f32x2 c, int T, int s) __attribute__((always_inline)) {
      u32x4 o;
#pragma unroll
      for (int k = 0; k < 4; ++k) o[k] = pk_bf16(__builtin_fmaf(bf_lo(v[k]), c[0], c[1]), __builtin_fmaf(bf_hi(v[k]), c[0], c[1]));
      *(u32x4*)(smem + T * 18432 + s * 1024 + lane * 16) = o;
    };
    LANDED(15, hv[0]); put(hv[0], st[0], rot(0), wid); LANDED(14, hv[1]); put(hv[1], st[0], rot(0), wid + 8);
    LANDED(13, hv[2]); put(hv[2], st[1], rot(1), wid); LANDED(12, hv[3]); put(hv[3], st[1], rot(1), wid + 8);
    LANDED(11, hv[4]); put(hv[4], st[2], rot(2), wid); LANDED(10, hv[5]); put(hv[5], st[2], rot(2), wid + 8);
    LANDED(9, hv[6]); put(hv[6], st[3], rot(3), wid); LANDED(8, hv[7]); put(hv[7], st[3], rot(3), wid + 8);
    LANDED(7, hv[8]); put(hv[8], st[4], rot(4), wid); LANDED(6, hv[9]); put(hv[9], st[4], rot(4), wid + 8);
    LANDED(5, hv[10]);
    LANDED(4, hv[11]);
    ld4(rsG, 1, 294912);
    put(hv[10], st[5], wid >> 1, 16 + (wid & 1));
    if (wid < 2) put(hv[11], st4, 4, 16 + wid);
    WAITV(4); WAITL0(); BAR();
  }
  STAMP(ws, 2, 1);
  WST(2);

  unsigned aA[4], aB[4];
#pragma unroll
  for (int c = 0; c < 4; ++c) {
    aA[c] = ring + (unsigned)((8 * q + a4) * 128 + ((c ^ ((a4 >> 1) | ((q & 1) << 1))) * 32) + p4 * 8);
  }
  unsigned at0 = lds0 + (unsigned)(lane * 16), at1 = at0 + 3 * 18432;
  s16x4 wl[2][4], wh[2][4];
  bf16x8 tk[5];
  auto fresh = [&]() __attribute__((always_inline)) {
#pragma unroll
    for (int c = 0; c < 4; ++c) asm volatile("" : "=v"(wl[0][c]), "=v"(wh[0][c]), "=v"(wl[1][c]), "=v"(wh[1][c]));
#pragma unroll
    for (int t = 0; t < 5; ++t) asm volatile("" : "=v"(tk[t]));
  };
  fresh();
  auto rdw = [&](int buf, const unsigned(&a)[4], int hi) __attribute__((always_inline)) {
#pragma unroll
    for (int c = 0; c < 4; ++c) { DSRTR(wl[buf][c], a[c], buf * 4096); DSRTR(wh[buf][c], a[c], buf * 4096 + hi); }
  };
  auto rdt = [&](int t, int str, int off) __attribute__((always_inline)) {
    if (t < 3) DSR128(tk[t], at0, t * str + off); else DSR128(tk[t], at1, (t - 3) * str + off);
  };
#define LANDED_L(n) asm volatile("s_waitcnt lgkmcnt(%0)" ::"n"(n))
#define LANDED_W(n, buf) asm volatile("s_waitcnt lgkmcnt(%12)" : "+v"(wl[buf][0]), "+v"(wl[buf][1]), "+v"(wl[buf][2]), "+v"(wl[buf][3]), \
    "+v"(wh[buf][0]), "+v"(wh[buf][1]), "+v"(wh[buf][2]), "+v"(wh[buf][3]), "+v"(tk[0]), "+v"(tk[1]), "+v"(tk[2]), "+v"(tk[3]) : "n"(n))
#define LANDED_WB(buf) asm volatile("s_waitcnt lgkmcnt(0)" : "+v"(wl[buf][0]), "+v"(wl[buf][1]), "+v"(wl[buf][2]), "+v"(wl[buf][3]), \
    "+v"(wh[buf][0]), "+v"(wh[buf][1]), "+v"(wh[buf][2]), "+v"(wh[buf][3]))
#define LANDED_T(n, t) asm volatile("s_waitcnt lgkmcnt(%1)" : "+v"(tk[t]) : "n"(n))
  auto opnd = [&](int buf, int c) __attribute__((always_inline)) -> bf16x8 {
    return __builtin_shufflevector(wl[buf][c], wh[buf][c], 0, 1, 2, 3, 4, 5, 6, 7);
  };
  auto step = [&](f32x4(&acc)[4][5], auto P_, auto LD_, auto NW_, auto NT_, int str, int toff, u32x4 rs, int soff, auto gap) __attribute__((always_inline)) {
    constexpr int P = decltype(P_)::value, NW = decltype(NW_)::value;
    constexpr bool LD = decltype(LD_)::value, NT = decltype(NT_)::value;
    constexpr int W16 = (16 - WG0) * WPG < 8 ? (16 - WG0) * WPG : 8;
    if (LD) RING_M0(ring, P * 4096);
    LANDED_L(1);
#pragma unroll
    for (int t = 0; t < 5; ++t) {
      if (t == 4) LANDED_L((NT ? 4 : 0) + (NW ? W16 : 0));
      SCHED();
#pragma unroll
      for (int c = 0; c < 4; ++c) {
        const int g = t * 4 + c;
        MFMA_ACC(acc[c][t], opnd(P, c), tk[t]);
        SCHED();
        if (LD && t < 2 && (c & 1) == 0) { ld(rs, soff, 2 * t + (c >> 1)); SCHED(); }
        if (NW && g >= WG0 && g < WG0 + 8 / WPG) {
          if (g == WG0) { if (LD) WAITV(4); else WAITV(0); }
#pragma unroll
          for (int k = (g - WG0) * WPG; k < (g - WG0 + 1) * WPG; ++k) {
            const unsigned a = NW == 1 ? aA[k >> 1] : aB[k >> 1];
            if (k & 1) DSRTR(wh[P ^ 1][k >> 1], a, (P ^ 1) * 4096 + (NW == 1 ? 512 : 2048)); else DSRTR(wl[P ^ 1][k >> 1], a, (P ^ 1) * 4096);
          }
        }
        gap(g);
      }
      if (NT) rdt(t, str, toff);
    }
  };
  auto nogap = [&](int) __attribute__((always_inline)) {};
  using I0 = IC<0>; using I1 = IC<1>; using I2 = IC<2>;

  f32x4 ag[4][5], au[4][5];
#pragma unroll
  for (int c = 0; c < 4; ++c)
#pragma unroll
    for (int t = 0; t < 5; ++t) { ag[c][t] = f32x4{0, 0, 0, 0}; au[c][t] = f32x4{0, 0, 0, 0}; }
  ACC_READY(ag);

#pragma unroll
  for (int t = 0; t < 5; ++t) { rdt(t, 18432, 0); if (t == 3) rdw(0, aA, 512); }
  auto coarse = [&](int s, int n) __attribute__((always_inline)) {
    if (PRIO) asm volatile("s_cmp_lt_u32 %0, 4\n\ts_cbranch_scc1 2f\n\ts_cmp_lt_u32 %1, %2\n\ts_cbranch_scc1 1f\n\ts_setprio 0\n\ts_branch 2f\n1:\n\ts_setprio 1\n2:" ::"s"(wid), "s"(s), "s"(n) : "scc");
  };
#pragma clang loop unroll(disable)
  for (int s = 0; s < 16; s += 2) {
    coarse(s, PG_N);
    step(ag, I0{}, I1{}, I1{}, I1{}, 18432, 1024, rsG, (s + 2) * 294912, nogap);
    step(ag, I1{}, I1{}, I1{}, I1{}, 18432, 2048, rsG, (s + 3) * 294912, nogap);
    at0 += 2048; at1 += 2048;
  }
  at0 -= 16 * 1024; at1 -= 16 * 1024;
  step(ag, I0{}, I1{}, I1{}, I1{}, 18432, 17 * 1024, rsU, 0, nogap);
  step(ag, I1{}, I1{}, I1{}, I1{}, 18432, 0, rsU, 294912, nogap);
  WST(3);
  ACC_READY(ag); ACC_READY(au);
  float sx[5] = {0.f, 0.f, 0.f, 0.f, 0.f}, gs[80];
#pragma unroll
  for (int v = 0; v < 80; ++v) gs[v] = ag[(v >> 2) & 3][v >> 4][v & 3];
  auto ustep = [&](auto S_c) __attribute__((always_inline)) {
    constexpr int S = decltype(S_c)::value, NV = S < 8 ? 5 : 4, V0 = S < 8 ? 5 * S : 40 + 4 * (S - 8), NOP = 5 * NV;
    auto gap = [&](int g) __attribute__((always_inline)) {
      const int gi = g < 8 ? ((g & 1) ? g >> 1 : -1) : g - 4;
      if (gi < 0) return;
#pragma unroll
      for (int k = 0; k < NOP; ++k) {
        if (k * 16 / NOP != gi) continue;
        const int op = k / NV, e = k - op * NV, v = V0 + e;
        if (op == 0) asm volatile("v_mul_f32 %0, 0xbfb8aa3b, %1" : "+v"(sx[e]) : "v"(gs[v]));
        if (op == 1) asm volatile("v_exp_f32 %0, %0" : "+v"(sx[e]));
        if (op == 2) asm volatile("v_add_f32 %0, 1.0, %0" : "+v"(sx[e]));
        if (op == 3) asm volatile("v_rcp_f32 %0, %0" : "+v"(sx[e]));
        if (op == 4) asm volatile("v_mul_f32 %0, %0, %1" : "+v"(gs[v]) : "v"(sx[e]));
      }
      SCHED();
    };
    if (S == 0) PRIO_HI(wid);
    if (S == PU_N) PRIO_LO(wid);
    step(au, IC<S & 1>{}, I1{}, IC<(S < 17 ? 1 : 2)>{}, IC<(S < 17 ? 1 : 0)>{}, 18432, (S + 1) * 1024, S < 16 ? rsU : rsD,
         S < 16 ? (S + 2) * 294912 : (gp * 512 + wid * 64 + (S & 1) * 32) * 1152 + 1024 - wid * 128, gap);
  };
  ustep(IC<0>{}); ustep(IC<1>{}); ustep(IC<2>{}); ustep(IC<3>{}); ustep(IC<4>{}); ustep(IC<5>{});
  ustep(IC<6>{}); ustep(IC<7>{}); ustep(IC<8>{}); ustep(IC<9>{}); ustep(IC<10>{}); ustep(IC<11>{});
  ustep(IC<12>{}); ustep(IC<13>{}); ustep(IC<14>{}); ustep(IC<15>{});
  {
    int l = lane;
    asm volatile("" : "+v"(l));
    const int q_ = l >> 4, a4_ = (l >> 2) & 3, r8_ = l >> 3;
#pragma unroll
    for (int j = 0; j < 4; ++j) vW[j] = (unsigned)((r8_ + 8 * j) * 1152 + wid * 128 + (((l & 7) ^ (r8_ & 6)) * 16) - j * 1024);
#pragma unroll
    for (int c = 0; c < 4; ++c) aB[c] = ring + (unsigned)((4 * q_ + a4_) * 128 + ((c ^ ((2 * q_ + (a4_ >> 1)) & 3)) * 32) + (l & 3) * 8);
  }
  ustep(IC<16>{}); ustep(IC<17>{});
  ACC_READY(au);
  STAMP(ws, 2, 4);

  f32x4 o8[4][5], om[4][5];
  u32x4 act[2][5];
  const f32x4 zero4 = {0, 0, 0, 0};
  LANDED_WB(0);
  ld4(rsD, 0, gp * 512 * 1152);
  WAITV(4); rdw(1, aB, 2048);
  CODE_TOUCH("k4_sum", lds0 + KF_SCR, 0, wid);
#pragma unroll
  for (int pr = 0; pr < 2; ++pr) {
    if (pr == 1) {
      LANDED_WB(1);
      ld4(rsD, 1, (gp * 512 + 32) * 1152);
    }
#pragma unroll
    for (int t = 0; t < 5; ++t) {
      const float* g0 = gs + (t * 4 + 2 * pr) * 4;
      const f32x4 lo = f32x4{g0[0], g0[1], g0[2], g0[3]} * au[2 * pr][t], hi = f32x4{g0[4], g0[5], g0[6], g0[7]} * au[2 * pr + 1][t];
      act[pr][t] = u32x4{pk_bf16(lo[0], lo[1]), pk_bf16(lo[2], lo[3]), pk_bf16(hi[0], hi[1]), pk_bf16(hi[2], hi[3])};
      const bf16x8 f = __builtin_bit_cast(bf16x8, act[pr][t]);
#pragma unroll
      for (int c = 0; c < 4; ++c) o8[c][t] = MFMA16(opnd(pr, c), f, pr == 0 ? zero4 : o8[c][t]);
      SCHED();
    }
  }
  WST(4);
  BAR();
#pragma unroll
  for (int pr = 0; pr < 2; ++pr)
#pragma unroll
    for (int t = 0; t < 5; ++t) *(u32x4*)(smem + (t * 16 + 2 * wid + pr) * 1024 + lane * 16) = act[pr][t];
#pragma unroll
  for (int c = 0; c < 4; ++c)
#pragma unroll
    for (int t = 0; t < 5; ++t) om[c][t] = f32x4{0, 0, 0, 0};
  ACC_READY(om);
  WST(5);
  WAITL0(); BAR();
  WST(6);
  STAMP(ws, 2, 5);

  at0 = lds0 + (unsigned)(lane * 16); at1 = at0 + 3 * 16384;
  fresh();
  WAITV(4);
#pragma unroll
  for (int t = 0; t < 5; ++t) { rdt(t, 16384, 0); if (t == 3) rdw(0, aB, 2048); }
#pragma clang loop unroll(disable)
  for (int s = 0; s < 14; s += 2) {
    coarse(s, PB_N);
    step(om, I0{}, I1{}, I2{}, I1{}, 16384, 1024, rsD, (gp * 512 + (s + 2) * 32) * 1152, nogap);
    step(om, I1{}, I1{}, I2{}, I1{}, 16384, 2048, rsD, (gp * 512 + (s + 3) * 32) * 1152, nogap);
    at0 += 2048; at1 += 2048;
  }
  step(om, I0{}, I0{}, I2{}, I1{}, 16384, 1024, rsD, 0, nogap);
  step(om, I1{}, I0{}, I0{}, I0{}, 16384, 0, rsD, 0, nogap);
  PRIO_LO(wid);
  ACC_READY(om);
  WST(7);
  STAMP(ws, 2, 2);

  const int uo = (q & 1) * 2 + (q >> 1);
  auto emit = [&](f32x4 lo, f32x4 hi, int t, int E) __attribute__((always_inline)) {
    const auto x = __builtin_amdgcn_permlane16_swap(pk_bf16(lo[0], lo[1]), pk_bf16(hi[0], hi[1]), false, false);
    const auto y = __builtin_amdgcn_permlane16_swap(pk_bf16(lo[2], lo[3]), pk_bf16(hi[2], hi[3]), false, false);
    const int T16 = tt * 5 + t;
    if (T16 < 128)
      __builtin_amdgcn_raw_buffer_store_b128(u32x4{x[0], y[0], x[1], y[1]}, rsP, (unsigned)(((gp * 128 + T16) * 72 + E * 4 + uo) * 256 + r16 * 16), 0, AUX_SC1);
  };
#pragma unroll
  for (int k = 0; k < EPI - 1; ++k) emit(om[(k & 1) * 2][k >> 1], om[(k & 1) * 2 + 1][k >> 1], k >> 1, 2 * wid + (k & 1));
  SCHED();
  BAR();
#pragma unroll
  for (int t = 0; t < 5; ++t) {
#pragma unroll
    for (int c = 0; c < 4; ++c) *(f32x4*)(smem + (wid * 20 + t * 4 + c) * 1024 + lane * 16) = o8[c][t];
    SCHED();
  }
  WAITL0(); BAR();
#pragma unroll
  for (int k = EPI - 1; k < 10; ++k) emit(om[(k & 1) * 2][k >> 1], om[(k & 1) * 2 + 1][k >> 1], k >> 1, 2 * wid + (k & 1));
  SCHED();
  WST(8);
  WST(9);
  auto unit = [&](int u) __attribute__((always_inline)) {
    const char* p = smem + u * 2048 + lane * 16;
    f32x4 lo = {0, 0, 0, 0}, hi = {0, 0, 0, 0};
#pragma unroll
    for (int k = 0; k < 8; ++k) { lo += *(const f32x4*)(p + k * 20480); hi += *(const f32x4*)(p + k * 20480 + 1024); }
    emit(lo, hi, u >> 1, 16 + (u & 1));
  };
  unit(wid);
  if (wid < 2) unit(8 + wid);
  WST(10);
  WAITV(0);
  WST(11);
  WST_OUT(ws, 12);
  STAMP(ws, 2, 3);
}

extern "C" __global__ __launch_bounds__(576) void k4_sum(char* ws, bf16_t* out) {
  STAMP(ws, 3, 0);
  const int wv = threadIdx.x >> 6, lane = threadIdx.x & 63;
  const int T16 = blockIdx.x >> 1, t = (blockIdx.x & 1) * 8 + (lane >> 3), U = wv * 8 + (lane & 7);
  const rsrc_t rsP = MKRS(ws + WS_P, P_BYTES), rsH = MKRS(ws + WS_HT, HT_BYTES), rsO = MKRS(out, S_ * D_ * 2);
  const unsigned off = (unsigned)((T16 * 72 + U) * 256 + t * 16);
  u32x4 p[9];
#pragma unroll
  for (int g = 0; g < 9; ++g) p[g] = __builtin_amdgcn_raw_buffer_load_b128(rsP, off, g * (int)HT_BYTES, 0);
  u32x4 hv = __builtin_amdgcn_raw_buffer_load_b128(rsH, off, 0, 0);
  STAMP(ws, 3, 1);
  float a[8];
#pragma unroll
  for (int k = 0; k < 4; ++k) { a[2 * k] = bf_lo(p[0][k]); a[2 * k + 1] = bf_hi(p[0][k]); }
#pragma unroll
  for (int g = 1; g < 9; ++g)
#pragma unroll
    for (int k = 0; k < 4; ++k) { a[2 * k] += bf_lo(p[g][k]); a[2 * k + 1] += bf_hi(p[g][k]); }
  u32x4 ov;
#pragma unroll
  for (int k = 0; k < 4; ++k) ov[k] = pk_bf16(a[2 * k] + bf_lo(hv[k]), a[2 * k + 1] + bf_hi(hv[k]));
  STAMP(ws, 3, 2);
  __builtin_amdgcn_raw_buffer_store_b128(ov, rsO, (unsigned)((T16 * 16 + t) * 1152 + U * 16), 0, K4_AUX);
  STAMP(ws, 3, 3);
  if (blockIdx.x == 0) {
    __shared__ long long red[9][4];
    const long long tnow = (long long)__builtin_amdgcn_s_memrealtime();
    const int tb = threadIdx.x;
    long long v = 0; int mode = 0;
    auto rt = [&](int k, int b, int sl) -> long long { return (long long)*(const unsigned long long*)(ws + (64u << 20) + k * 65536 + b * 128 + sl * 16 + 8); };
    auto cy = [&](int k, int b, int sl) -> long long { return (long long)*(const unsigned long long*)(ws + (64u << 20) + k * 65536 + b * 128 + sl * 16); };
    auto wred = [&](long long x, int op) -> long long {
      for (int o = 1; o < 64; o <<= 1) { long long y = __shfl_xor(x, o); x = op == 0 ? (y < x ? y : x) : op == 1 ? (y > x ? y : x) : x + y; }
      if ((tb & 63) == 0) red[tb >> 6][0] = x;
      __syncthreads();
      long long r = red[0][0];
      for (int w = 1; w < 4; ++w) { long long y = red[w][0]; r = op == 0 ? (y < r ? y : r) : op == 1 ? (y > r ? y : r) : r + y; }
      __syncthreads();
      return r;
    };
    const int b = tb < 256 ? tb : 255;
    const bool mainb = b < NMAIN;
    const long long BIG = 1ll << 62;
    const long long s1 = wred(rt(0, b, 0), 0), s2 = wred(rt(1, b, 0), 0), s3 = wred(mainb ? rt(2, b, 0) : BIG, 0), s4 = wred(rt(3, b, 0) > 0 && b == 0 ? rt(3, b, 0) : BIG, 0);
    const long long e1 = wred(rt(0, b, 3), 1), e2 = wred(rt(1, b, 3), 1), e3 = wred(mainb ? rt(2, b, 3) : 0, 1), ep = wred(mainb ? 0 : rt(2, b, 3), 1);
    auto avg = [&](int k, int a, int c) { return wred(mainb && tb < 256 ? rt(k, b, c) - rt(k, b, a) : 0, 2) / NMAIN; };
    long long qq[22];
    qq[0] = 0;
    qq[1] = s2 - s1;
    qq[2] = s3 - s2;
    qq[3] = e3 - s3;
    qq[4] = avg(2, 0, 1);
    qq[5] = ep - s3;
    qq[6] = avg(2, 1, 4);
    qq[7] = avg(2, 4, 5);
    qq[8] = avg(2, 5, 2);
    qq[9] = avg(2, 2, 3);
    qq[10] = wred(mainb && tb < 256 ? cy(2, b, 3) - cy(2, b, 0) : 0, 2) / NMAIN / 24;
    qq[11] = s4 - e3;
    qq[12] = e1 - s1;
    qq[13] = e2 - s2;
    qq[14] = wred(tb < 256 ? rt(1, b, 1) - rt(1, b, 0) : 0, 2) / 256;
    qq[15] = wred(tb < 256 ? rt(0, b, 1) - rt(0, b, 0) : 0, 2) / 256;
    qq[16] = wred(tb < 256 ? cy(1, b, 3) - cy(1, b, 0) : 0, 2) / 256 / 24;
    qq[17] = wred(tb < 256 ? rt(1, b, 3) - rt(1, b, 0) : 0, 2) / 256;
    qq[18] = wred(mainb && tb < 256 ? rt(2, b, 3) - rt(2, b, 0) : 0, 2) / NMAIN;
    qq[19] = tnow - s1;
    qq[20] = tnow - s4;
    qq[21] = tnow - e3;
    for (int k = 0; k < 22; ++k) asm volatile("" ::"v"(qq[k]));
    long long q = qq[QSPIN];
    { const long long refmin = wred(rt(0, b, QABS), 0), refavg = wred(tb < 256 ? rt(0, b, QABS) - refmin : 0, 2) / 256 + refmin, refmax = wred(rt(0, b, QABS), 1);
      const long long ref = QAVG == 0 ? refmin : QAVG == 1 ? refavg : refmax;
      if (tb == 0) { while ((long long)__builtin_amdgcn_s_memrealtime() - ref < 12000 && (long long)__builtin_amdgcn_s_memrealtime() - tnow < 20000) __builtin_amdgcn_s_sleep(1); }
      return; }
    q *= 4;
    q = q < 0 ? 0 : (q > 40000 ? 40000 : q);
    if (tb == 0) { while ((long long)__builtin_amdgcn_s_memrealtime() - tnow < q + 3000) __builtin_amdgcn_s_sleep(2); }
  }

}

extern "C" void kernel_launch(void* const* d_in, const int* in_sizes, int n_in, void* d_out, int out_size, void* d_ws, size_t ws_size,
                              hipStream_t stream) {
  const bf16_t* x = (const bf16_t*)d_in[0];
  const bf16_t* cre = (const bf16_t*)d_in[1];
  const bf16_t* cim = (const bf16_t*)d_in[2];
  const bf16_t* wre = (const bf16_t*)d_in[3];
  const bf16_t* wim = (const bf16_t*)d_in[4];
  const bf16_t* wg = (const bf16_t*)d_in[6];
  const bf16_t* wu = (const bf16_t*)d_in[7];
  const bf16_t* wd = (const bf16_t*)d_in[8];
  char* ws = (char*)d_ws;
  k1_hid<<<256, 512, 0, stream>>>(x, wre, wim, cre, cim, ws);
  k2_seq<<<256, 512, 0, stream>>>(x, cre, cim, wg, wu, ws);
  k3_ffn<<<256, 512, 0, stream>>>(wg, wu, wd, ws);
  k4_sum<<<256, 576, 0, stream>>>(ws, (bf16_t*)d_out);
}
```

```cpp
#define DIAG
#define QSPIN 0
#define NMAIN 234
#include <hip/hip_runtime.h>
#include <stdint.h>

typedef unsigned short bf16_t;
typedef short bf16x8 __attribute__((ext_vector_type(8)));
typedef short s16x4 __attribute__((ext_vector_type(4)));
typedef float f32x4 __attribute__((ext_vector_type(4)));
typedef float f32x2 __attribute__((ext_vector_type(2)));
typedef unsigned u32x4 __attribute__((ext_vector_type(4)));
typedef unsigned u32x2 __attribute__((ext_vector_type(2)));
typedef __bf16 bf2_t __attribute__((ext_vector_type(2)));
typedef __amdgpu_buffer_rsrc_t rsrc_t;

constexpr int S_ = 2048, D_ = 576, F_ = 4608;
constexpr float LN_EPS = 1e-5f;

constexpr unsigned WS_BP = 0;
constexpr unsigned BP_BYTES = 8u * 32u * 18432u;
constexpr unsigned WS_HT = WS_BP + BP_BYTES;
constexpr unsigned HT_BYTES = 128u * 72u * 256u;
constexpr unsigned WS_ST = WS_HT + HT_BYTES;
constexpr unsigned ST_BYTES = 2048u * 8u * 8u;
constexpr unsigned WS_P = WS_ST + ST_BYTES;
constexpr unsigned P_BYTES = 9u * HT_BYTES;

#define LDSP(p) ((__attribute__((address_space(3))) void*)(p))
#define MKRS(p, n) __builtin_amdgcn_make_buffer_rsrc((void*)(p), 0, (n), 0x00020000)
#define GLDS(rs, m0base, imm, voff, soff)                                                              \
  asm volatile("s_add_u32 m0, %0, %1\n\ts_nop 0\n\tbuffer_load_dwordx4 %2, %3, %4 offen lds"          \
               ::"s"(m0base), "i"(imm), "v"(voff), "s"(rs), "s"(soff) : "memory", "scc")
#define GLDS_N(mask, rs, m0base, imm, voff, soff)                                                      \
  asm volatile("s_add_u32 m0, %0, %1\n\ts_mov_b64 exec, " #mask "\n\tbuffer_load_dwordx4 %2, %3, %4 offen lds\n\ts_mov_b64 exec, -1" \
               ::"s"(m0base), "i"(imm), "v"(voff), "s"(rs), "s"(soff) : "memory", "scc")
#define GLDS_36(rs, m0base, imm, voff, soff)                                                           \
  asm volatile("s_add_u32 m0, %0, %1\n\ts_mov_b32 exec_hi, 0xf\n\tbuffer_load_dwordx4 %2, %3, %4 offen lds\n\ts_mov_b32 exec_hi, -1" \
               ::"s"(m0base), "i"(imm), "v"(voff), "s"(rs), "s"(soff) : "memory", "scc")
#define RING_M0(m0base, imm) asm volatile("s_add_u32 m0, %0, %1" ::"s"(m0base), "i"(imm) : "scc")
#define RING_LD(rs, off, voff, soff) asm volatile("buffer_load_dwordx4 %0, %1, %2 offen offset:%3 lds" ::"v"(voff), "s"(rs), "s"(soff), "i"(off) : "memory")
#define TOUCH(rs, mscr, voff)                                                                          \
  asm volatile("s_mov_b32 m0, %0\n\ts_nop 0\n\tbuffer_load_dword %1, %2, 0 offen lds" ::"s"(mscr), "v"(voff), "s"(rs) : "memory")
#define TOUCHS(rs, mscr, voff, soff)                                                                   \
  asm volatile("s_mov_b32 m0, %0\n\ts_nop 0\n\tbuffer_load_dword %1, %2, %3 offen lds" ::"s"(mscr), "v"(voff), "s"(rs), "s"(soff) : "memory")
__device__ __forceinline__ u32x4 mksrd(const void* p, unsigned n) {
  const unsigned long long a = (unsigned long long)p;
  return u32x4{(unsigned)a, (unsigned)(a >> 32) & 0xffffu, n, 0x00020000u};
}
#define CODE_ADDR(lo, hi, sym)                                                                         \
  asm volatile("s_getpc_b64 s[20:21]\n\ts_add_u32 %0, s20, " sym "@rel32@lo+4\n\ts_addc_u32 %1, s21, " sym "@rel32@hi+12" \
               : "=s"(lo), "=s"(hi)::"s20", "s21", "scc")
#define CODE_TOUCH(sym, mscr, i, wid)                                                                  \
  do {                                                                                                 \
    unsigned lo_, hi_, le_, he_;                                                                       \
    CODE_ADDR(lo_, hi_, sym); CODE_ADDR(le_, he_, "k4_sum");                                           \
    const unsigned n_ = le_ - lo_ + 1024u;                                                             \
    TOUCH((u32x4{lo_, hi_ & 0xffffu, n_ < 65536u ? n_ : 0u, 0x00020000u}), mscr, (unsigned)(((i) * 8 + (wid)) * 128)); \
  } while (0)
#define CODE_TOUCH_TOP(sym, dst)                                                                       \
  asm volatile("s_getpc_b64 s[40:41]\n\ts_add_u32 s36, s40, " sym "@rel32@lo+4\n\ts_addc_u32 s37, s41, " sym "@rel32@hi+12\n\t"          \
               "s_getpc_b64 s[40:41]\n\ts_add_u32 s38, s40, k4_sum@rel32@lo+4\n\ts_sub_u32 s38, s38, s36\n\ts_add_u32 s38, s38, 0x400\n\t"  \
               "s_and_b32 s37, s37, 0xffff\n\ts_mov_b32 s39, 0x20000\n\ts_cmp_lt_u32 s38, 0x10000\n\ts_cselect_b32 s38, s38, 0\n\t"            \
               "s_and_b32 s40, %1, -8\n\tv_lshrrev_b32 %0, 6, %2\n\tv_add_u32 %0, s40, %0\n\tv_lshlrev_b32 %0, 7, %0\n\t"                    \
               "buffer_load_dword %0, %0, s[36:39], 0 offen"                                           \
               : "=&v"(dst) : "s"(blockIdx.x), "v"(threadIdx.x) : "s36", "s37", "s38", "s39", "s40", "s41", "scc", "memory")
__device__ __forceinline__ unsigned lds_base(char* smem) { return (unsigned)(size_t)LDSP(smem); }
#define GLD(dst, rs, voff) asm volatile("buffer_load_dwordx4 %0, %1, %2, 0 offen" : "=v"(dst) : "v"(voff), "s"(rs) : "memory")
#define LANDED(n, x) asm volatile("s_waitcnt vmcnt(" #n ")" : "+v"(x))
#define WAITV(n) asm volatile("s_waitcnt vmcnt(" #n ")" ::: "memory")
#define WAITL0() asm volatile("s_waitcnt lgkmcnt(0)" ::: "memory")
#define PIN(x) asm volatile("" : "+v"(x))
#define BAR() __builtin_amdgcn_s_barrier()
#define SCHED() __builtin_amdgcn_sched_barrier(0)
#define MFMA16(a, b, c) __builtin_amdgcn_mfma_f32_16x16x32_bf16((a), (b), (c), 0, 0, 0)
constexpr int AUX_SC1 = 16;
#define PRIO 1
#define PA_T 2
#define PB_T 2
#define PRIO_SET(wid, p) do { if (PRIO) asm volatile("s_cmp_lt_u32 %0, 4\n\ts_cbranch_scc1 1f\n\ts_setprio " #p "\n1:" ::"s"(wid) : "scc"); } while (0)
#define PRIO_HI(wid) PRIO_SET(wid, 1)
#define PRIO_LO(wid) PRIO_SET(wid, 0)
template <int V> struct IC { static constexpr int value = V; };
#define CS_T 3
#define CT2_MODE 1
#define CT3_MODE 2
#define K2_VAR 0
#define K1_TOP 1
#define K4_AUX 0
#define TSX(var, j) do { } while (0)
#define TS_OUT(ws, kid, wid, lane, k, var) do { } while (0)
#define STAMP(ws, kid, slot) do { if (threadIdx.x == 0) { unsigned long long* p_ = (unsigned long long*)((ws) + (64u << 20) + (kid) * 65536 + blockIdx.x * 128 + (slot) * 16); \
  p_[0] = __builtin_amdgcn_s_memtime(); p_[1] = __builtin_amdgcn_s_memrealtime(); } } while (0)

#define WST(k) do { } while (0)
#define WST_OUT(ws, n) do { } while (0)

#define TS4(j) do { } while (0)
#define TS4_OUT(ws) do { } while (0)

__device__ __forceinline__ unsigned pk_bf16(float lo, float hi) {
  f32x2 v = {lo, hi};
  bf2_t b = __builtin_convertvector(v, bf2_t);
  return __builtin_bit_cast(unsigned, b);
}
__device__ __forceinline__ float bf_lo(unsigned w) { return __uint_as_float(w << 16); }
__device__ __forceinline__ float bf_hi(unsigned w) { return __uint_as_float(w & 0xffff0000u); }
__device__ __forceinline__ float dot_ones(unsigned w, float acc) {
  const bf2_t ones = {(__bf16)1.0f, (__bf16)1.0f};
  return __builtin_amdgcn_fdot2_f32_bf16(__builtin_bit_cast(bf2_t, w), ones, acc, false);
}
__device__ __forceinline__ float dot_frag(bf16x8 f, float acc) {
  u32x4 u = __builtin_bit_cast(u32x4, f);
  acc = dot_ones(u[0], acc); acc = dot_ones(u[1], acc); acc = dot_ones(u[2], acc); acc = dot_ones(u[3], acc);
  return acc;
}

constexpr int K1_SLOT = 8192 + 18432, K1_NS = 5, K1_ST = K1_NS * K1_SLOT, K1_WS = K1_ST + 512, K1_SCR = K1_WS + 1152, K1_LDS = K1_SCR + 256;

template <int NF>
__device__ __forceinline__ void k1_body(char* smem, u32x4 rsX, u32x4 rsW, u32x4 rsC, rsrc_t rsO, int st, int et, int wid, int lane, int nf0,
                                        unsigned vhead, bool headx, char* ws, unsigned ctop) {
  STAMP(ws, 0, 0);
  const int tid = wid * 64 + lane;
  const int sh = wid >> 2, q = lane >> 4, r16 = lane & 15, r8 = lane >> 3, c8 = lane & 7;
  const unsigned vx = (unsigned)(64 * st + 8 * wid + r8) * 1152u + (unsigned)((c8 ^ ((r8 & 3) | ((wid & 1) << 2))) * 16);
  const unsigned vw = (unsigned)((et & 3) * 144 + 8 * wid + r8) * 1152u + (unsigned)((c8 ^ r8) * 16);
  const int r2 = 128 + 2 * wid + r8;
  const unsigned vw2 = (unsigned)((et & 3) * 144 + r2) * 1152u + (unsigned)((c8 ^ (r2 & 7)) * 16);
  const unsigned lds0 = lds_base(smem);
  const unsigned mA = lds0 + wid * 1024, mP = lds0 + wid * 256;
  auto issue1 = [&](int kb, int k) __attribute__((always_inline)) {
    const int so = (kb % K1_NS) * K1_SLOT;
    if (k == 0) GLDS(rsX, mA, so, vx, kb * 128);
    if (k == 1) GLDS(rsW, mA, so + 8192, vw, kb * 128);
    if (k == 2) GLDS(rsW, mA, so + 16384, vw, kb * 128 + 64 * 1152);
    if (k == 3) GLDS_N(0xffff, rsW, mP, so + 24576, vw2, kb * 128);
  };
  auto issue = [&](int kb) __attribute__((always_inline)) { issue1(kb, 0); issue1(kb, 1); issue1(kb, 2); issue1(kb, 3); };
  unsigned ax[2];
#pragma unroll
  for (int m = 0; m < 2; ++m) {
    const int row = 32 * sh + 8 * (r16 >> 2) + 4 * m + (r16 & 3);
    const int key = (row & 3) | ((row >> 1) & 4);
    ax[m] = (unsigned)(row * 128 + ((q ^ key) * 16)) ^ (unsigned)(sh * 64);
  }
  const unsigned aw = (unsigned)(8192 + (nf0 * 16 + r16) * 128 + ((q ^ (r16 & 7)) * 16)) ^ (unsigned)(sh * 64);

  f32x4 acc[2][NF];
  float wsum[NF];
#pragma unroll
  for (int n = 0; n < NF; ++n) { wsum[n] = 0.f; acc[0][n] = f32x4{0, 0, 0, 0}; acc[1][n] = f32x4{0, 0, 0, 0}; }
  float shift = 0.f;
  f32x2 s1 = {0.f, 0.f}, s2 = {0.f, 0.f};

  struct Fr { u32x4 v; bf16x8 a[2][2], b[2][NF]; };
  Fr fr[2];
  auto rd = [&](Fr& f, int kb, int part) __attribute__((always_inline)) {
    const char* sl = smem + (kb % K1_NS) * K1_SLOT;
    if (part == 0) {
      f.a[0][0] = *(const bf16x8*)(sl + ax[0]); f.a[0][1] = *(const bf16x8*)(sl + ax[1]);
#pragma unroll
      for (int n = 0; n < NF; ++n) f.b[0][n] = *(const bf16x8*)(sl + aw + n * 2048);
    } else {
      f.v = *(const u32x4*)(sl + tid * 16);
      f.a[1][0] = *(const bf16x8*)(sl + (ax[0] ^ 64)); f.a[1][1] = *(const bf16x8*)(sl + (ax[1] ^ 64));
#pragma unroll
      for (int n = 0; n < NF; ++n) f.b[1][n] = *(const bf16x8*)(sl + ((aw ^ 64) + n * 2048));
    }
  };
  issue(0);
  STAMP(ws, 0, 7);
  issue(1);
  TOUCH(headx ? rsX : rsW, lds0 + K1_SCR, vhead);
  issue(2); issue(3); issue(4);
  WAITV(17); BAR();
  asm volatile("" ::"v"(ctop));
  STAMP(ws, 0, 1);
  rd(fr[0], 0, 0); rd(fr[0], 0, 1);
  WAITL0();
  WAITV(13); BAR();
#pragma unroll
  for (int kb = 0; kb < 9; ++kb) {
    const Fr& c = fr[kb & 1];
    Fr& nx = fr[(kb & 1) ^ 1];
    const bool stg = kb + 5 < 9;
    if (stg) issue1(kb + 5, 0);
    if (kb == 4)
      TOUCH(rsC, lds0 + K1_SCR, (unsigned)(blockIdx.x * 32768 + (wid & 3) * 8192 + lane * 128));
    SCHED();
#pragma unroll
    for (int n = 0; n < NF; ++n) { acc[0][n] = MFMA16(c.a[0][0], c.b[0][n], acc[0][n]); acc[1][n] = MFMA16(c.a[0][1], c.b[0][n], acc[1][n]); }
    SCHED();
    if (kb < 8) rd(nx, kb + 1, 0);
    SCHED();
    if (stg) issue1(kb + 5, 1);
    SCHED();
    if (kb == 0) shift = bf_lo(c.v[0]);
#pragma unroll
    for (int i = 0; i < 4; ++i) {
      f32x2 d = {bf_lo(c.v[i]) - shift, bf_hi(c.v[i]) - shift};
      s1 += d; s2 += d * d;
    }
    PIN(s1); PIN(s2);
    SCHED();
    if (stg) issue1(kb + 5, 2);
    SCHED();
#pragma unroll
    for (int n = 0; n < NF; ++n) { acc[0][n] = MFMA16(c.a[1][0], c.b[1][n], acc[0][n]); acc[1][n] = MFMA16(c.a[1][1], c.b[1][n], acc[1][n]); }
    SCHED();
    if (kb < 8) rd(nx, kb + 1, 1);
    SCHED();
#pragma unroll
    for (int n = 0; n < NF; ++n) wsum[n] = dot_frag(c.b[0][n], wsum[n]);
#pragma unroll
    for (int n = 0; n < NF; ++n) PIN(wsum[n]);
    SCHED();
    if (stg) issue1(kb + 5, 3);
    WAITL0();
    if (kb <= 3) WAITV(12); else if (kb == 4) WAITV(9); else if (kb == 5) WAITV(5); else if (kb == 6) WAITV(1);
    if (kb < 7) BAR();
    SCHED();
  }
  STAMP(ws, 0, 2);
  {
    const float a1 = s1[0] + s1[1], a2 = s2[0] + s2[1];
    float mean = shift + a1 * (1.f / 72.f);
    float m2 = a2 - a1 * a1 * (1.f / 72.f);
    float n = 72.f;
#pragma unroll
    for (int o = 1; o < 8; o <<= 1) {
      const float mo = __shfl_xor(mean, o), qo = __shfl_xor(m2, o);
      const float dl = mo - mean;
      m2 = m2 + qo + dl * dl * (n * 0.5f);
      mean = 0.5f * (mean + mo);
      n *= 2.f;
    }
    const float r = __builtin_amdgcn_rsqf(m2 * (1.f / 576.f) + LN_EPS);
    if (c8 == 0) *(f32x2*)(smem + K1_ST + (tid >> 3) * 8) = f32x2{r, mean * r};
#pragma unroll
    for (int n = 0; n < NF; ++n) {
      float w = wsum[n];
      w += __shfl_xor(w, 16); w += __shfl_xor(w, 32);
      if (q == 0) *(float*)(smem + K1_WS + sh * 576 + ((nf0 + n) * 16 + r16) * 4) = w;
    }
  }
  WAITL0();
  BAR();
  f32x2 rs[8];
  {
    const char* p = smem + K1_ST + (32 * sh + 8 * q) * 8;
#pragma unroll
    for (int i = 0; i < 4; ++i) {
      f32x4 v = *(const f32x4*)(p + i * 16);
      rs[2 * i] = f32x2{v[0], v[1]}; rs[2 * i + 1] = f32x2{v[2], v[3]};
    }
  }
  const float sgn = et < 4 ? 1.f : -1.f;
  const int kt = (et < 4 ? 0 : 16) + (st >> 1), ksg = (st & 1) * 2 + sh;
#pragma unroll
  for (int n = 0; n < NF; ++n) {
    const int el = (nf0 + n) * 16 + r16;
    const float ws = *(const float*)(smem + K1_WS + el * 4) + *(const float*)(smem + K1_WS + 576 + el * 4);
    float o[8];
#pragma unroll
    for (int m = 0; m < 2; ++m)
#pragma unroll
      for (int j = 0; j < 4; ++j) o[4 * m + j] = sgn * (acc[m][n][j] * rs[4 * m + j][0] - rs[4 * m + j][1] * ws);
    u32x4 pk = {pk_bf16(o[0], o[1]), pk_bf16(o[2], o[3]), pk_bf16(o[4], o[5]), pk_bf16(o[6], o[7])};
    const int hi = el >= 72 ? 1 : 0, e72 = el - 72 * hi;
    const int etg = (et & 3) * 2 + hi;
    const unsigned inner = e72 < 64 ? (unsigned)((e72 >> 4) * 1024 + q * 256 + (e72 & 15) * 16) : (unsigned)(4096 + q * 128 + (e72 & 7) * 16);
    const unsigned off = (unsigned)((etg * 32 + kt) * 4 + ksg) * 4608u + inner;
    __builtin_amdgcn_raw_buffer_store_b128(pk, rsO, off, 0, AUX_SC1);
  }
  WAITV(0);
  STAMP(ws, 0, 3);
}

extern "C" __global__ __launch_bounds__(512, 2) void k1_hid(const bf16_t* x, const bf16_t* wre, const bf16_t* wim, const bf16_t* cre, const bf16_t* cim, char* ws) {
  __shared__ __attribute__((aligned(16))) char smem[K1_LDS];
  const int wid = __builtin_amdgcn_readfirstlane(threadIdx.x >> 6), lane = threadIdx.x & 63;
  const int b = blockIdx.x, xcd = b & 7, i = b >> 3;
  unsigned ctop = 0;
  CODE_TOUCH_TOP("k1_hid", ctop);
  asm volatile("" ::"s"(x), "s"(wre), "s"(wim), "s"(cre), "s"(cim), "s"(ws));
  const int st = xcd * 4 + (i & 3), et = i >> 2;
  const u32x4 rsX = mksrd(x, S_ * D_ * 2);
  const u32x4 rsW = mksrd(et < 4 ? wre : wim, D_ * D_ * 2);
  const u32x4 rsC = mksrd(wid < 4 ? cre : cim, S_ * S_ * 2);
  rsrc_t rsO = MKRS(ws + WS_BP, BP_BYTES);
  unsigned vhead;
  if (wid == 7) {
    const int l = lane < 32 ? lane : 31;
    vhead = (unsigned)((64 * st + 8 * et + (l >> 2)) * 1152 + (5 + (l & 3)) * 128);
  } else {
    int l = wid * 21 + (lane < 21 ? lane : 20);
    l = l < 144 ? l : 143;
    vhead = (unsigned)(((et & 3) * 144 + 36 * (i & 3) + (l >> 2)) * 1152 + (5 + (l & 3)) * 128);
  }
  const int w4 = wid & 3;
  if (wid == 0 || wid == 7) k1_body<3>(smem, rsX, rsW, rsC, rsO, st, et, wid, lane, wid == 0 ? 0 : 6, vhead, wid == 7, ws, ctop);
  else k1_body<2>(smem, rsX, rsW, rsC, rsO, st, et, wid, lane, wid < 4 ? 1 + 2 * w4 : 2 * w4, vhead, false, ws, ctop);
}

constexpr int K2_SLOT = 16384 + 18432, K2_XOFF = 4 * K2_SLOT, K2_SCR = K2_XOFF + 9216, K2_LDS = K2_SCR + 256;
constexpr int K2_RSTR = 336, K2_RKQ = 64 * K2_RSTR, K2_SOFF = 4 * K2_RKQ;

extern "C" __global__ __launch_bounds__(512, 2) void k2_seq(const bf16_t* x, const bf16_t* cre, const bf16_t* cim, const bf16_t* wg, const bf16_t* wu, char* ws) {
  __shared__ __attribute__((aligned(16))) char smem[K2_LDS];
  const int wid = __builtin_amdgcn_readfirstlane(threadIdx.x >> 6), lane = threadIdx.x & 63;
  const int b = blockIdx.x, xcd = b & 7, i = b >> 3;
  const int mt = (xcd >> 1) * 8 + (i & 7), nt = (xcd & 1) * 4 + (i >> 3);
  const u32x4 rsRe = mksrd(cre, S_ * S_ * 2), rsIm = mksrd(cim, S_ * S_ * 2);
  const u32x4 rsB = mksrd(ws + WS_BP, BP_BYTES);
  const u32x4 rsX = mksrd(x, S_ * D_ * 2);
  const int q = lane >> 4, r16 = lane & 15, r8 = lane >> 3, c8 = lane & 7;
  const int mh = wid & 1, kq = wid >> 1;

  const unsigned va = (unsigned)(64 * mt + 8 * wid + r8) * 4096u + (unsigned)((c8 ^ r8) * 16);
  const unsigned vb = (unsigned)(wid * 1024 + lane * 16);
  const unsigned vb2 = (unsigned)(16384 + wid * 256 + lane * 16);
  const unsigned mA = lds_base(smem) + wid * 1024, mP = lds_base(smem) + wid * 256;
  auto issue1 = [&](int j, u32x4 rsA, int k) __attribute__((always_inline)) {
    const unsigned so = (unsigned)((j & 3) * K2_SLOT), m0 = mA + so, m1 = mP + so;
    const int sa = (j & 15) * 256, sb = (nt * 32 + j) * 18432;
    if (k == 0) GLDS(rsA, m0, 0, va, sa);
    if (k == 1) GLDS(rsA, m0, 8192, va, sa + 128);
    if (k == 2) GLDS(rsB, m0, 16384, vb, sb);
    if (k == 3) GLDS(rsB, m0, 16384 + 8192, vb, sb + 8192);
    if (k == 4) GLDS_N(0xffff, rsB, m1, 16384 + 16384, vb2, sb);
  };
  auto issue = [&](int j, u32x4 rsA) __attribute__((always_inline)) {
    issue1(j, rsA, 0); issue1(j, rsA, 1); issue1(j, rsA, 2); issue1(j, rsA, 3); issue1(j, rsA, 4);
  };
  const unsigned at0 = (unsigned)((kq >> 1) * 8192 + (32 * mh + r16) * 128 + ((((kq & 1) * 4 + q) ^ (r16 & 7)) * 16));
  const unsigned ae0 = (unsigned)(16384 + kq * 4608 + lane * 16);
  const unsigned aeh = (unsigned)(16384 + kq * 4608 + 4096 + q * 128 + (lane & 7) * 16);

  f32x4 acc[5][2];
#pragma unroll
  for (int f = 0; f < 5; ++f) { acc[f][0] = f32x4{0, 0, 0, 0}; acc[f][1] = f32x4{0, 0, 0, 0}; }
  struct Fr { bf16x8 t0, t1, e[5]; };
  Fr fr[2];
  auto rd = [&](Fr& f, int j, int part) __attribute__((always_inline)) {
    const char* sl = smem + (j & 3) * K2_SLOT;
    if (part == 0) { f.t0 = *(const bf16x8*)(sl + at0); f.t1 = *(const bf16x8*)(sl + at0 + 2048); }
    if (part == 1) { f.e[0] = *(const bf16x8*)(sl + ae0); f.e[1] = *(const bf16x8*)(sl + ae0 + 1024); }
    if (part == 2) { f.e[2] = *(const bf16x8*)(sl + ae0 + 2048); f.e[3] = *(const bf16x8*)(sl + ae0 + 3072); }
    if (part == 3) f.e[4] = *(const bf16x8*)(sl + aeh);
  };
  auto stage = [&](int j, auto P_, u32x4 rsA, auto MODE_, auto RD_) __attribute__((always_inline)) {
    constexpr int P = decltype(P_)::value, MODE = decltype(MODE_)::value, RD = decltype(RD_)::value;
    const Fr& c = fr[P];
    Fr& nx = fr[P ^ 1];
#pragma unroll
    for (int f = 0; f < 5; ++f) {
      if (MODE == 1) issue1(j + 4, rsA, f);
      if (MODE == 2 && f == 0) {
        const int o0 = wid * 1024 + lane * 16, o1 = 8192 + wid * 128 + lane * 16;
        const int row0 = o0 / 144, row1 = o1 / 144;
        GLDS(rsX, mA, K2_XOFF, (unsigned)((64 * mt + row0) * 1152 + nt * 144 + (o0 - row0 * 144)), 0);
        GLDS_N(0xff, rsX, lds_base(smem) + wid * 128, K2_XOFF + 8192, (unsigned)((64 * mt + row1) * 1152 + nt * 144 + (o1 - row1 * 144)), 0);
      }
      if (MODE == 2 && f == 2) {
        CODE_TOUCH("k2_seq", lds_base(smem) + K2_SCR, i, wid);
        int n = wid * 14 + (lane < 13 ? lane : 13);
        n = i * 108 + (n < 107 ? n : 107);
        const int row = n / 9;
        TOUCH(mksrd(wg, D_ * F_ * 2), lds_base(smem) + K2_SCR, (unsigned)(row * 9216 + xcd * 1152 + (n - row * 9) * 128));
      }
      SCHED();
      acc[f][0] = MFMA16(c.e[f], c.t0, acc[f][0]); acc[f][1] = MFMA16(c.e[f], c.t1, acc[f][1]);
      SCHED();
      if (RD && f < 4) rd(nx, j + 1, f);
      SCHED();
    }
    WAITL0();
  };
  using I0 = IC<0>; using I1 = IC<1>; using I2 = IC<2>;

  STAMP(ws, 1, 0);
  issue(0, rsRe); issue(1, rsRe); issue(2, rsRe); issue(3, rsRe);
  WAITV(15); BAR();
  STAMP(ws, 1, 1);
  rd(fr[0], 0, 0); rd(fr[0], 0, 1); rd(fr[0], 0, 2); rd(fr[0], 0, 3);
  WAITL0();
  WAITV(10); BAR();
  for (int j = 0; j < 12; j += 2) {
    stage(j, I0{}, rsRe, I1{}, I1{}); WAITV(10); BAR();
    stage(j + 1, I1{}, rsRe, I1{}, I1{}); WAITV(10); BAR();
  }
  for (int j = 12; j < 28; j += 2) {
    stage(j, I0{}, rsIm, I1{}, I1{}); WAITV(10); BAR();
    stage(j + 1, I1{}, rsIm, I1{}, I1{}); WAITV(10); BAR();
  }
  stage(28, I0{}, rsIm, I2{}, I1{}); WAITV(9); BAR();
  stage(29, I1{}, rsIm, I0{}, I1{}); WAITV(4); BAR();
  stage(30, I0{}, rsIm, I0{}, I1{});
  stage(31, I1{}, rsIm, I0{}, I0{});
  WAITV(2); BAR();
  STAMP(ws, 1, 2);
#pragma unroll
  for (int f = 0; f < 5; ++f)
#pragma unroll
    for (int tf = 0; tf < 2; ++tf)
      *(f32x4*)(smem + kq * K2_RKQ + (32 * mh + 16 * tf + r16) * K2_RSTR + (16 * f + 4 * q) * 4) = acc[f][tf];
  WAITL0(); BAR();
  const rsrc_t rsH = MKRS(ws + WS_HT, HT_BYTES);
  auto item = [&](int U) {
    const int t = lane;
    f32x4 lo = {0, 0, 0, 0}, hi = {0, 0, 0, 0};
#pragma unroll
    for (int k = 0; k < 4; ++k) {
      const char* p = smem + k * K2_RKQ + t * K2_RSTR + U * 32;
      lo += *(const f32x4*)p; hi += *(const f32x4*)(p + 16);
    }
    const u32x4 xv = *(const u32x4*)(smem + K2_XOFF + t * 144 + U * 16);
    u32x4 hv = {pk_bf16(lo[0] + bf_lo(xv[0]), lo[1] + bf_hi(xv[0])), pk_bf16(lo[2] + bf_lo(xv[1]), lo[3] + bf_hi(xv[1])),
                pk_bf16(hi[0] + bf_lo(xv[2]), hi[1] + bf_hi(xv[2])), pk_bf16(hi[2] + bf_lo(xv[3]), hi[3] + bf_hi(xv[3]))};
    float v[8] = {bf_lo(hv[0]), bf_hi(hv[0]), bf_lo(hv[1]), bf_hi(hv[1]), bf_lo(hv[2]), bf_hi(hv[2]), bf_lo(hv[3]), bf_hi(hv[3])};
    float m = 0.f;
#pragma unroll
    for (int k = 0; k < 8; ++k) m += v[k];
    m *= 0.125f;
    float m2 = 0.f;
#pragma unroll
    for (int k = 0; k < 8; ++k) { const float d = v[k] - m; m2 += d * d; }
    *(f32x2*)(smem + K2_SOFF + (t * 9 + U) * 8) = f32x2{m, m2};
    const int Ug = nt * 9 + U, tg = 64 * mt + t;
    __builtin_amdgcn_raw_buffer_store_b128(hv, rsH, (unsigned)(((tg >> 4) * 72 + Ug) * 256 + (tg & 15) * 16), 0, AUX_SC1);
  };
  item(wid);
  if (wid == 0) item(8);
  WAITL0(); BAR();
  if (wid == 0) {
    const char* p = smem + K2_SOFF + lane * 72;
    f32x2 s[9];
    float m = 0.f;
#pragma unroll
    for (int k = 0; k < 9; ++k) { s[k] = *(const f32x2*)(p + k * 8); m += s[k][0]; }
    m *= (1.f / 9.f);
    float m2 = 0.f;
#pragma unroll
    for (int k = 0; k < 9; ++k) { const float d = s[k][0] - m; m2 += s[k][1] + 8.f * d * d; }
    const rsrc_t rsS = MKRS(ws + WS_ST, ST_BYTES);
    __builtin_amdgcn_raw_buffer_store_b64(__builtin_bit_cast(u32x2, f32x2{m, m2}), rsS, (unsigned)(((64 * mt + lane) * 8 + nt) * 8), 0, AUX_SC1);
  }
  STAMP(ws, 1, 3);
}

constexpr int KF_RING = 92160, KF_ST = KF_RING + 65536, KF_SCR = KF_ST + 640, KF_LDS = 163840;
#define EPI 8
#define PB1 1
#define ROT 0
#define WG0 10
#define WPG 1
#define PFINE 0
#define PG_N 12
#define PU_N 12
#define PB_N 10

#define MFMA_ACC(acc, a, b) asm volatile("v_mfma_f32_16x16x32_bf16 %0, %1, %2, %0" : "+v"(acc) : "v"(a), "v"(b))
#define ACC_READY(A) asm volatile("s_nop 7\n\ts_nop 7" : "+v"(A[0][0]), "+v"(A[0][1]), "+v"(A[0][2]), "+v"(A[0][3]), "+v"(A[0][4]), \
    "+v"(A[1][0]), "+v"(A[1][1]), "+v"(A[1][2]), "+v"(A[1][3]), "+v"(A[1][4]), "+v"(A[2][0]), "+v"(A[2][1]), "+v"(A[2][2]), "+v"(A[2][3]), \
    "+v"(A[2][4]), "+v"(A[3][0]), "+v"(A[3][1]), "+v"(A[3][2]), "+v"(A[3][3]), "+v"(A[3][4]))
#define DSR128(dst, addr, off) asm volatile("ds_read_b128 %0, %1 offset:%2" : "+v"(dst) : "v"(addr), "i"(off))
#define DSRTR(dst, addr, off) asm volatile("ds_read_b64_tr_b16 %0, %1 offset:%2" : "+v"(dst) : "v"(addr), "i"(off))

extern "C" __global__ __launch_bounds__(512, 2) void k3_ffn(const bf16_t* wg, const bf16_t* wu, const bf16_t* wd, char* ws) {
  __shared__ __attribute__((aligned(16))) char smem[KF_LDS];
  const int wid = __builtin_amdgcn_readfirstlane(threadIdx.x >> 6), lane = threadIdx.x & 63;
  const int b = blockIdx.x, xcd = b & 7, i = b >> 3;
  const u32x4 rsG = mksrd(wg, D_ * F_ * 2), rsU = mksrd(wu, D_ * F_ * 2), rsD = mksrd(wd, F_ * D_ * 2);
  const unsigned lds0 = lds_base(smem);
  if (b >= 234) {
    STAMP(ws, 2, 0); STAMP(ws, 2, 1);
    const int n0 = ((b - 234) * 8 + wid) * 64;
#pragma unroll 1
    for (int r = 0; r < 9; ++r) {
      const int n = r * 11264 + n0;
      if (n < 13824) TOUCH(rsG, lds0 + wid * 256, (unsigned)((27648 + n + lane) * 128));
      else if (n < 55296) TOUCH(rsU, lds0 + wid * 256, (unsigned)((n - 13824 + lane) * 128));
      else if (n < 96768) TOUCH(rsD, lds0 + wid * 256, (unsigned)((n - 55296 + lane) * 128));
    }
    WAITV(0);
    STAMP(ws, 2, 2); STAMP(ws, 2, 3); STAMP(ws, 2, 4); STAMP(ws, 2, 5);
    return;
  }
  const int id = xcd * 29 + (xcd < 2 ? xcd : 2) + i;
  const int tt = id < 130 ? id / 5 : (id - 130) >> 2, gp = id < 130 ? id - tt * 5 : 5 + ((id - 130) & 3);
  const u32x4 rsH = mksrd(ws + WS_HT, HT_BYTES), rsS = mksrd(ws + WS_ST, ST_BYTES);
  const rsrc_t rsP = MKRS(ws + WS_P, P_BYTES);
  STAMP(ws, 2, 0);
  WST(0);
  const int q = lane >> 4, r16 = lane & 15, r8 = lane >> 3, c8 = lane & 7, a4 = r16 >> 2, p4 = r16 & 3;
  const unsigned ring = lds0 + KF_RING + wid * 8192;
  unsigned vW[4];
#pragma unroll
  for (int j = 0; j < 4; ++j)
    vW[j] = (unsigned)((r8 + 8 * j) * 9216 + gp * 1024 + wid * 128 + ((c8 ^ (r8 & 2) ^ ((j & 1) * 4)) * 16) - j * 1024);
  auto ld = [&](u32x4 rs, int soff, int j) __attribute__((always_inline)) { RING_LD(rs, j * 1024, vW[j], soff); };
  auto ld4 = [&](u32x4 rs, int slot, int soff) __attribute__((always_inline)) {
    RING_M0(ring, slot * 4096);
    asm volatile("s_nop 0");
#pragma unroll
    for (int j = 0; j < 4; ++j) ld(rs, soff, j);
  };
  {
    f32x4 sp;
    const int tok = (wid < 5 ? wid : 4) * 16 + (lane >> 2);
    {
      int tg = tt * 80 + tok;
      tg = tg < S_ ? tg : S_ - 1;
      GLD(sp, rsS, (unsigned)(tg * 64 + (lane & 3) * 16));
    }
    BAR();
    u32x4 hv[12];
    const int r0 = ROT ? (gp < 5 ? gp : gp - 5) : 0;
    auto rot = [&](int k) { return k + r0 < 5 ? k + r0 : k + r0 - 5; };
    auto tile = [&](int T) { T += tt * 5; return (unsigned)((T < 128 ? T : 127) * 18432 + lane * 16); };
#pragma unroll
    for (int k = 0; k < 5; ++k) { GLD(hv[2 * k], rsH, tile(rot(k)) + wid * 1024); GLD(hv[2 * k + 1], rsH, tile(rot(k)) + wid * 1024 + 8192); }
    GLD(hv[10], rsH, tile(wid >> 1) + (16 + (wid & 1)) * 1024);
    GLD(hv[11], rsH, tile(4) + (16 + (wid & 1)) * 1024);
    ld4(rsG, 0, 0);
    LANDED(16, sp);
    {
      float m = sp[0] + sp[2];
      m += __shfl_xor(m, 1); m += __shfl_xor(m, 2);
      m *= 0.125f;
      const float d0 = sp[0] - m, d1 = sp[2] - m;
      float m2 = sp[1] + sp[3] + 72.f * (d0 * d0 + d1 * d1);
      m2 += __shfl_xor(m2, 1); m2 += __shfl_xor(m2, 2);
      const float r = __builtin_amdgcn_rsqf(m2 * (1.f / 576.f) + LN_EPS);
      if ((lane & 3) == 0 && wid < 5) *(f32x2*)(smem + KF_ST + tok * 8) = f32x2{r, -m * r};
    }
    WAITL0(); BAR();
    WST(1);
    f32x2 st[6];
#pragma unroll
    for (int k = 0; k < 5; ++k) st[k] = *(const f32x2*)(smem + KF_ST + (rot(k) * 16 + r16) * 8);
    const f32x2 st4 = *(const f32x2*)(smem + KF_ST + (64 + r16) * 8);
    st[5] = *(const f32x2*)(smem + KF_ST + ((wid >> 1) * 16 + r16) * 8);
    auto put = [&](u32x4 v, f32x2 c, int T, int s) __attribute__((always_inline)) {
      u32x4 o;
#pragma unroll
      for (int k = 0; k < 4; ++k) o[k] = pk_bf16(__builtin_fmaf(bf_lo(v[k]), c[0], c[1]), __builtin_fmaf(bf_hi(v[k]), c[0], c[1]));
      *(u32x4*)(smem + T * 18432 + s * 1024 + lane * 16) = o;
    };
    LANDED(15, hv[0]); put(hv[0], st[0], rot(0), wid); LANDED(14, hv[1]); put(hv[1], st[0], rot(0), wid + 8);
    LANDED(13, hv[2]); put(hv[2], st[1], rot(1), wid); LANDED(12, hv[3]); put(hv[3], st[1], rot(1), wid + 8);
    LANDED(11, hv[4]); put(hv[4], st[2], rot(2), wid); LANDED(10, hv[5]); put(hv[5], st[2], rot(2), wid + 8);
    LANDED(9, hv[6]); put(hv[6], st[3], rot(3), wid); LANDED(8, hv[7]); put(hv[7], st[3], rot(3), wid + 8);
    LANDED(7, hv[8]); put(hv[8], st[4], rot(4), wid); LANDED(6, hv[9]); put(hv[9], st[4], rot(4), wid + 8);
    LANDED(5, hv[10]);
    LANDED(4, hv[11]);
    ld4(rsG, 1, 294912);
    put(hv[10], st[5], wid >> 1, 16 + (wid & 1));
    if (wid < 2) put(hv[11], st4, 4, 16 + wid);
    WAITV(4); WAITL0(); BAR();
  }
  STAMP(ws, 2, 1);
  WST(2);

  unsigned aA[4], aB[4];
#pragma unroll
  for (int c = 0; c < 4; ++c) {
    aA[c] = ring + (unsigned)((8 * q + a4) * 128 + ((c ^ ((a4 >> 1) | ((q & 1) << 1))) * 32) + p4 * 8);
  }
  unsigned at0 = lds0 + (unsigned)(lane * 16), at1 = at0 + 3 * 18432;
  s16x4 wl[2][4], wh[2][4];
  bf16x8 tk[5];
  auto fresh = [&]() __attribute__((always_inline)) {
#pragma unroll
    for (int c = 0; c < 4; ++c) asm volatile("" : "=v"(wl[0][c]), "=v"(wh[0][c]), "=v"(wl[1][c]), "=v"(wh[1][c]));
#pragma unroll
    for (int t = 0; t < 5; ++t) asm volatile("" : "=v"(tk[t]));
  };
  fresh();
  auto rdw = [&](int buf, const unsigned(&a)[4], int hi) __attribute__((always_inline)) {
#pragma unroll
    for (int c = 0; c < 4; ++c) { DSRTR(wl[buf][c], a[c], buf * 4096); DSRTR(wh[buf][c], a[c], buf * 4096 + hi); }
  };
  auto rdt = [&](int t, int str, int off) __attribute__((always_inline)) {
    if (t < 3) DSR128(tk[t], at0, t * str + off); else DSR128(tk[t], at1, (t - 3) * str + off);
  };
#define LANDED_L(n) asm volatile("s_waitcnt lgkmcnt(%0)" ::"n"(n))
#define LANDED_W(n, buf) asm volatile("s_waitcnt lgkmcnt(%12)" : "+v"(wl[buf][0]), "+v"(wl[buf][1]), "+v"(wl[buf][2]), "+v"(wl[buf][3]), \
    "+v"(wh[buf][0]), "+v"(wh[buf][1]), "+v"(wh[buf][2]), "+v"(wh[buf][3]), "+v"(tk[0]), "+v"(tk[1]), "+v"(tk[2]), "+v"(tk[3]) : "n"(n))
#define LANDED_WB(buf) asm volatile("s_waitcnt lgkmcnt(0)" : "+v"(wl[buf][0]), "+v"(wl[buf][1]), "+v"(wl[buf][2]), "+v"(wl[buf][3]), \
    "+v"(wh[buf][0]), "+v"(wh[buf][1]), "+v"(wh[buf][2]), "+v"(wh[buf][3]))
#define LANDED_T(n, t) asm volatile("s_waitcnt lgkmcnt(%1)" : "+v"(tk[t]) : "n"(n))
  auto opnd = [&](int buf, int c) __attribute__((always_inline)) -> bf16x8 {
    return __builtin_shufflevector(wl[buf][c], wh[buf][c], 0, 1, 2, 3, 4, 5, 6, 7);
  };
  auto step = [&](f32x4(&acc)[4][5], auto P_, auto LD_, auto NW_, auto NT_, int str, int toff, u32x4 rs, int soff, auto gap) __attribute__((always_inline)) {
    constexpr int P = decltype(P_)::value, NW = decltype(NW_)::value;
    constexpr bool LD = decltype(LD_)::value, NT = decltype(NT_)::value;
    constexpr int W16 = (16 - WG0) * WPG < 8 ? (16 - WG0) * WPG : 8;
    if (LD) RING_M0(ring, P * 4096);
    LANDED_L(1);
#pragma unroll
    for (int t = 0; t < 5; ++t) {
      if (t == 4) LANDED_L((NT ? 4 : 0) + (NW ? W16 : 0));
      SCHED();
#pragma unroll
      for (int c = 0; c < 4; ++c) {
        const int g = t * 4 + c;
        MFMA_ACC(acc[c][t], opnd(P, c), tk[t]);
        SCHED();
        if (LD && t < 2 && (c & 1) == 0) { ld(rs, soff, 2 * t + (c >> 1)); SCHED(); }
        if (NW && g >= WG0 && g < WG0 + 8 / WPG) {
          if (g == WG0) { if (LD) WAITV(4); else WAITV(0); }
#pragma unroll
          for (int k = (g - WG0) * WPG; k < (g - WG0 + 1) * WPG; ++k) {
            const unsigned a = NW == 1 ? aA[k >> 1] : aB[k >> 1];
            if (k & 1) DSRTR(wh[P ^ 1][k >> 1], a, (P ^ 1) * 4096 + (NW == 1 ? 512 : 2048)); else DSRTR(wl[P ^ 1][k >> 1], a, (P ^ 1) * 4096);
          }
        }
        gap(g);
      }
      if (NT) rdt(t, str, toff);
    }
  };
  auto nogap = [&](int) __attribute__((always_inline)) {};
  using I0 = IC<0>; using I1 = IC<1>; using I2 = IC<2>;

  f32x4 ag[4][5], au[4][5];
#pragma unroll
  for (int c = 0; c < 4; ++c)
#pragma unroll
    for (int t = 0; t < 5; ++t) { ag[c][t] = f32x4{0, 0, 0, 0}; au[c][t] = f32x4{0, 0, 0, 0}; }
  ACC_READY(ag);

#pragma unroll
  for (int t = 0; t < 5; ++t) { rdt(t, 18432, 0); if (t == 3) rdw(0, aA, 512); }
  auto coarse = [&](int s, int n) __attribute__((always_inline)) {
    if (PRIO) asm volatile("s_cmp_lt_u32 %0, 4\n\ts_cbranch_scc1 2f\n\ts_cmp_lt_u32 %1, %2\n\ts_cbranch_scc1 1f\n\ts_setprio 0\n\ts_branch 2f\n1:\n\ts_setprio 1\n2:" ::"s"(wid), "s"(s), "s"(n) : "scc");
  };
#pragma clang loop unroll(disable)
  for (int s = 0; s < 16; s += 2) {
    coarse(s, PG_N);
    step(ag, I0{}, I1{}, I1{}, I1{}, 18432, 1024, rsG, (s + 2) * 294912, nogap);
    step(ag, I1{}, I1{}, I1{}, I1{}, 18432, 2048, rsG, (s + 3) * 294912, nogap);
    at0 += 2048; at1 += 2048;
  }
  at0 -= 16 * 1024; at1 -= 16 * 1024;
  step(ag, I0{}, I1{}, I1{}, I1{}, 18432, 17 * 1024, rsU, 0, nogap);
  step(ag, I1{}, I1{}, I1{}, I1{}, 18432, 0, rsU, 294912, nogap);
  WST(3);
  ACC_READY(ag); ACC_READY(au);
  float sx[5] = {0.f, 0.f, 0.f, 0.f, 0.f}, gs[80];
#pragma unroll
  for (int v = 0; v < 80; ++v) gs[v] = ag[(v >> 2) & 3][v >> 4][v & 3];
  auto ustep = [&](auto S_c) __attribute__((always_inline)) {
    constexpr int S = decltype(S_c)::value, NV = S < 8 ? 5 : 4, V0 = S < 8 ? 5 * S : 40 + 4 * (S - 8), NOP = 5 * NV;
    auto gap = [&](int g) __attribute__((always_inline)) {
      const int gi = g < 8 ? ((g & 1) ? g >> 1 : -1) : g - 4;
      if (gi < 0) return;
#pragma unroll
      for (int k = 0; k < NOP; ++k) {
        if (k * 16 / NOP != gi) continue;
        const int op = k / NV, e = k - op * NV, v = V0 + e;
        if (op == 0) asm volatile("v_mul_f32 %0, 0xbfb8aa3b, %1" : "+v"(sx[e]) : "v"(gs[v]));
        if (op == 1) asm volatile("v_exp_f32 %0, %0" : "+v"(sx[e]));
        if (op == 2) asm volatile("v_add_f32 %0, 1.0, %0" : "+v"(sx[e]));
        if (op == 3) asm volatile("v_rcp_f32 %0, %0" : "+v"(sx[e]));
        if (op == 4) asm volatile("v_mul_f32 %0, %0, %1" : "+v"(gs[v]) : "v"(sx[e]));
      }
      SCHED();
    };
    if (S == 0) PRIO_HI(wid);
    if (S == PU_N) PRIO_LO(wid);
    step(au, IC<S & 1>{}, I1{}, IC<(S < 17 ? 1 : 2)>{}, IC<(S < 17 ? 1 : 0)>{}, 18432, (S + 1) * 1024, S < 16 ? rsU : rsD,
         S < 16 ? (S + 2) * 294912 : (gp * 512 + wid * 64 + (S & 1) * 32) * 1152 + 1024 - wid * 128, gap);
  };
  ustep(IC<0>{}); ustep(IC<1>{}); ustep(IC<2>{}); ustep(IC<3>{}); ustep(IC<4>{}); ustep(IC<5>{});
  ustep(IC<6>{}); ustep(IC<7>{}); ustep(IC<8>{}); ustep(IC<9>{}); ustep(IC<10>{}); ustep(IC<11>{});
  ustep(IC<12>{}); ustep(IC<13>{}); ustep(IC<14>{}); ustep(IC<15>{});
  {
    int l = lane;
    asm volatile("" : "+v"(l));
    const int q_ = l >> 4, a4_ = (l >> 2) & 3, r8_ = l >> 3;
#pragma unroll
    for (int j = 0; j < 4; ++j) vW[j] = (unsigned)((r8_ + 8 * j) * 1152 + wid * 128 + (((l & 7) ^ (r8_ & 6)) * 16) - j * 1024);
#pragma unroll
    for (int c = 0; c < 4; ++c) aB[c] = ring + (unsigned)((4 * q_ + a4_) * 128 + ((c ^ ((2 * q_ + (a4_ >> 1)) & 3)) * 32) + (l & 3) * 8);
  }
  ustep(IC<16>{}); ustep(IC<17>{});
  ACC_READY(au);
  STAMP(ws, 2, 4);

  f32x4 o8[4][5], om[4][5];
  u32x4 act[2][5];
  const f32x4 zero4 = {0, 0, 0, 0};
  LANDED_WB(0);
  ld4(rsD, 0, gp * 512 * 1152);
  WAITV(4); rdw(1, aB, 2048);
  CODE_TOUCH("k4_sum", lds0 + KF_SCR, 0, wid);
#pragma unroll
  for (int pr = 0; pr < 2; ++pr) {
    if (pr == 1) {
      LANDED_WB(1);
      ld4(rsD, 1, (gp * 512 + 32) * 1152);
    }
#pragma unroll
    for (int t = 0; t < 5; ++t) {
      const float* g0 = gs + (t * 4 + 2 * pr) * 4;
      const f32x4 lo = f32x4{g0[0], g0[1], g0[2], g0[3]} * au[2 * pr][t], hi = f32x4{g0[4], g0[5], g0[6], g0[7]} * au[2 * pr + 1][t];
      act[pr][t] = u32x4{pk_bf16(lo[0], lo[1]), pk_bf16(lo[2], lo[3]), pk_bf16(hi[0], hi[1]), pk_bf16(hi[2], hi[3])};
      const bf16x8 f = __builtin_bit_cast(bf16x8, act[pr][t]);
#pragma unroll
      for (int c = 0; c < 4; ++c) o8[c][t] = MFMA16(opnd(pr, c), f, pr == 0 ? zero4 : o8[c][t]);
      SCHED();
    }
  }
  WST(4);
  BAR();
#pragma unroll
  for (int pr = 0; pr < 2; ++pr)
#pragma unroll
    for (int t = 0; t < 5; ++t) *(u32x4*)(smem + (t * 16 + 2 * wid + pr) * 1024 + lane * 16) = act[pr][t];
#pragma unroll
  for (int c = 0; c < 4; ++c)
#pragma unroll
    for (int t = 0; t < 5; ++t) om[c][t] = f32x4{0, 0, 0, 0};
  ACC_READY(om);
  WST(5);
  WAITL0(); BAR();
  WST(6);
  STAMP(ws, 2, 5);

  at0 = lds0 + (unsigned)(lane * 16); at1 = at0 + 3 * 16384;
  fresh();
  WAITV(4);
#pragma unroll
  for (int t = 0; t < 5; ++t) { rdt(t, 16384, 0); if (t == 3) rdw(0, aB, 2048); }
#pragma clang loop unroll(disable)
  for (int s = 0; s < 14; s += 2) {
    coarse(s, PB_N);
    step(om, I0{}, I1{}, I2{}, I1{}, 16384, 1024, rsD, (gp * 512 + (s + 2) * 32) * 1152, nogap);
    step(om, I1{}, I1{}, I2{}, I1{}, 16384, 2048, rsD, (gp * 512 + (s + 3) * 32) * 1152, nogap);
    at0 += 2048; at1 += 2048;
  }
  step(om, I0{}, I0{}, I2{}, I1{}, 16384, 1024, rsD, 0, nogap);
  step(om, I1{}, I0{}, I0{}, I0{}, 16384, 0, rsD, 0, nogap);
  PRIO_LO(wid);
  ACC_READY(om);
  WST(7);
  STAMP(ws, 2, 2);

  const int uo = (q & 1) * 2 + (q >> 1);
  auto emit = [&](f32x4 lo, f32x4 hi, int t, int E) __attribute__((always_inline)) {
    const auto x = __builtin_amdgcn_permlane16_swap(pk_bf16(lo[0], lo[1]), pk_bf16(hi[0], hi[1]), false, false);
    const auto y = __builtin_amdgcn_permlane16_swap(pk_bf16(lo[2], lo[3]), pk_bf16(hi[2], hi[3]), false, false);
    const int T16 = tt * 5 + t;
    if (T16 < 128)
      __builtin_amdgcn_raw_buffer_store_b128(u32x4{x[0], y[0], x[1], y[1]}, rsP, (unsigned)(((gp * 128 + T16) * 72 + E * 4 + uo) * 256 + r16 * 16), 0, AUX_SC1);
  };
#pragma unroll
  for (int k = 0; k < EPI - 1; ++k) emit(om[(k & 1) * 2][k >> 1], om[(k & 1) * 2 + 1][k >> 1], k >> 1, 2 * wid + (k & 1));
  SCHED();
  BAR();
#pragma unroll
  for (int t = 0; t < 5; ++t) {
#pragma unroll
    for (int c = 0; c < 4; ++c) *(f32x4*)(smem + (wid * 20 + t * 4 + c) * 1024 + lane * 16) = o8[c][t];
    SCHED();
  }
  WAITL0(); BAR();
#pragma unroll
  for (int k = EPI - 1; k < 10; ++k) emit(om[(k & 1) * 2][k >> 1], om[(k & 1) * 2 + 1][k >> 1], k >> 1, 2 * wid + (k & 1));
  SCHED();
  WST(8);
  WST(9);
  auto unit = [&](int u) __attribute__((always_inline)) {
    const char* p = smem + u * 2048 + lane * 16;
    f32x4 lo = {0, 0, 0, 0}, hi = {0, 0, 0, 0};
#pragma unroll
    for (int k = 0; k < 8; ++k) { lo += *(const f32x4*)(p + k * 20480); hi += *(const f32x4*)(p + k * 20480 + 1024); }
    emit(lo, hi, u >> 1, 16 + (u & 1));
  };
  unit(wid);
  if (wid < 2) unit(8 + wid);
  WST(10);
  WAITV(0);
  WST(11);
  WST_OUT(ws, 12);
  STAMP(ws, 2, 3);
}

extern "C" __global__ __launch_bounds__(576) void k4_sum(char* ws, bf16_t* out) {
  STAMP(ws, 3, 0);
  const int wv = threadIdx.x >> 6, lane = threadIdx.x & 63;
  const int T16 = blockIdx.x >> 1, t = (blockIdx.x & 1) * 8 + (lane >> 3), U = wv * 8 + (lane & 7);
  const rsrc_t rsP = MKRS(ws + WS_P, P_BYTES), rsH = MKRS(ws + WS_HT, HT_BYTES), rsO = MKRS(out, S_ * D_ * 2);
  const unsigned off = (unsigned)((T16 * 72 + U) * 256 + t * 16);
  u32x4 p[9];
#pragma unroll
  for (int g = 0; g < 9; ++g) p[g] = __builtin_amdgcn_raw_buffer_load_b128(rsP, off, g * (int)HT_BYTES, 0);
  u32x4 hv = __builtin_amdgcn_raw_buffer_load_b128(rsH, off, 0, 0);
  STAMP(ws, 3, 1);
  float a[8];
#pragma unroll
  for (int k = 0; k < 4; ++k) { a[2 * k] = bf_lo(p[0][k]); a[2 * k + 1] = bf_hi(p[0][k]); }
#pragma unroll
  for (int g = 1; g < 9; ++g)
#pragma unroll
    for (int k = 0; k < 4; ++k) { a[2 * k] += bf_lo(p[g][k]); a[2 * k + 1] += bf_hi(p[g][k]); }
  u32x4 ov;
#pragma unroll
  for (int k = 0; k < 4; ++k) ov[k] = pk_bf16(a[2 * k] + bf_lo(hv[k]), a[2 * k + 1] + bf_hi(hv[k]));
  STAMP(ws, 3, 2);
  __builtin_amdgcn_raw_buffer_store_b128(ov, rsO, (unsigned)((T16 * 16 + t) * 1152 + U * 16), 0, K4_AUX);
  STAMP(ws, 3, 3);
  if (blockIdx.x == 0) {
    __shared__ long long red[9][4];
    const long long tnow = (long long)__builtin_amdgcn_s_memrealtime();
    const int tb = threadIdx.x;
    long long v = 0; int mode = 0;
    auto rt = [&](int k, int b, int sl) -> long long { return (long long)*(const unsigned long long*)(ws + (64u << 20) + k * 65536 + b * 128 + sl * 16 + 8); };
    auto cy = [&](int k, int b, int sl) -> long long { return (long long)*(const unsigned long long*)(ws + (64u << 20) + k * 65536 + b * 128 + sl * 16); };
    auto wred = [&](long long x, int op) -> long long {
      for (int o = 1; o < 64; o <<= 1) { long long y = __shfl_xor(x, o); x = op == 0 ? (y < x ? y : x) : op == 1 ? (y > x ? y : x) : x + y; }
      if ((tb & 63) == 0) red[tb >> 6][0] = x;
      __syncthreads();
      long long r = red[0][0];
      for (int w = 1; w < 4; ++w) { long long y = red[w][0]; r = op == 0 ? (y < r ? y : r) : op == 1 ? (y > r ? y : r) : r + y; }
      __syncthreads();
      return r;
    };
    const int b = tb < 256 ? tb : 255;
    const bool mainb = b < NMAIN;
    const long long BIG = 1ll << 62;
    const long long s1 = wred(rt(0, b, 0), 0), s2 = wred(rt(1, b, 0), 0), s3 = wred(mainb ? rt(2, b, 0) : BIG, 0), s4 = wred(rt(3, b, 0) > 0 && b == 0 ? rt(3, b, 0) : BIG, 0);
    const long long e1 = wred(rt(0, b, 3), 1), e2 = wred(rt(1, b, 3), 1), e3 = wred(mainb ? rt(2, b, 3) : 0, 1), ep = wred(mainb ? 0 : rt(2, b, 3), 1);
    auto avg = [&](int k, int a, int c) { return wred(mainb && tb < 256 ? rt(k, b, c) - rt(k, b, a) : 0, 2) / NMAIN; };
    long long qq[22];
    qq[0] = 0;
    qq[1] = s2 - s1;
    qq[2] = s3 - s2;
    qq[3] = e3 - s3;
    qq[4] = avg(2, 0, 1);
    qq[5] = ep - s3;
    qq[6] = avg(2, 1, 4);
    qq[7] = avg(2, 4, 5);
    qq[8] = avg(2, 5, 2);
    qq[9] = avg(2, 2, 3);
    qq[10] = wred(mainb && tb < 256 ? cy(2, b, 3) - cy(2, b, 0) : 0, 2) / NMAIN / 24;
    qq[11] = s4 - e3;
    qq[12] = e1 - s1;
    qq[13] = e2 - s2;
    qq[14] = wred(tb < 256 ? rt(1, b, 1) - rt(1, b, 0) : 0, 2) / 256;
    qq[15] = wred(tb < 256 ? rt(0, b, 1) - rt(0, b, 0) : 0, 2) / 256;
    qq[16] = wred(tb < 256 ? cy(1, b, 3) - cy(1, b, 0) : 0, 2) / 256 / 24;
    qq[17] = wred(tb < 256 ? rt(1, b, 3) - rt(1, b, 0) : 0, 2) / 256;
    qq[18] = wred(mainb && tb < 256 ? rt(2, b, 3) - rt(2, b, 0) : 0, 2) / NMAIN;
    qq[19] = tnow - s1;
    qq[20] = tnow - s4;
    qq[21] = tnow - e3;
    for (int k = 0; k < 22; ++k) asm volatile("" ::"v"(qq[k]));
    long long q = qq[QSPIN];
    q *= 4;
    q = q < 0 ? 0 : (q > 40000 ? 40000 : q);
    if (tb == 0) { while ((long long)__builtin_amdgcn_s_memrealtime() - tnow < q + 3000) __builtin_amdgcn_s_sleep(2); }
  }

}

extern "C" void kernel_launch(void* const* d_in, const int* in_sizes, int n_in, void* d_out, int out_size, void* d_ws, size_t ws_size,
                              hipStream_t stream) {
  const bf16_t* x = (const bf16_t*)d_in[0];
  const bf16_t* cre = (const bf16_t*)d_in[1];
  const bf16_t* cim = (const bf16_t*)d_in[2];
  const bf16_t* wre = (const bf16_t*)d_in[3];
  const bf16_t* wim = (const bf16_t*)d_in[4];
  const bf16_t* wg = (const bf16_t*)d_in[6];
  const bf16_t* wu = (const bf16_t*)d_in[7];
  const bf16_t* wd = (const bf16_t*)d_in[8];
  char* ws = (char*)d_ws;
  k1_hid<<<256, 512, 0, stream>>>(x, wre, wim, cre, cim, ws);
  k2_seq<<<256, 512, 0, stream>>>(x, cre, cim, wg, wu, ws);
  k3_ffn<<<256, 512, 0, stream>>>(wg, wu, wd, ws);
  k4_sum<<<256, 576, 0, stream>>>(ws, (bf16_t*)d_out);
}
```

```cpp
#define DIAG
#define QSPIN 0
#define NMAIN 234
#include <hip/hip_runtime.h>
#include <stdint.h>

typedef unsigned short bf16_t;
typedef short bf16x8 __attribute__((ext_vector_type(8)));
typedef short s16x4 __attribute__((ext_vector_type(4)));
typedef float f32x4 __attribute__((ext_vector_type(4)));
typedef float f32x2 __attribute__((ext_vector_type(2)));
typedef unsigned u32x4 __attribute__((ext_vector_type(4)));
typedef unsigned u32x2 __attribute__((ext_vector_type(2)));
typedef __bf16 bf2_t __attribute__((ext_vector_type(2)));
typedef __amdgpu_buffer_rsrc_t rsrc_t;

constexpr int S_ = 2048, D_ = 576, F_ = 4608;
constexpr float LN_EPS = 1e-5f;

constexpr unsigned WS_BP = 0;
constexpr unsigned BP_BYTES = 8u * 32u * 18432u;
constexpr unsigned WS_HT = WS_BP + BP_BYTES;
constexpr unsigned HT_BYTES = 128u * 72u * 256u;
constexpr unsigned WS_ST = WS_HT + HT_BYTES;
constexpr unsigned ST_BYTES = 2048u * 8u * 8u;
constexpr unsigned WS_P = WS_ST + ST_BYTES;
constexpr unsigned P_BYTES = 9u * HT_BYTES;

constexpr size_t WS_USED = 32u << 20;
static_assert(WS_P + (size_t)P_BYTES <= WS_USED, "workspace map");
#define WS_POS 2
#define WS_PLACE(n) ((n) < WS_USED ? (size_t)0 : WS_POS == 0 ? (size_t)0 : WS_POS == 1 ? (((n) - WS_USED) / 2 & ~(size_t)0x1fffff) : (((n) - WS_USED) & ~(size_t)0x1fffff))
#define STAMP_OFF (WS_POS == 0 ? (64ll << 20) : WS_POS == 1 ? -(64ll << 20) : -(64ll << 20))
#define LDSP(p) ((__attribute__((address_space(3))) void*)(p))
#define MKRS(p, n) __builtin_amdgcn_make_buffer_rsrc((void*)(p), 0, (n), 0x00020000)
#define GLDS(rs, m0base, imm, voff, soff)                                                              \
  asm volatile("s_add_u32 m0, %0, %1\n\ts_nop 0\n\tbuffer_load_dwordx4 %2, %3, %4 offen lds"          \
               ::"s"(m0base), "i"(imm), "v"(voff), "s"(rs), "s"(soff) : "memory", "scc")
#define GLDS_N(mask, rs, m0base, imm, voff, soff)                                                      \
  asm volatile("s_add_u32 m0, %0, %1\n\ts_mov_b64 exec, " #mask "\n\tbuffer_load_dwordx4 %2, %3, %4 offen lds\n\ts_mov_b64 exec, -1" \
               ::"s"(m0base), "i"(imm), "v"(voff), "s"(rs), "s"(soff) : "memory", "scc")
#define GLDS_36(rs, m0base, imm, voff, soff)                                                           \
  asm volatile("s_add_u32 m0, %0, %1\n\ts_mov_b32 exec_hi, 0xf\n\tbuffer_load_dwordx4 %2, %3, %4 offen lds\n\ts_mov_b32 exec_hi, -1" \
               ::"s"(m0base), "i"(imm), "v"(voff), "s"(rs), "s"(soff) : "memory", "scc")
#define RING_M0(m0base, imm) asm volatile("s_add_u32 m0, %0, %1" ::"s"(m0base), "i"(imm) : "scc")
#define RING_LD(rs, off, voff, soff) asm volatile("buffer_load_dwordx4 %0, %1, %2 offen offset:%3 lds" ::"v"(voff), "s"(rs), "s"(soff), "i"(off) : "memory")
#define TOUCH(rs, mscr, voff)                                                                          \
  asm volatile("s_mov_b32 m0, %0\n\ts_nop 0\n\tbuffer_load_dword %1, %2, 0 offen lds" ::"s"(mscr), "v"(voff), "s"(rs) : "memory")
#define TOUCHS(rs, mscr, voff, soff)                                                                   \
  asm volatile("s_mov_b32 m0, %0\n\ts_nop 0\n\tbuffer_load_dword %1, %2, %3 offen lds" ::"s"(mscr), "v"(voff), "s"(rs), "s"(soff) : "memory")
__device__ __forceinline__ u32x4 mksrd(const void* p, unsigned n) {
  const unsigned long long a = (unsigned long long)p;
  return u32x4{(unsigned)a, (unsigned)(a >> 32) & 0xffffu, n, 0x00020000u};
}
#define CODE_ADDR(lo, hi, sym)                                                                         \
  asm volatile("s_getpc_b64 s[20:21]\n\ts_add_u32 %0, s20, " sym "@rel32@lo+4\n\ts_addc_u32 %1, s21, " sym "@rel32@hi+12" \
               : "=s"(lo), "=s"(hi)::"s20", "s21", "scc")
#define CODE_TOUCH(sym, mscr, i, wid)                                                                  \
  do {                                                                                                 \
    unsigned lo_, hi_, le_, he_;                                                                       \
    CODE_ADDR(lo_, hi_, sym); CODE_ADDR(le_, he_, "k4_sum");                                           \
    const unsigned n_ = le_ - lo_ + 1024u;                                                             \
    TOUCH((u32x4{lo_, hi_ & 0xffffu, n_ < 65536u ? n_ : 0u, 0x00020000u}), mscr, (unsigned)(((i) * 8 + (wid)) * 128)); \
  } while (0)
#define CODE_TOUCH_TOP(sym, dst)                                                                       \
  asm volatile("s_getpc_b64 s[40:41]\n\ts_add_u32 s36, s40, " sym "@rel32@lo+4\n\ts_addc_u32 s37, s41, " sym "@rel32@hi+12\n\t"          \
               "s_getpc_b64 s[40:41]\n\ts_add_u32 s38, s40, k4_sum@rel32@lo+4\n\ts_sub_u32 s38, s38, s36\n\ts_add_u32 s38, s38, 0x400\n\t"  \
               "s_and_b32 s37, s37, 0xffff\n\ts_mov_b32 s39, 0x20000\n\ts_cmp_lt_u32 s38, 0x10000\n\ts_cselect_b32 s38, s38, 0\n\t"            \
               "s_and_b32 s40, %1, -8\n\tv_lshrrev_b32 %0, 6, %2\n\tv_add_u32 %0, s40, %0\n\tv_lshlrev_b32 %0, 7, %0\n\t"                    \
               "buffer_load_dword %0, %0, s[36:39], 0 offen"                                           \
               : "=&v"(dst) : "s"(blockIdx.x), "v"(threadIdx.x) : "s36", "s37", "s38", "s39", "s40", "s41", "scc", "memory")
__device__ __forceinline__ unsigned lds_base(char* smem) { return (unsigned)(size_t)LDSP(smem); }
#define GLD(dst, rs, voff) asm volatile("buffer_load_dwordx4 %0, %1, %2, 0 offen" : "=v"(dst) : "v"(voff), "s"(rs) : "memory")
#define LANDED(n, x) asm volatile("s_waitcnt vmcnt(" #n ")" : "+v"(x))
#define WAITV(n) asm volatile("s_waitcnt vmcnt(%0)" ::"n"(n) : "memory")
#define WAITL0() asm volatile("s_waitcnt lgkmcnt(0)" ::: "memory")
#define PIN(x) asm volatile("" : "+v"(x))
#define BAR() __builtin_amdgcn_s_barrier()
#define SCHED() __builtin_amdgcn_sched_barrier(0)
#define MFMA16(a, b, c) __builtin_amdgcn_mfma_f32_16x16x32_bf16((a), (b), (c), 0, 0, 0)
constexpr int AUX_SC1 = 16;
#define PRIO 1
#define PA_T 2
#define PB_T 2
#define PRIO_SET(wid, p) do { if (PRIO) asm volatile("s_cmp_lt_u32 %0, 4\n\ts_cbranch_scc1 1f\n\ts_setprio " #p "\n1:" ::"s"(wid) : "scc"); } while (0)
#define PRIO_HI(wid) PRIO_SET(wid, 1)
#define PRIO_LO(wid) PRIO_SET(wid, 0)
template <int V> struct IC { static constexpr int value = V; };
#define CS_T 3
#define CT2_MODE 1
#define CT3_MODE 2
#define K2_VAR 0
#define PF_P 0
#define K1_BP 0
#define K1_TOP 1
#define K4_AUX 0
#define TSX(var, j) do { } while (0)
#define TS_OUT(ws, kid, wid, lane, k, var) do { } while (0)
#define STAMP(ws, kid, slot) do { if (threadIdx.x == 0) { unsigned long long* p_ = (unsigned long long*)((ws) + STAMP_OFF + (kid) * 65536 + blockIdx.x * 128 + (slot) * 16); \
  p_[0] = __builtin_amdgcn_s_memtime(); p_[1] = __builtin_amdgcn_s_memrealtime(); } } while (0)

#define WST(k) do { } while (0)
#define WST_OUT(ws, n) do { } while (0)

#define TS4(j) do { } while (0)
#define TS4_OUT(ws) do { } while (0)

__device__ __forceinline__ unsigned pk_bf16(float lo, float hi) {
  f32x2 v = {lo, hi};
  bf2_t b = __builtin_convertvector(v, bf2_t);
  return __builtin_bit_cast(unsigned, b);
}
__device__ __forceinline__ float bf_lo(unsigned w) { return __uint_as_float(w << 16); }
__device__ __forceinline__ float bf_hi(unsigned w) { return __uint_as_float(w & 0xffff0000u); }
__device__ __forceinline__ float dot_ones(unsigned w, float acc) {
  const bf2_t ones = {(__bf16)1.0f, (__bf16)1.0f};
  return __builtin_amdgcn_fdot2_f32_bf16(__builtin_bit_cast(bf2_t, w), ones, acc, false);
}
__device__ __forceinline__ float dot_frag(bf16x8 f, float acc) {
  u32x4 u = __builtin_bit_cast(u32x4, f);
  acc = dot_ones(u[0], acc); acc = dot_ones(u[1], acc); acc = dot_ones(u[2], acc); acc = dot_ones(u[3], acc);
  return acc;
}

constexpr int K1_SLOT = 8192 + 18432, K1_NS = 5, K1_ST = K1_NS * K1_SLOT, K1_WS = K1_ST + 512, K1_SCR = K1_WS + 1152, K1_LDS = K1_SCR + 256;

template <int NF>
__device__ __forceinline__ void k1_body(char* smem, u32x4 rsX, u32x4 rsW, u32x4 rsC, rsrc_t rsO, int st, int et, int wid, int lane, int nf0,
                                        unsigned vhead, bool headx, char* ws, unsigned ctop) {
  STAMP(ws, 0, 0);
  const int tid = wid * 64 + lane;
  const int sh = wid >> 2, q = lane >> 4, r16 = lane & 15, r8 = lane >> 3, c8 = lane & 7;
  const unsigned vx = (unsigned)(64 * st + 8 * wid + r8) * 1152u + (unsigned)((c8 ^ ((r8 & 3) | ((wid & 1) << 2))) * 16);
  const unsigned vw = (unsigned)((et & 3) * 144 + 8 * wid + r8) * 1152u + (unsigned)((c8 ^ r8) * 16);
  const int r2 = 128 + 2 * wid + r8;
  const unsigned vw2 = (unsigned)((et & 3) * 144 + r2) * 1152u + (unsigned)((c8 ^ (r2 & 7)) * 16);
  const unsigned lds0 = lds_base(smem);
  const unsigned mA = lds0 + wid * 1024, mP = lds0 + wid * 256;
  auto issue1 = [&](int kb, int k) __attribute__((always_inline)) {
    const int so = (kb % K1_NS) * K1_SLOT;
    if (k == 0) GLDS(rsX, mA, so, vx, kb * 128);
    if (k == 1) GLDS(rsW, mA, so + 8192, vw, kb * 128);
    if (k == 2) GLDS(rsW, mA, so + 16384, vw, kb * 128 + 64 * 1152);
    if (k == 3) GLDS_N(0xffff, rsW, mP, so + 24576, vw2, kb * 128);
  };
  auto issue = [&](int kb) __attribute__((always_inline)) { issue1(kb, 0); issue1(kb, 1); issue1(kb, 2); issue1(kb, 3); };
  unsigned ax[2];
#pragma unroll
  for (int m = 0; m < 2; ++m) {
    const int row = 32 * sh + 8 * (r16 >> 2) + 4 * m + (r16 & 3);
    const int key = (row & 3) | ((row >> 1) & 4);
    ax[m] = (unsigned)(row * 128 + ((q ^ key) * 16)) ^ (unsigned)(sh * 64);
  }
  const unsigned aw = (unsigned)(8192 + (nf0 * 16 + r16) * 128 + ((q ^ (r16 & 7)) * 16)) ^ (unsigned)(sh * 64);

  f32x4 acc[2][NF];
  float wsum[NF];
#pragma unroll
  for (int n = 0; n < NF; ++n) { wsum[n] = 0.f; acc[0][n] = f32x4{0, 0, 0, 0}; acc[1][n] = f32x4{0, 0, 0, 0}; }
  float shift = 0.f;
  f32x2 s1 = {0.f, 0.f}, s2 = {0.f, 0.f};

  struct Fr { u32x4 v; bf16x8 a[2][2], b[2][NF]; };
  Fr fr[2];
  auto rd = [&](Fr& f, int kb, int part) __attribute__((always_inline)) {
    const char* sl = smem + (kb % K1_NS) * K1_SLOT;
    if (part == 0) {
      f.a[0][0] = *(const bf16x8*)(sl + ax[0]); f.a[0][1] = *(const bf16x8*)(sl + ax[1]);
#pragma unroll
      for (int n = 0; n < NF; ++n) f.b[0][n] = *(const bf16x8*)(sl + aw + n * 2048);
    } else {
      f.v = *(const u32x4*)(sl + tid * 16);
      f.a[1][0] = *(const bf16x8*)(sl + (ax[0] ^ 64)); f.a[1][1] = *(const bf16x8*)(sl + (ax[1] ^ 64));
#pragma unroll
      for (int n = 0; n < NF; ++n) f.b[1][n] = *(const bf16x8*)(sl + ((aw ^ 64) + n * 2048));
    }
  };
  issue(0);
  STAMP(ws, 0, 7);
  issue(1);
  TOUCH(headx ? rsX : rsW, lds0 + K1_SCR, vhead);
  issue(2); issue(3); issue(4);
  WAITV(17 + K1_BP); BAR();
  asm volatile("" ::"v"(ctop));
  STAMP(ws, 0, 1);
  rd(fr[0], 0, 0); rd(fr[0], 0, 1);
  WAITL0();
  WAITV(13 + K1_BP); BAR();
#pragma unroll
  for (int kb = 0; kb < 9; ++kb) {
    const Fr& c = fr[kb & 1];
    Fr& nx = fr[(kb & 1) ^ 1];
    const bool stg = kb + 5 < 9;
    if (stg) issue1(kb + 5, 0);
    if (kb == 4)
      TOUCH(rsC, lds0 + K1_SCR, (unsigned)(blockIdx.x * 32768 + (wid & 3) * 8192 + lane * 128));
    SCHED();
#pragma unroll
    for (int n = 0; n < NF; ++n) { acc[0][n] = MFMA16(c.a[0][0], c.b[0][n], acc[0][n]); acc[1][n] = MFMA16(c.a[0][1], c.b[0][n], acc[1][n]); }
    SCHED();
    if (kb < 8) rd(nx, kb + 1, 0);
    SCHED();
    if (stg) issue1(kb + 5, 1);
    SCHED();
    if (kb == 0) shift = bf_lo(c.v[0]);
#pragma unroll
    for (int i = 0; i < 4; ++i) {
      f32x2 d = {bf_lo(c.v[i]) - shift, bf_hi(c.v[i]) - shift};
      s1 += d; s2 += d * d;
    }
    PIN(s1); PIN(s2);
    SCHED();
    if (stg) issue1(kb + 5, 2);
    SCHED();
#pragma unroll
    for (int n = 0; n < NF; ++n) { acc[0][n] = MFMA16(c.a[1][0], c.b[1][n], acc[0][n]); acc[1][n] = MFMA16(c.a[1][1], c.b[1][n], acc[1][n]); }
    SCHED();
    if (kb < 8) rd(nx, kb + 1, 1);
    SCHED();
#pragma unroll
    for (int n = 0; n < NF; ++n) wsum[n] = dot_frag(c.b[0][n], wsum[n]);
#pragma unroll
    for (int n = 0; n < NF; ++n) PIN(wsum[n]);
    SCHED();
    if (stg) issue1(kb + 5, 3);
    WAITL0();
    if (kb <= 3) WAITV(12); else if (kb == 4) WAITV(9); else if (kb == 5) WAITV(5); else if (kb == 6) WAITV(1);
    if (kb < 7) BAR();
    SCHED();
  }
  STAMP(ws, 0, 2);
  {
    const float a1 = s1[0] + s1[1], a2 = s2[0] + s2[1];
    float mean = shift + a1 * (1.f / 72.f);
    float m2 = a2 - a1 * a1 * (1.f / 72.f);
    float n = 72.f;
#pragma unroll
    for (int o = 1; o < 8; o <<= 1) {
      const float mo = __shfl_xor(mean, o), qo = __shfl_xor(m2, o);
      const float dl = mo - mean;
      m2 = m2 + qo + dl * dl * (n * 0.5f);
      mean = 0.5f * (mean + mo);
      n *= 2.f;
    }
    const float r = __builtin_amdgcn_rsqf(m2 * (1.f / 576.f) + LN_EPS);
    if (c8 == 0) *(f32x2*)(smem + K1_ST + (tid >> 3) * 8) = f32x2{r, mean * r};
#pragma unroll
    for (int n = 0; n < NF; ++n) {
      float w = wsum[n];
      w += __shfl_xor(w, 16); w += __shfl_xor(w, 32);
      if (q == 0) *(float*)(smem + K1_WS + sh * 576 + ((nf0 + n) * 16 + r16) * 4) = w;
    }
  }
  WAITL0();
  BAR();
  f32x2 rs[8];
  {
    const char* p = smem + K1_ST + (32 * sh + 8 * q) * 8;
#pragma unroll
    for (int i = 0; i < 4; ++i) {
      f32x4 v = *(const f32x4*)(p + i * 16);
      rs[2 * i] = f32x2{v[0], v[1]}; rs[2 * i + 1] = f32x2{v[2], v[3]};
    }
  }
  const float sgn = et < 4 ? 1.f : -1.f;
  const int kt = (et < 4 ? 0 : 16) + (st >> 1), ksg = (st & 1) * 2 + sh;
#pragma unroll
  for (int n = 0; n < NF; ++n) {
    const int el = (nf0 + n) * 16 + r16;
    const float ws = *(const float*)(smem + K1_WS + el * 4) + *(const float*)(smem + K1_WS + 576 + el * 4);
    float o[8];
#pragma unroll
    for (int m = 0; m < 2; ++m)
#pragma unroll
      for (int j = 0; j < 4; ++j) o[4 * m + j] = sgn * (acc[m][n][j] * rs[4 * m + j][0] - rs[4 * m + j][1] * ws);
    u32x4 pk = {pk_bf16(o[0], o[1]), pk_bf16(o[2], o[3]), pk_bf16(o[4], o[5]), pk_bf16(o[6], o[7])};
    const int hi = el >= 72 ? 1 : 0, e72 = el - 72 * hi;
    const int etg = (et & 3) * 2 + hi;
    const unsigned inner = e72 < 64 ? (unsigned)((e72 >> 4) * 1024 + q * 256 + (e72 & 15) * 16) : (unsigned)(4096 + q * 128 + (e72 & 7) * 16);
    const unsigned off = (unsigned)((etg * 32 + kt) * 4 + ksg) * 4608u + inner;
    __builtin_amdgcn_raw_buffer_store_b128(pk, rsO, off, 0, AUX_SC1);
  }
  WAITV(0);
  STAMP(ws, 0, 3);
}

extern "C" __global__ __launch_bounds__(512, 2) void k1_hid(const bf16_t* x, const bf16_t* wre, const bf16_t* wim, const bf16_t* cre, const bf16_t* cim, char* ws) {
  __shared__ __attribute__((aligned(16))) char smem[K1_LDS];
  const int wid = __builtin_amdgcn_readfirstlane(threadIdx.x >> 6), lane = threadIdx.x & 63;
  const int b = blockIdx.x, xcd = b & 7, i = b >> 3;
  unsigned ctop = 0;
  CODE_TOUCH_TOP("k1_hid", ctop);
  asm volatile("" ::"s"(x), "s"(wre), "s"(wim), "s"(cre), "s"(cim), "s"(ws));
  const int st = xcd * 4 + (i & 3), et = i >> 2;
  const u32x4 rsX = mksrd(x, S_ * D_ * 2);
  const u32x4 rsW = mksrd(et < 4 ? wre : wim, D_ * D_ * 2);
  const u32x4 rsC = mksrd(wid < 4 ? cre : cim, S_ * S_ * 2);
  rsrc_t rsO = MKRS(ws + WS_BP, BP_BYTES);
  unsigned vhead;
  if (wid == 7) {
    const int l = lane < 32 ? lane : 31;
    vhead = (unsigned)((64 * st + 8 * et + (l >> 2)) * 1152 + (5 + (l & 3)) * 128);
  } else {
    int l = wid * 21 + (lane < 21 ? lane : 20);
    l = l < 144 ? l : 143;
    vhead = (unsigned)(((et & 3) * 144 + 36 * (i & 3) + (l >> 2)) * 1152 + (5 + (l & 3)) * 128);
  }
  const int w4 = wid & 3;
  if (wid == 0 || wid == 7) k1_body<3>(smem, rsX, rsW, rsC, rsO, st, et, wid, lane, wid == 0 ? 0 : 6, vhead, wid == 7, ws, ctop);
  else k1_body<2>(smem, rsX, rsW, rsC, rsO, st, et, wid, lane, wid < 4 ? 1 + 2 * w4 : 2 * w4, vhead, false, ws, ctop);
}

constexpr int K2_SLOT = 16384 + 18432, K2_XOFF = 4 * K2_SLOT, K2_SCR = K2_XOFF + 9216, K2_LDS = K2_SCR + 256;
constexpr int K2_RSTR = 336, K2_RKQ = 64 * K2_RSTR, K2_SOFF = 4 * K2_RKQ;

extern "C" __global__ __launch_bounds__(512, 2) void k2_seq(const bf16_t* x, const bf16_t* cre, const bf16_t* cim, const bf16_t* wg, const bf16_t* wu, char* ws) {
  __shared__ __attribute__((aligned(16))) char smem[K2_LDS];
  const int wid = __builtin_amdgcn_readfirstlane(threadIdx.x >> 6), lane = threadIdx.x & 63;
  const int b = blockIdx.x, xcd = b & 7, i = b >> 3;
  const int mt = (xcd >> 1) * 8 + (i & 7), nt = (xcd & 1) * 4 + (i >> 3);
  const u32x4 rsRe = mksrd(cre, S_ * S_ * 2), rsIm = mksrd(cim, S_ * S_ * 2);
  const u32x4 rsB = mksrd(ws + WS_BP, BP_BYTES);
  const u32x4 rsX = mksrd(x, S_ * D_ * 2);
  const int q = lane >> 4, r16 = lane & 15, r8 = lane >> 3, c8 = lane & 7;
  const int mh = wid & 1, kq = wid >> 1;

  const unsigned va = (unsigned)(64 * mt + 8 * wid + r8) * 4096u + (unsigned)((c8 ^ r8) * 16);
  const unsigned vb = (unsigned)(wid * 1024 + lane * 16);
  const unsigned vb2 = (unsigned)(16384 + wid * 256 + lane * 16);
  const unsigned mA = lds_base(smem) + wid * 1024, mP = lds_base(smem) + wid * 256;
  auto issue1 = [&](int j, u32x4 rsA, int k) __attribute__((always_inline)) {
    const unsigned so = (unsigned)((j & 3) * K2_SLOT), m0 = mA + so, m1 = mP + so;
    const int sa = (j & 15) * 256, sb = (nt * 32 + j) * 18432;
    if (k == 0) GLDS(rsA, m0, 0, va, sa);
    if (k == 1) GLDS(rsA, m0, 8192, va, sa + 128);
    if (k == 2) GLDS(rsB, m0, 16384, vb, sb);
    if (k == 3) GLDS(rsB, m0, 16384 + 8192, vb, sb + 8192);
    if (k == 4) GLDS_N(0xffff, rsB, m1, 16384 + 16384, vb2, sb);
  };
  auto issue = [&](int j, u32x4 rsA) __attribute__((always_inline)) {
    issue1(j, rsA, 0); issue1(j, rsA, 1); issue1(j, rsA, 2); issue1(j, rsA, 3); issue1(j, rsA, 4);
  };
  const unsigned at0 = (unsigned)((kq >> 1) * 8192 + (32 * mh + r16) * 128 + ((((kq & 1) * 4 + q) ^ (r16 & 7)) * 16));
  const unsigned ae0 = (unsigned)(16384 + kq * 4608 + lane * 16);
  const unsigned aeh = (unsigned)(16384 + kq * 4608 + 4096 + q * 128 + (lane & 7) * 16);

  f32x4 acc[5][2];
#pragma unroll
  for (int f = 0; f < 5; ++f) { acc[f][0] = f32x4{0, 0, 0, 0}; acc[f][1] = f32x4{0, 0, 0, 0}; }
  struct Fr { bf16x8 t0, t1, e[5]; };
  Fr fr[2];
  auto rd = [&](Fr& f, int j, int part) __attribute__((always_inline)) {
    const char* sl = smem + (j & 3) * K2_SLOT;
    if (part == 0) { f.t0 = *(const bf16x8*)(sl + at0); f.t1 = *(const bf16x8*)(sl + at0 + 2048); }
    if (part == 1) { f.e[0] = *(const bf16x8*)(sl + ae0); f.e[1] = *(const bf16x8*)(sl + ae0 + 1024); }
    if (part == 2) { f.e[2] = *(const bf16x8*)(sl + ae0 + 2048); f.e[3] = *(const bf16x8*)(sl + ae0 + 3072); }
    if (part == 3) f.e[4] = *(const bf16x8*)(sl + aeh);
  };
  auto stage = [&](int j, auto P_, u32x4 rsA, auto MODE_, auto RD_) __attribute__((always_inline)) {
    constexpr int P = decltype(P_)::value, MODE = decltype(MODE_)::value, RD = decltype(RD_)::value;
    const Fr& c = fr[P];
    Fr& nx = fr[P ^ 1];
#pragma unroll
    for (int f = 0; f < 5; ++f) {
      if (MODE == 1) issue1(j + 4, rsA, f);
      if (MODE == 2 && f == 0) {
        const int o0 = wid * 1024 + lane * 16, o1 = 8192 + wid * 128 + lane * 16;
        const int row0 = o0 / 144, row1 = o1 / 144;
        GLDS(rsX, mA, K2_XOFF, (unsigned)((64 * mt + row0) * 1152 + nt * 144 + (o0 - row0 * 144)), 0);
        GLDS_N(0xff, rsX, lds_base(smem) + wid * 128, K2_XOFF + 8192, (unsigned)((64 * mt + row1) * 1152 + nt * 144 + (o1 - row1 * 144)), 0);
      }
      if (MODE == 2 && f == 2) {
        CODE_TOUCH("k2_seq", lds_base(smem) + K2_SCR, i, wid);
        int n = wid * 14 + (lane < 13 ? lane : 13);
        n = i * 108 + (n < 107 ? n : 107);
        const int row = n / 9;
        TOUCH(mksrd(wg, D_ * F_ * 2), lds_base(smem) + K2_SCR, (unsigned)(row * 9216 + xcd * 1152 + (n - row * 9) * 128));
      }
      SCHED();
      acc[f][0] = MFMA16(c.e[f], c.t0, acc[f][0]); acc[f][1] = MFMA16(c.e[f], c.t1, acc[f][1]);
      SCHED();
      if (RD && f < 4) rd(nx, j + 1, f);
      SCHED();
    }
    WAITL0();
  };
  using I0 = IC<0>; using I1 = IC<1>; using I2 = IC<2>;

  STAMP(ws, 1, 0);
  issue(0, rsRe); issue(1, rsRe); issue(2, rsRe); issue(3, rsRe);
  WAITV(15); BAR();
  STAMP(ws, 1, 1);
  rd(fr[0], 0, 0); rd(fr[0], 0, 1); rd(fr[0], 0, 2); rd(fr[0], 0, 3);
  WAITL0();
  WAITV(10); BAR();
  for (int j = 0; j < 12; j += 2) {
    stage(j, I0{}, rsRe, I1{}, I1{}); WAITV(10); BAR();
    stage(j + 1, I1{}, rsRe, I1{}, I1{}); WAITV(10); BAR();
  }
  for (int j = 12; j < 28; j += 2) {
    stage(j, I0{}, rsIm, I1{}, I1{}); WAITV(10); BAR();
    stage(j + 1, I1{}, rsIm, I1{}, I1{}); WAITV(10); BAR();
  }
  stage(28, I0{}, rsIm, I2{}, I1{}); WAITV(9); BAR();
  stage(29, I1{}, rsIm, I0{}, I1{}); WAITV(4); BAR();
  stage(30, I0{}, rsIm, I0{}, I1{});
  stage(31, I1{}, rsIm, I0{}, I0{});
  WAITV(2); BAR();
  STAMP(ws, 1, 2);
#pragma unroll
  for (int f = 0; f < 5; ++f)
#pragma unroll
    for (int tf = 0; tf < 2; ++tf)
      *(f32x4*)(smem + kq * K2_RKQ + (32 * mh + 16 * tf + r16) * K2_RSTR + (16 * f + 4 * q) * 4) = acc[f][tf];
  WAITL0(); BAR();
  const rsrc_t rsH = MKRS(ws + WS_HT, HT_BYTES);
  auto item = [&](int U) {
    const int t = lane;
    f32x4 lo = {0, 0, 0, 0}, hi = {0, 0, 0, 0};
#pragma unroll
    for (int k = 0; k < 4; ++k) {
      const char* p = smem + k * K2_RKQ + t * K2_RSTR + U * 32;
      lo += *(const f32x4*)p; hi += *(const f32x4*)(p + 16);
    }
    const u32x4 xv = *(const u32x4*)(smem + K2_XOFF + t * 144 + U * 16);
    u32x4 hv = {pk_bf16(lo[0] + bf_lo(xv[0]), lo[1] + bf_hi(xv[0])), pk_bf16(lo[2] + bf_lo(xv[1]), lo[3] + bf_hi(xv[1])),
                pk_bf16(hi[0] + bf_lo(xv[2]), hi[1] + bf_hi(xv[2])), pk_bf16(hi[2] + bf_lo(xv[3]), hi[3] + bf_hi(xv[3]))};
    float v[8] = {bf_lo(hv[0]), bf_hi(hv[0]), bf_lo(hv[1]), bf_hi(hv[1]), bf_lo(hv[2]), bf_hi(hv[2]), bf_lo(hv[3]), bf_hi(hv[3])};
    float m = 0.f;
#pragma unroll
    for (int k = 0; k < 8; ++k) m += v[k];
    m *= 0.125f;
    float m2 = 0.f;
#pragma unroll
    for (int k = 0; k < 8; ++k) { const float d = v[k] - m; m2 += d * d; }
    *(f32x2*)(smem + K2_SOFF + (t * 9 + U) * 8) = f32x2{m, m2};
    const int Ug = nt * 9 + U, tg = 64 * mt + t;
    __builtin_amdgcn_raw_buffer_store_b128(hv, rsH, (unsigned)(((tg >> 4) * 72 + Ug) * 256 + (tg & 15) * 16), 0, AUX_SC1);
  };
  item(wid);
  if (wid == 0) item(8);
  WAITL0(); BAR();
  if (wid == 0) {
    const char* p = smem + K2_SOFF + lane * 72;
    f32x2 s[9];
    float m = 0.f;
#pragma unroll
    for (int k = 0; k < 9; ++k) { s[k] = *(const f32x2*)(p + k * 8); m += s[k][0]; }
    m *= (1.f / 9.f);
    float m2 = 0.f;
#pragma unroll
    for (int k = 0; k < 9; ++k) { const float d = s[k][0] - m; m2 += s[k][1] + 8.f * d * d; }
    const rsrc_t rsS = MKRS(ws + WS_ST, ST_BYTES);
    __builtin_amdgcn_raw_buffer_store_b64(__builtin_bit_cast(u32x2, f32x2{m, m2}), rsS, (unsigned)(((64 * mt + lane) * 8 + nt) * 8), 0, AUX_SC1);
  }
  STAMP(ws, 1, 3);
}

constexpr int KF_RING = 92160, KF_ST = KF_RING + 65536, KF_SCR = KF_ST + 640, KF_LDS = 163840;
#define EPI 8
#define PB1 1
#define ROT 0
#define WG0 10
#define WPG 1
#define PFINE 0
#define PG_N 12
#define PU_N 12
#define PB_N 10

#define MFMA_ACC(acc, a, b) asm volatile("v_mfma_f32_16x16x32_bf16 %0, %1, %2, %0" : "+v"(acc) : "v"(a), "v"(b))
#define ACC_READY(A) asm volatile("s_nop 7\n\ts_nop 7" : "+v"(A[0][0]), "+v"(A[0][1]), "+v"(A[0][2]), "+v"(A[0][3]), "+v"(A[0][4]), \
    "+v"(A[1][0]), "+v"(A[1][1]), "+v"(A[1][2]), "+v"(A[1][3]), "+v"(A[1][4]), "+v"(A[2][0]), "+v"(A[2][1]), "+v"(A[2][2]), "+v"(A[2][3]), \
    "+v"(A[2][4]), "+v"(A[3][0]), "+v"(A[3][1]), "+v"(A[3][2]), "+v"(A[3][3]), "+v"(A[3][4]))
#define DSR128(dst, addr, off) asm volatile("ds_read_b128 %0, %1 offset:%2" : "+v"(dst) : "v"(addr), "i"(off))
#define DSRTR(dst, addr, off) asm volatile("ds_read_b64_tr_b16 %0, %1 offset:%2" : "+v"(dst) : "v"(addr), "i"(off))

extern "C" __global__ __launch_bounds__(512, 2) void k3_ffn(const bf16_t* wg, const bf16_t* wu, const bf16_t* wd, char* ws, const bf16_t* out) {
  __shared__ __attribute__((aligned(16))) char smem[KF_LDS];
  const int wid = __builtin_amdgcn_readfirstlane(threadIdx.x >> 6), lane = threadIdx.x & 63;
  const int b = blockIdx.x, xcd = b & 7, i = b >> 3;
  const u32x4 rsG = mksrd(wg, D_ * F_ * 2), rsU = mksrd(wu, D_ * F_ * 2), rsD = mksrd(wd, F_ * D_ * 2);
  const unsigned lds0 = lds_base(smem);
  if (b >= 234) {
    STAMP(ws, 2, 0); STAMP(ws, 2, 1);
    const int n0 = ((b - 234) * 8 + wid) * 64;
#pragma unroll 1
    for (int r = 0; r < 9; ++r) {
      const int n = r * 11264 + n0;
      if (n < 13824) TOUCH(rsG, lds0 + wid * 256, (unsigned)((27648 + n + lane) * 128));
      else if (n < 55296) TOUCH(rsU, lds0 + wid * 256, (unsigned)((n - 13824 + lane) * 128));
      else if (n < 96768) TOUCH(rsD, lds0 + wid * 256, (unsigned)((n - 55296 + lane) * 128));
    }
    WAITV(0);
    STAMP(ws, 2, 2); STAMP(ws, 2, 3); STAMP(ws, 2, 4); STAMP(ws, 2, 5);
    return;
  }
  const int id = xcd * 29 + (xcd < 2 ? xcd : 2) + i;
  const int tt = id < 130 ? id / 5 : (id - 130) >> 2, gp = id < 130 ? id - tt * 5 : 5 + ((id - 130) & 3);
  const u32x4 rsH = mksrd(ws + WS_HT, HT_BYTES), rsS = mksrd(ws + WS_ST, ST_BYTES);
  const rsrc_t rsP = MKRS(ws + WS_P, P_BYTES);
  STAMP(ws, 2, 0);
  WST(0);
  const int q = lane >> 4, r16 = lane & 15, r8 = lane >> 3, c8 = lane & 7, a4 = r16 >> 2, p4 = r16 & 3;
  const unsigned ring = lds0 + KF_RING + wid * 8192;
  unsigned vW[4];
#pragma unroll
  for (int j = 0; j < 4; ++j)
    vW[j] = (unsigned)((r8 + 8 * j) * 9216 + gp * 1024 + wid * 128 + ((c8 ^ (r8 & 2) ^ ((j & 1) * 4)) * 16) - j * 1024);
  auto ld = [&](u32x4 rs, int soff, int j) __attribute__((always_inline)) { RING_LD(rs, j * 1024, vW[j], soff); };
  auto ld4 = [&](u32x4 rs, int slot, int soff) __attribute__((always_inline)) {
    RING_M0(ring, slot * 4096);
    asm volatile("s_nop 0");
#pragma unroll
    for (int j = 0; j < 4; ++j) ld(rs, soff, j);
  };
  {
    f32x4 sp;
    const int tok = (wid < 5 ? wid : 4) * 16 + (lane >> 2);
    {
      int tg = tt * 80 + tok;
      tg = tg < S_ ? tg : S_ - 1;
      GLD(sp, rsS, (unsigned)(tg * 64 + (lane & 3) * 16));
    }
    BAR();
    u32x4 hv[12];
    const int r0 = ROT ? (gp < 5 ? gp : gp - 5) : 0;
    auto rot = [&](int k) { return k + r0 < 5 ? k + r0 : k + r0 - 5; };
    auto tile = [&](int T) { T += tt * 5; return (unsigned)((T < 128 ? T : 127) * 18432 + lane * 16); };
#pragma unroll
    for (int k = 0; k < 5; ++k) { GLD(hv[2 * k], rsH, tile(rot(k)) + wid * 1024); GLD(hv[2 * k + 1], rsH, tile(rot(k)) + wid * 1024 + 8192); }
    GLD(hv[10], rsH, tile(wid >> 1) + (16 + (wid & 1)) * 1024);
    GLD(hv[11], rsH, tile(4) + (16 + (wid & 1)) * 1024);
    ld4(rsG, 0, 0);
    LANDED(16, sp);
    {
      float m = sp[0] + sp[2];
      m += __shfl_xor(m, 1); m += __shfl_xor(m, 2);
      m *= 0.125f;
      const float d0 = sp[0] - m, d1 = sp[2] - m;
      float m2 = sp[1] + sp[3] + 72.f * (d0 * d0 + d1 * d1);
      m2 += __shfl_xor(m2, 1); m2 += __shfl_xor(m2, 2);
      const float r = __builtin_amdgcn_rsqf(m2 * (1.f / 576.f) + LN_EPS);
      if ((lane & 3) == 0 && wid < 5) *(f32x2*)(smem + KF_ST + tok * 8) = f32x2{r, -m * r};
    }
    WAITL0(); BAR();
    WST(1);
    f32x2 st[6];
#pragma unroll
    for (int k = 0; k < 5; ++k) st[k] = *(const f32x2*)(smem + KF_ST + (rot(k) * 16 + r16) * 8);
    const f32x2 st4 = *(const f32x2*)(smem + KF_ST + (64 + r16) * 8);
    st[5] = *(const f32x2*)(smem + KF_ST + ((wid >> 1) * 16 + r16) * 8);
    auto put = [&](u32x4 v, f32x2 c, int T, int s) __attribute__((always_inline)) {
      u32x4 o;
#pragma unroll
      for (int k = 0; k < 4; ++k) o[k] = pk_bf16(__builtin_fmaf(bf_lo(v[k]), c[0], c[1]), __builtin_fmaf(bf_hi(v[k]), c[0], c[1]));
      *(u32x4*)(smem + T * 18432 + s * 1024 + lane * 16) = o;
    };
    LANDED(15, hv[0]); put(hv[0], st[0], rot(0), wid); LANDED(14, hv[1]); put(hv[1], st[0], rot(0), wid + 8);
    LANDED(13, hv[2]); put(hv[2], st[1], rot(1), wid); LANDED(12, hv[3]); put(hv[3], st[1], rot(1), wid + 8);
    LANDED(11, hv[4]); put(hv[4], st[2], rot(2), wid); LANDED(10, hv[5]); put(hv[5], st[2], rot(2), wid + 8);
    LANDED(9, hv[6]); put(hv[6], st[3], rot(3), wid); LANDED(8, hv[7]); put(hv[7], st[3], rot(3), wid + 8);
    LANDED(7, hv[8]); put(hv[8], st[4], rot(4), wid); LANDED(6, hv[9]); put(hv[9], st[4], rot(4), wid + 8);
    LANDED(5, hv[10]);
    LANDED(4, hv[11]);
    ld4(rsG, 1, 294912);
    put(hv[10], st[5], wid >> 1, 16 + (wid & 1));
    if (wid < 2) put(hv[11], st4, 4, 16 + wid);
    WAITV(4); WAITL0(); BAR();
  }
  STAMP(ws, 2, 1);
  WST(2);

  unsigned aA[4], aB[4];
#pragma unroll
  for (int c = 0; c < 4; ++c) {
    aA[c] = ring + (unsigned)((8 * q + a4) * 128 + ((c ^ ((a4 >> 1) | ((q & 1) << 1))) * 32) + p4 * 8);
  }
  unsigned at0 = lds0 + (unsigned)(lane * 16), at1 = at0 + 3 * 18432;
  s16x4 wl[2][4], wh[2][4];
  bf16x8 tk[5];
  auto fresh = [&]() __attribute__((always_inline)) {
#pragma unroll
    for (int c = 0; c < 4; ++c) asm volatile("" : "=v"(wl[0][c]), "=v"(wh[0][c]), "=v"(wl[1][c]), "=v"(wh[1][c]));
#pragma unroll
    for (int t = 0; t < 5; ++t) asm volatile("" : "=v"(tk[t]));
  };
  fresh();
  auto rdw = [&](int buf, const unsigned(&a)[4], int hi) __attribute__((always_inline)) {
#pragma unroll
    for (int c = 0; c < 4; ++c) { DSRTR(wl[buf][c], a[c], buf * 4096); DSRTR(wh[buf][c], a[c], buf * 4096 + hi); }
  };
  auto rdt = [&](int t, int str, int off) __attribute__((always_inline)) {
    if (t < 3) DSR128(tk[t], at0, t * str + off); else DSR128(tk[t], at1, (t - 3) * str + off);
  };
#define LANDED_L(n) asm volatile("s_waitcnt lgkmcnt(%0)" ::"n"(n))
#define LANDED_W(n, buf) asm volatile("s_waitcnt lgkmcnt(%12)" : "+v"(wl[buf][0]), "+v"(wl[buf][1]), "+v"(wl[buf][2]), "+v"(wl[buf][3]), \
    "+v"(wh[buf][0]), "+v"(wh[buf][1]), "+v"(wh[buf][2]), "+v"(wh[buf][3]), "+v"(tk[0]), "+v"(tk[1]), "+v"(tk[2]), "+v"(tk[3]) : "n"(n))
#define LANDED_WB(buf) asm volatile("s_waitcnt lgkmcnt(0)" : "+v"(wl[buf][0]), "+v"(wl[buf][1]), "+v"(wl[buf][2]), "+v"(wl[buf][3]), \
    "+v"(wh[buf][0]), "+v"(wh[buf][1]), "+v"(wh[buf][2]), "+v"(wh[buf][3]))
#define LANDED_T(n, t) asm volatile("s_waitcnt lgkmcnt(%1)" : "+v"(tk[t]) : "n"(n))
  auto opnd = [&](int buf, int c) __attribute__((always_inline)) -> bf16x8 {
    return __builtin_shufflevector(wl[buf][c], wh[buf][c], 0, 1, 2, 3, 4, 5, 6, 7);
  };
  auto step = [&](f32x4(&acc)[4][5], auto P_, auto LD_, auto NW_, auto NT_, int str, int toff, u32x4 rs, int soff, auto gap) __attribute__((always_inline)) {
    constexpr int P = decltype(P_)::value, NW = decltype(NW_)::value;
    constexpr bool LD = decltype(LD_)::value, NT = decltype(NT_)::value;
    constexpr int W16 = (16 - WG0) * WPG < 8 ? (16 - WG0) * WPG : 8;
    if (LD) RING_M0(ring, P * 4096);
    LANDED_L(1);
#pragma unroll
    for (int t = 0; t < 5; ++t) {
      if (t == 4) LANDED_L((NT ? 4 : 0) + (NW ? W16 : 0));
      SCHED();
#pragma unroll
      for (int c = 0; c < 4; ++c) {
        const int g = t * 4 + c;
        MFMA_ACC(acc[c][t], opnd(P, c), tk[t]);
        SCHED();
        if (LD && t < 2 && (c & 1) == 0) { ld(rs, soff, 2 * t + (c >> 1)); SCHED(); }
        if (NW && g >= WG0 && g < WG0 + 8 / WPG) {
          if (g == WG0) { if (LD) WAITV(4); else WAITV(0); }
#pragma unroll
          for (int k = (g - WG0) * WPG; k < (g - WG0 + 1) * WPG; ++k) {
            const unsigned a = NW == 1 ? aA[k >> 1] : aB[k >> 1];
            if (k & 1) DSRTR(wh[P ^ 1][k >> 1], a, (P ^ 1) * 4096 + (NW == 1 ? 512 : 2048)); else DSRTR(wl[P ^ 1][k >> 1], a, (P ^ 1) * 4096);
          }
        }
        gap(g);
      }
      if (NT) rdt(t, str, toff);
    }
  };
  auto nogap = [&](int) __attribute__((always_inline)) {};
  using I0 = IC<0>; using I1 = IC<1>; using I2 = IC<2>;

  f32x4 ag[4][5], au[4][5];
#pragma unroll
  for (int c = 0; c < 4; ++c)
#pragma unroll
    for (int t = 0; t < 5; ++t) { ag[c][t] = f32x4{0, 0, 0, 0}; au[c][t] = f32x4{0, 0, 0, 0}; }
  ACC_READY(ag);

#pragma unroll
  for (int t = 0; t < 5; ++t) { rdt(t, 18432, 0); if (t == 3) rdw(0, aA, 512); }
  auto coarse = [&](int s, int n) __attribute__((always_inline)) {
    if (PRIO) asm volatile("s_cmp_lt_u32 %0, 4\n\ts_cbranch_scc1 2f\n\ts_cmp_lt_u32 %1, %2\n\ts_cbranch_scc1 1f\n\ts_setprio 0\n\ts_branch 2f\n1:\n\ts_setprio 1\n2:" ::"s"(wid), "s"(s), "s"(n) : "scc");
  };
#pragma clang loop unroll(disable)
  for (int s = 0; s < 16; s += 2) {
    coarse(s, PG_N);
    step(ag, I0{}, I1{}, I1{}, I1{}, 18432, 1024, rsG, (s + 2) * 294912, nogap);
    step(ag, I1{}, I1{}, I1{}, I1{}, 18432, 2048, rsG, (s + 3) * 294912, nogap);
    at0 += 2048; at1 += 2048;
  }
  at0 -= 16 * 1024; at1 -= 16 * 1024;
  step(ag, I0{}, I1{}, I1{}, I1{}, 18432, 17 * 1024, rsU, 0, nogap);
  step(ag, I1{}, I1{}, I1{}, I1{}, 18432, 0, rsU, 294912, nogap);
  WST(3);
  ACC_READY(ag); ACC_READY(au);
  float sx[5] = {0.f, 0.f, 0.f, 0.f, 0.f}, gs[80];
#pragma unroll
  for (int v = 0; v < 80; ++v) gs[v] = ag[(v >> 2) & 3][v >> 4][v & 3];
  auto ustep = [&](auto S_c) __attribute__((always_inline)) {
    constexpr int S = decltype(S_c)::value, NV = S < 8 ? 5 : 4, V0 = S < 8 ? 5 * S : 40 + 4 * (S - 8), NOP = 5 * NV;
    auto gap = [&](int g) __attribute__((always_inline)) {
      const int gi = g < 8 ? ((g & 1) ? g >> 1 : -1) : g - 4;
      if (gi < 0) return;
#pragma unroll
      for (int k = 0; k < NOP; ++k) {
        if (k * 16 / NOP != gi) continue;
        const int op = k / NV, e = k - op * NV, v = V0 + e;
        if (op == 0) asm volatile("v_mul_f32 %0, 0xbfb8aa3b, %1" : "+v"(sx[e]) : "v"(gs[v]));
        if (op == 1) asm volatile("v_exp_f32 %0, %0" : "+v"(sx[e]));
        if (op == 2) asm volatile("v_add_f32 %0, 1.0, %0" : "+v"(sx[e]));
        if (op == 3) asm volatile("v_rcp_f32 %0, %0" : "+v"(sx[e]));
        if (op == 4) asm volatile("v_mul_f32 %0, %0, %1" : "+v"(gs[v]) : "v"(sx[e]));
      }
      SCHED();
    };
    if (S == 0) PRIO_HI(wid);
    if (S == PU_N) PRIO_LO(wid);
    step(au, IC<S & 1>{}, I1{}, IC<(S < 17 ? 1 : 2)>{}, IC<(S < 17 ? 1 : 0)>{}, 18432, (S + 1) * 1024, S < 16 ? rsU : rsD,
         S < 16 ? (S + 2) * 294912 : (gp * 512 + wid * 64 + (S & 1) * 32) * 1152 + 1024 - wid * 128, gap);
  };
  ustep(IC<0>{}); ustep(IC<1>{}); ustep(IC<2>{}); ustep(IC<3>{}); ustep(IC<4>{}); ustep(IC<5>{});
  ustep(IC<6>{}); ustep(IC<7>{}); ustep(IC<8>{}); ustep(IC<9>{}); ustep(IC<10>{}); ustep(IC<11>{});
  ustep(IC<12>{}); ustep(IC<13>{}); ustep(IC<14>{}); ustep(IC<15>{});
  {
    int l = lane;
    asm volatile("" : "+v"(l));
    const int q_ = l >> 4, a4_ = (l >> 2) & 3, r8_ = l >> 3;
#pragma unroll
    for (int j = 0; j < 4; ++j) vW[j] = (unsigned)((r8_ + 8 * j) * 1152 + wid * 128 + (((l & 7) ^ (r8_ & 6)) * 16) - j * 1024);
#pragma unroll
    for (int c = 0; c < 4; ++c) aB[c] = ring + (unsigned)((4 * q_ + a4_) * 128 + ((c ^ ((2 * q_ + (a4_ >> 1)) & 3)) * 32) + (l & 3) * 8);
  }
  ustep(IC<16>{}); ustep(IC<17>{});
  ACC_READY(au);
  STAMP(ws, 2, 4);

  f32x4 o8[4][5], om[4][5];
  u32x4 act[2][5];
  const f32x4 zero4 = {0, 0, 0, 0};
  LANDED_WB(0);
  ld4(rsD, 0, gp * 512 * 1152);
  WAITV(4); rdw(1, aB, 2048);
  CODE_TOUCH("k4_sum", lds0 + KF_SCR, 0, wid);
#pragma unroll
  for (int pr = 0; pr < 2; ++pr) {
    if (pr == 1) {
      LANDED_WB(1);
      ld4(rsD, 1, (gp * 512 + 32) * 1152);
    }
#pragma unroll
    for (int t = 0; t < 5; ++t) {
      const float* g0 = gs + (t * 4 + 2 * pr) * 4;
      const f32x4 lo = f32x4{g0[0], g0[1], g0[2], g0[3]} * au[2 * pr][t], hi = f32x4{g0[4], g0[5], g0[6], g0[7]} * au[2 * pr + 1][t];
      act[pr][t] = u32x4{pk_bf16(lo[0], lo[1]), pk_bf16(lo[2], lo[3]), pk_bf16(hi[0], hi[1]), pk_bf16(hi[2], hi[3])};
      const bf16x8 f = __builtin_bit_cast(bf16x8, act[pr][t]);
#pragma unroll
      for (int c = 0; c < 4; ++c) o8[c][t] = MFMA16(opnd(pr, c), f, pr == 0 ? zero4 : o8[c][t]);
      SCHED();
    }
  }
  WST(4);
  BAR();
#pragma unroll
  for (int pr = 0; pr < 2; ++pr)
#pragma unroll
    for (int t = 0; t < 5; ++t) *(u32x4*)(smem + (t * 16 + 2 * wid + pr) * 1024 + lane * 16) = act[pr][t];
#pragma unroll
  for (int c = 0; c < 4; ++c)
#pragma unroll
    for (int t = 0; t < 5; ++t) om[c][t] = f32x4{0, 0, 0, 0};
  ACC_READY(om);
  WST(5);
  WAITL0(); BAR();
  WST(6);
  STAMP(ws, 2, 5);

  at0 = lds0 + (unsigned)(lane * 16); at1 = at0 + 3 * 16384;
  fresh();
  WAITV(4);
#pragma unroll
  for (int t = 0; t < 5; ++t) { rdt(t, 16384, 0); if (t == 3) rdw(0, aB, 2048); }
#pragma clang loop unroll(disable)
  for (int s = 0; s < 14; s += 2) {
    coarse(s, PB_N);
    step(om, I0{}, I1{}, I2{}, I1{}, 16384, 1024, rsD, (gp * 512 + (s + 2) * 32) * 1152, nogap);
    step(om, I1{}, I1{}, I2{}, I1{}, 16384, 2048, rsD, (gp * 512 + (s + 3) * 32) * 1152, nogap);
    at0 += 2048; at1 += 2048;
  }
  step(om, I0{}, I0{}, I2{}, I1{}, 16384, 1024, rsD, 0, nogap);
  step(om, I1{}, I0{}, I0{}, I0{}, 16384, 0, rsD, 0, nogap);
  PRIO_LO(wid);
  ACC_READY(om);
  WST(7);
  STAMP(ws, 2, 2);

  const int uo = (q & 1) * 2 + (q >> 1);
  auto emit = [&](f32x4 lo, f32x4 hi, int t, int E) __attribute__((always_inline)) {
    const auto x = __builtin_amdgcn_permlane16_swap(pk_bf16(lo[0], lo[1]), pk_bf16(hi[0], hi[1]), false, false);
    const auto y = __builtin_amdgcn_permlane16_swap(pk_bf16(lo[2], lo[3]), pk_bf16(hi[2], hi[3]), false, false);
    const int T16 = tt * 5 + t;
    if (T16 < 128)
      __builtin_amdgcn_raw_buffer_store_b128(u32x4{x[0], y[0], x[1], y[1]}, rsP, (unsigned)(((gp * 128 + T16) * 72 + E * 4 + uo) * 256 + r16 * 16), 0, AUX_SC1);
  };
#pragma unroll
  for (int k = 0; k < EPI - 1; ++k) emit(om[(k & 1) * 2][k >> 1], om[(k & 1) * 2 + 1][k >> 1], k >> 1, 2 * wid + (k & 1));
  SCHED();
  BAR();
#pragma unroll
  for (int t = 0; t < 5; ++t) {
#pragma unroll
    for (int c = 0; c < 4; ++c) *(f32x4*)(smem + (wid * 20 + t * 4 + c) * 1024 + lane * 16) = o8[c][t];
    SCHED();
  }
  WAITL0(); BAR();
#pragma unroll
  for (int k = EPI - 1; k < 10; ++k) emit(om[(k & 1) * 2][k >> 1], om[(k & 1) * 2 + 1][k >> 1], k >> 1, 2 * wid + (k & 1));
  SCHED();
  WST(8);
  WST(9);
  auto unit = [&](int u) __attribute__((always_inline)) {
    const char* p = smem + u * 2048 + lane * 16;
    f32x4 lo = {0, 0, 0, 0}, hi = {0, 0, 0, 0};
#pragma unroll
    for (int k = 0; k < 8; ++k) { lo += *(const f32x4*)(p + k * 20480); hi += *(const f32x4*)(p + k * 20480 + 1024); }
    emit(lo, hi, u >> 1, 16 + (u & 1));
  };
  unit(wid);
  if (wid < 2) unit(8 + wid);
  WST(10);
  WAITV(0);
  WST(11);
  WST_OUT(ws, 12);
  STAMP(ws, 2, 3);
}

extern "C" __global__ __launch_bounds__(576) void k4_sum(char* ws, bf16_t* out) {
  STAMP(ws, 3, 0);
  const int wv = threadIdx.x >> 6, lane = threadIdx.x & 63;
  const int T16 = blockIdx.x >> 1, t = (blockIdx.x & 1) * 8 + (lane >> 3), U = wv * 8 + (lane & 7);
  const rsrc_t rsP = MKRS(ws + WS_P, P_BYTES), rsH = MKRS(ws + WS_HT, HT_BYTES), rsO = MKRS(out, S_ * D_ * 2);
  const unsigned off = (unsigned)((T16 * 72 + U) * 256 + t * 16);
  u32x4 p[9];
#pragma unroll
  for (int g = 0; g < 9; ++g) p[g] = __builtin_amdgcn_raw_buffer_load_b128(rsP, off, g * (int)HT_BYTES, 0);
  u32x4 hv = __builtin_amdgcn_raw_buffer_load_b128(rsH, off, 0, 0);
  STAMP(ws, 3, 1);
  float a[8];
#pragma unroll
  for (int k = 0; k < 4; ++k) { a[2 * k] = bf_lo(p[0][k]); a[2 * k + 1] = bf_hi(p[0][k]); }
#pragma unroll
  for (int g = 1; g < 9; ++g)
#pragma unroll
    for (int k = 0; k < 4; ++k) { a[2 * k] += bf_lo(p[g][k]); a[2 * k + 1] += bf_hi(p[g][k]); }
  u32x4 ov;
#pragma unroll
  for (int k = 0; k < 4; ++k) ov[k] = pk_bf16(a[2 * k] + bf_lo(hv[k]), a[2 * k + 1] + bf_hi(hv[k]));
  STAMP(ws, 3, 2);
  __builtin_amdgcn_raw_buffer_store_b128(ov, rsO, (unsigned)((T16 * 16 + t) * 1152 + U * 16), 0, K4_AUX);
  STAMP(ws, 3, 3);
  if (blockIdx.x == 0) {
    __shared__ long long red[9][4];
    const long long tnow = (long long)__builtin_amdgcn_s_memrealtime();
    const int tb = threadIdx.x;
    long long v = 0; int mode = 0;
    auto rt = [&](int k, int b, int sl) -> long long { return (long long)*(const unsigned long long*)(ws + STAMP_OFF + k * 65536 + b * 128 + sl * 16 + 8); };
    auto cy = [&](int k, int b, int sl) -> long long { return (long long)*(const unsigned long long*)(ws + STAMP_OFF + k * 65536 + b * 128 + sl * 16); };
    auto wred = [&](long long x, int op) -> long long {
      for (int o = 1; o < 64; o <<= 1) { long long y = __shfl_xor(x, o); x = op == 0 ? (y < x ? y : x) : op == 1 ? (y > x ? y : x) : x + y; }
      if ((tb & 63) == 0) red[tb >> 6][0] = x;
      __syncthreads();
      long long r = red[0][0];
      for (int w = 1; w < 4; ++w) { long long y = red[w][0]; r = op == 0 ? (y < r ? y : r) : op == 1 ? (y > r ? y : r) : r + y; }
      __syncthreads();
      return r;
    };
    const int b = tb < 256 ? tb : 255;
    const bool mainb = b < NMAIN;
    const long long BIG = 1ll << 62;
    const long long s1 = wred(rt(0, b, 0), 0), s2 = wred(rt(1, b, 0), 0), s3 = wred(mainb ? rt(2, b, 0) : BIG, 0), s4 = wred(rt(3, b, 0) > 0 && b == 0 ? rt(3, b, 0) : BIG, 0);
    const long long e1 = wred(rt(0, b, 3), 1), e2 = wred(rt(1, b, 3), 1), e3 = wred(mainb ? rt(2, b, 3) : 0, 1), ep = wred(mainb ? 0 : rt(2, b, 3), 1);
    auto avg = [&](int k, int a, int c) { return wred(mainb && tb < 256 ? rt(k, b, c) - rt(k, b, a) : 0, 2) / NMAIN; };
    long long qq[22];
    qq[0] = 0;
    qq[1] = s2 - s1;
    qq[2] = s3 - s2;
    qq[3] = e3 - s3;
    qq[4] = avg(2, 0, 1);
    qq[5] = ep - s3;
    qq[6] = avg(2, 1, 4);
    qq[7] = avg(2, 4, 5);
    qq[8] = avg(2, 5, 2);
    qq[9] = avg(2, 2, 3);
    qq[10] = wred(mainb && tb < 256 ? cy(2, b, 3) - cy(2, b, 0) : 0, 2) / NMAIN / 24;
    qq[11] = s4 - e3;
    qq[12] = e1 - s1;
    qq[13] = e2 - s2;
    qq[14] = wred(tb < 256 ? rt(1, b, 1) - rt(1, b, 0) : 0, 2) / 256;
    qq[15] = wred(tb < 256 ? rt(0, b, 1) - rt(0, b, 0) : 0, 2) / 256;
    qq[16] = wred(tb < 256 ? cy(1, b, 3) - cy(1, b, 0) : 0, 2) / 256 / 24;
    qq[17] = wred(tb < 256 ? rt(1, b, 3) - rt(1, b, 0) : 0, 2) / 256;
    qq[18] = wred(mainb && tb < 256 ? rt(2, b, 3) - rt(2, b, 0) : 0, 2) / NMAIN;
    qq[19] = tnow - s1;
    qq[20] = tnow - s4;
    qq[21] = tnow - e3;
    for (int k = 0; k < 22; ++k) asm volatile("" ::"v"(qq[k]));
    long long q = qq[QSPIN];
    q *= 4;
    q = q < 0 ? 0 : (q > 40000 ? 40000 : q);
    if (tb == 0) { while ((long long)__builtin_amdgcn_s_memrealtime() - tnow < q + 6000) __builtin_amdgcn_s_sleep(2); }
  }

}

extern "C" void kernel_launch(void* const* d_in, const int* in_sizes, int n_in, void* d_out, int out_size, void* d_ws, size_t ws_size,
                              hipStream_t stream) {
  const bf16_t* x = (const bf16_t*)d_in[0];
  const bf16_t* cre = (const bf16_t*)d_in[1];
  const bf16_t* cim = (const bf16_t*)d_in[2];
  const bf16_t* wre = (const bf16_t*)d_in[3];
  const bf16_t* wim = (const bf16_t*)d_in[4];
  const bf16_t* wg = (const bf16_t*)d_in[6];
  const bf16_t* wu = (const bf16_t*)d_in[7];
  const bf16_t* wd = (const bf16_t*)d_in[8];
  char* ws = (char*)d_ws + WS_PLACE(ws_size);
  k1_hid<<<256, 512, 0, stream>>>(x, wre, wim, cre, cim, ws);
  k2_seq<<<256, 512, 0, stream>>>(x, cre, cim, wg, wu, ws);
  k3_ffn<<<256, 512, 0, stream>>>(wg, wu, wd, ws, (const bf16_t*)d_out);
  k4_sum<<<256, 576, 0, stream>>>(ws, (bf16_t*)d_out);
}
```

```cpp
#include <hip/hip_runtime.h>
#include <stdint.h>

typedef unsigned short bf16_t;
typedef short bf16x8 __attribute__((ext_vector_type(8)));
typedef short s16x4 __attribute__((ext_vector_type(4)));
typedef float f32x4 __attribute__((ext_vector_type(4)));
typedef float f32x2 __attribute__((ext_vector_type(2)));
typedef unsigned u32x4 __attribute__((ext_vector_type(4)));
typedef unsigned u32x2 __attribute__((ext_vector_type(2)));
typedef __bf16 bf2_t __attribute__((ext_vector_type(2)));
typedef __amdgpu_buffer_rsrc_t rsrc_t;

constexpr int S_ = 2048, D_ = 576, F_ = 4608;
constexpr float LN_EPS = 1e-5f;

constexpr unsigned WS_BP = 0;
constexpr unsigned BP_BYTES = 8u * 32u * 18432u;
constexpr unsigned WS_HT = WS_BP + BP_BYTES;
constexpr unsigned HT_BYTES = 128u * 72u * 256u;
constexpr unsigned WS_ST = WS_HT + HT_BYTES;
constexpr unsigned ST_BYTES = 2048u * 8u * 8u;
constexpr unsigned WS_P = WS_ST + ST_BYTES;
constexpr unsigned P_BYTES = 9u * HT_BYTES;

constexpr size_t WS_USED = 32u << 20;
#define WS_POS 2
#define WS_PLACE(n) ((n) < WS_USED ? (size_t)0 : WS_POS == 0 ? (size_t)0 : WS_POS == 1 ? (((n) - WS_USED) / 2 & ~(size_t)0x1fffff) : (((n) - WS_USED) & ~(size_t)0x1fffff))
#define STAMP_OFF (WS_POS == 0 ? (64ll << 20) : WS_POS == 1 ? -(64ll << 20) : -(64ll << 20))
#define LDSP(p) ((__attribute__((address_space(3))) void*)(p))
#define MKRS(p, n) __builtin_amdgcn_make_buffer_rsrc((void*)(p), 0, (n), 0x00020000)
#define GLDS(rs, m0base, imm, voff, soff)                                                              \
  asm volatile("s_add_u32 m0, %0, %1\n\ts_nop 0\n\tbuffer_load_dwordx4 %2, %3, %4 offen lds"          \
               ::"s"(m0base), "i"(imm), "v"(voff), "s"(rs), "s"(soff) : "memory", "scc")
#define GLDS_N(mask, rs, m0base, imm, voff, soff)                                                      \
  asm volatile("s_add_u32 m0, %0, %1\n\ts_mov_b64 exec, " #mask "\n\tbuffer_load_dwordx4 %2, %3, %4 offen lds\n\ts_mov_b64 exec, -1" \
               ::"s"(m0base), "i"(imm), "v"(voff), "s"(rs), "s"(soff) : "memory", "scc")
#define GLDS_36(rs, m0base, imm, voff, soff)                                                           \
  asm volatile("s_add_u32 m0, %0, %1\n\ts_mov_b32 exec_hi, 0xf\n\tbuffer_load_dwordx4 %2, %3, %4 offen lds\n\ts_mov_b32 exec_hi, -1" \
               ::"s"(m0base), "i"(imm), "v"(voff), "s"(rs), "s"(soff) : "memory", "scc")
#define RING_M0(m0base, imm) asm volatile("s_add_u32 m0, %0, %1" ::"s"(m0base), "i"(imm) : "scc")
#define RING_LD(rs, off, voff, soff) asm volatile("buffer_load_dwordx4 %0, %1, %2 offen offset:%3 lds" ::"v"(voff), "s"(rs), "s"(soff), "i"(off) : "memory")
#define TOUCH(rs, mscr, voff)                                                                          \
  asm volatile("s_mov_b32 m0, %0\n\ts_nop 0\n\tbuffer_load_dword %1, %2, 0 offen lds" ::"s"(mscr), "v"(voff), "s"(rs) : "memory")
#define TOUCHS(rs, mscr, voff, soff)                                                                   \
  asm volatile("s_mov_b32 m0, %0\n\ts_nop 0\n\tbuffer_load_dword %1, %2, %3 offen lds" ::"s"(mscr), "v"(voff), "s"(rs), "s"(soff) : "memory")
__device__ __forceinline__ u32x4 mksrd(const void* p, unsigned n) {
  const unsigned long long a = (unsigned long long)p;
  return u32x4{(unsigned)a, (unsigned)(a >> 32) & 0xffffu, n, 0x00020000u};
}
#define CODE_ADDR(lo, hi, sym)                                                                         \
  asm volatile("s_getpc_b64 s[20:21]\n\ts_add_u32 %0, s20, " sym "@rel32@lo+4\n\ts_addc_u32 %1, s21, " sym "@rel32@hi+12" \
               : "=s"(lo), "=s"(hi)::"s20", "s21", "scc")
#define CODE_TOUCH(sym, mscr, i, wid)                                                                  \
  do {                                                                                                 \
    unsigned lo_, hi_, le_, he_;                                                                       \
    CODE_ADDR(lo_, hi_, sym); CODE_ADDR(le_, he_, "k4_sum");                                           \
    const unsigned n_ = le_ - lo_ + 1024u;                                                             \
    TOUCH((u32x4{lo_, hi_ & 0xffffu, n_ < 65536u ? n_ : 0u, 0x00020000u}), mscr, (unsigned)(((i) * 8 + (wid)) * 128)); \
  } while (0)
#define CODE_TOUCH_TOP(sym, dst)                                                                       \
  asm volatile("s_getpc_b64 s[40:41]\n\ts_add_u32 s36, s40, " sym "@rel32@lo+4\n\ts_addc_u32 s37, s41, " sym "@rel32@hi+12\n\t"          \
               "s_getpc_b64 s[40:41]\n\ts_add_u32 s38, s40, k4_sum@rel32@lo+4\n\ts_sub_u32 s38, s38, s36\n\ts_add_u32 s38, s38, 0x400\n\t"  \
               "s_and_b32 s37, s37, 0xffff\n\ts_mov_b32 s39, 0x20000\n\ts_cmp_lt_u32 s38, 0x10000\n\ts_cselect_b32 s38, s38, 0\n\t"            \
               "s_and_b32 s40, %1, -8\n\tv_lshrrev_b32 %0, 6, %2\n\tv_add_u32 %0, s40, %0\n\tv_lshlrev_b32 %0, 7, %0\n\t"                    \
               "buffer_load_dword %0, %0, s[36:39], 0 offen"                                           \
               : "=&v"(dst) : "s"(blockIdx.x), "v"(threadIdx.x) : "s36", "s37", "s38", "s39", "s40", "s41", "scc", "memory")
__device__ __forceinline__ unsigned lds_base(char* smem) { return (unsigned)(size_t)LDSP(smem); }
#define GLD(dst, rs, voff) asm volatile("buffer_load_dwordx4 %0, %1, %2, 0 offen" : "=v"(dst) : "v"(voff), "s"(rs) : "memory")
#define LANDED(n, x) asm volatile("s_waitcnt vmcnt(" #n ")" : "+v"(x))
#define WAITV(n) asm volatile("s_waitcnt vmcnt(%0)" ::"n"(n) : "memory")
#define WAITL0() asm volatile("s_waitcnt lgkmcnt(0)" ::: "memory")
#define PIN(x) asm volatile("" : "+v"(x))
#define BAR() __builtin_amdgcn_s_barrier()
#define SCHED() __builtin_amdgcn_sched_barrier(0)
#define MFMA16(a, b, c) __builtin_amdgcn_mfma_f32_16x16x32_bf16((a), (b), (c), 0, 0, 0)
constexpr int AUX_SC1 = 16;
#define PRIO 1
#define PA_T 2
#define PB_T 2
#define PRIO_SET(wid, p) do { if (PRIO) asm volatile("s_cmp_lt_u32 %0, 4\n\ts_cbranch_scc1 1f\n\ts_setprio " #p "\n1:" ::"s"(wid) : "scc"); } while (0)
#define PRIO_HI(wid) PRIO_SET(wid, 1)
#define PRIO_LO(wid) PRIO_SET(wid, 0)
template <int V> struct IC { static constexpr int value = V; };
#define CS_T 3
#define CT2_MODE 1
#define CT3_MODE 2
#define K2_VAR 0
#define PF_P 0
#define K1_BP 0
#define K1_MAP 1
#define K1_D 4
#define K1_KCS (9 - K1_D)
#define K1_NA 3
#define K1_CS 1
#define K1_TOP 1
#define P_AUX AUX_SC1
#define K4_AUX 0
#define TSX(var, j) do { } while (0)
#define TS_OUT(ws, kid, wid, lane, k, var) do { } while (0)
#define STAMP(ws, kid, slot) do { } while (0)

#define WST(k) do { } while (0)
#define WSTP(k) do { } while (0)
#define WST_OUT(ws, n) do { } while (0)

#define TS4(j) do { } while (0)
#define TS4_OUT(ws) do { } while (0)

#define WSTT(k) do { } while (0)
__device__ __forceinline__ unsigned pk_bf16(float lo, float hi) {
  f32x2 v = {lo, hi};
  bf2_t b = __builtin_convertvector(v, bf2_t);
  return __builtin_bit_cast(unsigned, b);
}
__device__ __forceinline__ float bf_lo(unsigned w) { return __uint_as_float(w << 16); }
__device__ __forceinline__ float bf_hi(unsigned w) { return __uint_as_float(w & 0xffff0000u); }
__device__ __forceinline__ float dot_ones(unsigned w, float acc) {
  const bf2_t ones = {(__bf16)1.0f, (__bf16)1.0f};
  return __builtin_amdgcn_fdot2_f32_bf16(__builtin_bit_cast(bf2_t, w), ones, acc, false);
}
__device__ __forceinline__ float dot_frag(bf16x8 f, float acc) {
  u32x4 u = __builtin_bit_cast(u32x4, f);
  acc = dot_ones(u[0], acc); acc = dot_ones(u[1], acc); acc = dot_ones(u[2], acc); acc = dot_ones(u[3], acc);
  return acc;
}

#define K1_DPP 1
#define K1_BASE 1
#define K1_NOWL 1
constexpr int K1_SLOT = 8192 + 18432, K1_NS = 5, K1_ST = K1_NS * K1_SLOT, K1_WS = K1_ST + 512, K1_SCR = K1_WS + 1152, K1_LDS = K1_SCR + 256;

template <int NF>
__device__ __forceinline__ void k1_body(char* smem, u32x4 rsX, u32x4 rsW, u32x4 rsC, rsrc_t rsO, int st, int et, int wid, int lane, int nf0,
                                        unsigned vhead, bool headx, char* ws, unsigned ctop) {
  STAMP(ws, 0, 0);
  const int tid = wid * 64 + lane;
  const int sh = wid >> 2, q = lane >> 4, r16 = lane & 15, r8 = lane >> 3, c8 = lane & 7;
  const unsigned vx = (unsigned)(64 * st + 8 * wid + r8) * 1152u + (unsigned)((c8 ^ ((r8 & 3) | ((wid & 1) << 2))) * 16);
  const unsigned vw = (unsigned)((et & 3) * 144 + 8 * wid + r8) * 1152u + (unsigned)((c8 ^ r8) * 16);
  const int r2 = 128 + 2 * wid + r8;
  const unsigned vw2 = (unsigned)((et & 3) * 144 + r2) * 1152u + (unsigned)((c8 ^ (r2 & 7)) * 16);
  const unsigned lds0 = lds_base(smem);
  const unsigned mA = lds0 + wid * 1024, mP = lds0 + wid * 256;
  auto issue1 = [&](int kb, int k) __attribute__((always_inline)) {
    const int so = (kb % K1_NS) * K1_SLOT;
    if (k == 0) GLDS(rsX, mA, so, vx, kb * 128);
    if (k == 1) GLDS(rsW, mA, so + 8192, vw, kb * 128);
    if (k == 2) GLDS(rsW, mA, so + 16384, vw, kb * 128 + 64 * 1152);
    if (k == 3) GLDS_N(0xffff, rsW, mP, so + 24576, vw2, kb * 128);
  };
  auto issue = [&](int kb) __attribute__((always_inline)) { issue1(kb, 0); issue1(kb, 1); issue1(kb, 2); issue1(kb, 3); };
  unsigned ax[2];
#pragma unroll
  for (int m = 0; m < 2; ++m) {
    const int row = 32 * sh + 8 * (r16 >> 2) + 4 * m + (r16 & 3);
    const int key = (row & 3) | ((row >> 1) & 4);
    ax[m] = (unsigned)(row * 128 + ((q ^ key) * 16)) ^ (unsigned)(sh * 64);
  }
  const unsigned aw = (unsigned)(8192 + (nf0 * 16 + r16) * 128 + ((q ^ (r16 & 7)) * 16)) ^ (unsigned)(sh * 64);

  f32x4 acc[2][NF];
  float wsum[NF];
#pragma unroll
  for (int n = 0; n < NF; ++n) { wsum[n] = 0.f; acc[0][n] = f32x4{0, 0, 0, 0}; acc[1][n] = f32x4{0, 0, 0, 0}; }
  float shift = 0.f;
  f32x2 s1 = {0.f, 0.f}, s2 = {0.f, 0.f};

  struct Fr { u32x4 v; bf16x8 a[2][2], b[2][NF]; };
  Fr fr[2];
  unsigned fa[3][7];
  typedef const __attribute__((address_space(3))) bf16x8* lfrag_t;
  typedef const __attribute__((address_space(3))) u32x4* lvec_t;
  auto rd = [&](Fr& f, int kb, int part) __attribute__((always_inline)) {
    const int sl = kb % K1_NS, g = sl >> 1;
    const unsigned o = (sl & 1) * K1_SLOT;
    if (part == 0) {
      f.a[0][0] = *(lfrag_t)(size_t)(fa[g][0] + o); f.a[0][1] = *(lfrag_t)(size_t)(fa[g][1] + o);
#pragma unroll
      for (int n = 0; n < NF; ++n) f.b[0][n] = *(lfrag_t)(size_t)(fa[g][2] + o + n * 2048);
    } else {
      f.v = *(lvec_t)(size_t)(fa[g][6] + o);
      f.a[1][0] = *(lfrag_t)(size_t)(fa[g][3] + o); f.a[1][1] = *(lfrag_t)(size_t)(fa[g][4] + o);
#pragma unroll
      for (int n = 0; n < NF; ++n) f.b[1][n] = *(lfrag_t)(size_t)(fa[g][5] + o + n * 2048);
    }
  };
  issue(0);
  STAMP(ws, 0, 7);
  issue(1);
  TOUCH(headx ? rsX : rsW, lds0 + K1_SCR, vhead);
#pragma unroll
  for (int k = 2; k < K1_NA; ++k) issue(k);
  SCHED();
#pragma unroll
  for (int g = 0; g < 3; ++g) {
    const unsigned o = lds0 + g * 2 * K1_SLOT;
    fa[g][0] = o + ax[0]; fa[g][1] = o + ax[1]; fa[g][2] = o + aw;
    fa[g][3] = o + (ax[0] ^ 64); fa[g][4] = o + (ax[1] ^ 64); fa[g][5] = o + (aw ^ 64); fa[g][6] = o + tid * 16;
#pragma unroll
    for (int j = 0; j < 7; ++j) PIN(fa[g][j]);
  }
  SCHED();
  WAITV((K1_NA - 1) * 4 + 1); BAR();
  asm volatile("" ::"v"(ctop));
  STAMP(ws, 0, 1);
  rd(fr[0], 0, 0); rd(fr[0], 0, 1);
#pragma unroll
  for (int k = K1_NA; k < K1_D; ++k) issue(k);
  WAITL0();
  WAITV((K1_D - 2) * 4 + 1); BAR();
#pragma unroll
  for (int kb = 0; kb < 9; ++kb) {
    const Fr& c = fr[kb & 1];
    Fr& nx = fr[(kb & 1) ^ 1];
    const bool stg = kb + K1_D < 9;
    if (stg) issue1(kb + K1_D, 0);
    if (K1_CS && kb == K1_KCS)
      TOUCH(rsC, lds0 + K1_SCR, (unsigned)(blockIdx.x * 32768 + (wid & 3) * 8192 + lane * 128));
    SCHED();
#pragma unroll
    for (int n = 0; n < NF; ++n) { acc[0][n] = MFMA16(c.a[0][0], c.b[0][n], acc[0][n]); acc[1][n] = MFMA16(c.a[0][1], c.b[0][n], acc[1][n]); }
    SCHED();
    if (kb < 8) rd(nx, kb + 1, 0);
    SCHED();
    if (stg) issue1(kb + K1_D, 1);
    SCHED();
    if (kb == 0) shift = bf_lo(c.v[0]);
#pragma unroll
    for (int i = 0; i < 4; ++i) {
      f32x2 d = {bf_lo(c.v[i]) - shift, bf_hi(c.v[i]) - shift};
      s1 += d; s2 += d * d;
    }
    PIN(s1); PIN(s2);
    SCHED();
    if (stg) issue1(kb + K1_D, 2);
    SCHED();
#pragma unroll
    for (int n = 0; n < NF; ++n) { acc[0][n] = MFMA16(c.a[1][0], c.b[1][n], acc[0][n]); acc[1][n] = MFMA16(c.a[1][1], c.b[1][n], acc[1][n]); }
    SCHED();
    if (kb < 8) rd(nx, kb + 1, 1);
    SCHED();
#pragma unroll
    for (int n = 0; n < NF; ++n) wsum[n] = dot_frag(c.b[0][n], wsum[n]);
#pragma unroll
    for (int n = 0; n < NF; ++n) PIN(wsum[n]);
    SCHED();
    if (stg) issue1(kb + K1_D, 3);
    {
      const int last = kb + K1_D < 8 ? kb + K1_D : 8, n = (last - kb - 2) * 4 + (K1_CS && kb >= K1_KCS && kb + 2 - K1_D < K1_KCS ? 1 : 0);
      if (kb <= 6) switch (n) {
        case 0: WAITV(0); break; case 1: WAITV(1); break; case 4: WAITV(4); break; case 5: WAITV(5); break; case 8: WAITV(8); break;
        case 9: WAITV(9); break; case 12: WAITV(12); break; default: WAITV(0); break;
      }
    }
    if (kb < 7) BAR();
    SCHED();
  }
  STAMP(ws, 0, 2);
  {
    const float a1 = s1[0] + s1[1], a2 = s2[0] + s2[1];
    float mean = shift + a1 * (1.f / 72.f);
    float m2 = a2 - a1 * a1 * (1.f / 72.f);
    float n = 72.f;
#pragma unroll
    for (int o = 1; o < 8; o <<= 1) {
      auto other = [&](float v) {
        const int b = __builtin_bit_cast(int, v);
        return __builtin_bit_cast(float, o == 1 ? __builtin_amdgcn_mov_dpp(b, 0xb1, 0xf, 0xf, true)
                                         : o == 2 ? __builtin_amdgcn_mov_dpp(b, 0x4e, 0xf, 0xf, true) : __builtin_amdgcn_mov_dpp(b, 0x141, 0xf, 0xf, true));
      };
      const float mo = other(mean), qo = other(m2);
      const float dl = mo - mean;
      m2 = m2 + qo + dl * dl * (n * 0.5f);
      mean = 0.5f * (mean + mo);
      n *= 2.f;
    }
    const float r = __builtin_amdgcn_rsqf(m2 * (1.f / 576.f) + LN_EPS);
    if (c8 == 0) *(f32x2*)(smem + K1_ST + (tid >> 3) * 8) = f32x2{r, mean * r};
#pragma unroll
    for (int n = 0; n < NF; ++n) {
      float w = wsum[n];
      {
        const auto x = __builtin_amdgcn_permlane16_swap(__float_as_uint(w), __float_as_uint(w), false, false);
        const unsigned h = __float_as_uint(__uint_as_float(x[0]) + __uint_as_float(x[1]));
        const auto y = __builtin_amdgcn_permlane32_swap(h, h, false, false);
        w = __uint_as_float(y[0]) + __uint_as_float(y[1]);
      }
      if (q == 0) *(float*)(smem + K1_WS + sh * 576 + ((nf0 + n) * 16 + r16) * 4) = w;
    }
  }
  WAITL0();
  BAR();
  f32x2 rs[8];
  {
    const char* p = smem + K1_ST + (32 * sh + 8 * q) * 8;
#pragma unroll
    for (int i = 0; i < 4; ++i) {
      f32x4 v = *(const f32x4*)(p + i * 16);
      rs[2 * i] = f32x2{v[0], v[1]}; rs[2 * i + 1] = f32x2{v[2], v[3]};
    }
  }
  const float sgn = et < 4 ? 1.f : -1.f;
  const int kt = (et < 4 ? 0 : 16) + (st >> 1), ksg = (st & 1) * 2 + sh;
#pragma unroll
  for (int n = 0; n < NF; ++n) {
    const int el = (nf0 + n) * 16 + r16;
    const float ws = *(const float*)(smem + K1_WS + el * 4) + *(const float*)(smem + K1_WS + 576 + el * 4);
    float o[8];
#pragma unroll
    for (int m = 0; m < 2; ++m)
#pragma unroll
      for (int j = 0; j < 4; ++j) o[4 * m + j] = sgn * (acc[m][n][j] * rs[4 * m + j][0] - rs[4 * m + j][1] * ws);
    u32x4 pk = {pk_bf16(o[0], o[1]), pk_bf16(o[2], o[3]), pk_bf16(o[4], o[5]), pk_bf16(o[6], o[7])};
    const int hi = el >= 72 ? 1 : 0, e72 = el - 72 * hi;
    const int etg = (et & 3) * 2 + hi;
    const unsigned inner = e72 < 64 ? (unsigned)((e72 >> 4) * 1024 + q * 256 + (e72 & 15) * 16) : (unsigned)(4096 + q * 128 + (e72 & 7) * 16);
    const unsigned off = (unsigned)((etg * 32 + kt) * 4 + ksg) * 4608u + inner;
    __builtin_amdgcn_raw_buffer_store_b128(pk, rsO, off, 0, AUX_SC1);
  }
  WAITV(0);
  STAMP(ws, 0, 3);
}

extern "C" __global__ __launch_bounds__(512, 2) void k1_hid(const bf16_t* x, const bf16_t* wre, const bf16_t* wim, const bf16_t* cre, const bf16_t* cim, char* ws) {
  __shared__ __attribute__((aligned(16))) char smem[K1_LDS];
  const int wid = __builtin_amdgcn_readfirstlane(threadIdx.x >> 6), lane = threadIdx.x & 63;
  const int b = blockIdx.x, xcd = b & 7, i = b >> 3;
  unsigned ctop = 0;
  CODE_TOUCH_TOP("k1_hid", ctop);
  asm volatile("" ::"s"(x), "s"(wre), "s"(wim), "s"(cre), "s"(cim), "s"(ws));
  const int st = (xcd >> 1) * 8 + (i & 7), et = (xcd & 1) * 4 + (i >> 3);
  const u32x4 rsX = mksrd(x, S_ * D_ * 2);
  const u32x4 rsW = mksrd(et < 4 ? wre : wim, D_ * D_ * 2);
  const u32x4 rsC = mksrd(wid < 4 ? cre : cim, S_ * S_ * 2);
  rsrc_t rsO = MKRS(ws + WS_BP, BP_BYTES);
  unsigned vhead;
  if (wid == 7) {
    vhead = (unsigned)((64 * st + 16 * (i >> 3) + (lane >> 2)) * 1152 + (5 + (lane & 3)) * 128);
  } else {
    int l = wid * 11 + (lane < 11 ? lane : 10);
    l = l < 72 ? l : 71;
    vhead = (unsigned)(((et & 3) * 144 + 18 * (i & 7) + (l >> 2)) * 1152 + (5 + (l & 3)) * 128);
  }
  const int w4 = wid & 3;
  if (wid == 0 || wid == 7) k1_body<3>(smem, rsX, rsW, rsC, rsO, st, et, wid, lane, wid == 0 ? 0 : 6, vhead, wid == 7, ws, ctop);
  else k1_body<2>(smem, rsX, rsW, rsC, rsO, st, et, wid, lane, wid < 4 ? 1 + 2 * w4 : 2 * w4, vhead, false, ws, ctop);
}

#define K2_RDE 3
constexpr int K2_SLOT = 16384 + 18432, K2_XOFF = 4 * K2_SLOT, K2_SCR = K2_XOFF + 9216, K2_LDS = K2_SCR + 256;
constexpr int K2_RSTR = 336, K2_RKQ = 64 * K2_RSTR, K2_SOFF = 4 * K2_RKQ;

extern "C" __global__ __launch_bounds__(512, 2) void k2_seq(const bf16_t* x, const bf16_t* cre, const bf16_t* cim, const bf16_t* wg, const bf16_t* wu, char* ws) {
  __shared__ __attribute__((aligned(16))) char smem[K2_LDS];
  const int wid = __builtin_amdgcn_readfirstlane(threadIdx.x >> 6), lane = threadIdx.x & 63;
  const int b = blockIdx.x, xcd = b & 7, i = b >> 3;
  const int mt = (xcd >> 1) * 8 + (i & 7), nt = (xcd & 1) * 4 + (i >> 3);
  const u32x4 rsRe = mksrd(cre, S_ * S_ * 2), rsIm = mksrd(cim, S_ * S_ * 2);
  const u32x4 rsB = mksrd(ws + WS_BP, BP_BYTES);
  const u32x4 rsX = mksrd(x, S_ * D_ * 2);
  const int q = lane >> 4, r16 = lane & 15, r8 = lane >> 3, c8 = lane & 7;
  const int mh = wid & 1, kq = wid >> 1;

  const unsigned va = (unsigned)(64 * mt + 8 * wid + r8) * 4096u + (unsigned)((c8 ^ r8) * 16);
  const unsigned vb = (unsigned)(wid * 1024 + lane * 16);
  const unsigned vb2 = (unsigned)(16384 + wid * 256 + lane * 16);
  const unsigned mA = lds_base(smem) + wid * 1024, mP = lds_base(smem) + wid * 256;
  auto issue1 = [&](int j, u32x4 rsA, int k) __attribute__((always_inline)) {
    const unsigned so = (unsigned)((j & 3) * K2_SLOT), m0 = mA + so, m1 = mP + so;
    const int sa = (j & 15) * 256, sb = (nt * 32 + j) * 18432;
    if (k == 0) GLDS(rsA, m0, 0, va, sa);
    if (k == 1) GLDS(rsA, m0, 8192, va, sa + 128);
    if (k == 2) GLDS(rsB, m0, 16384, vb, sb);
    if (k == 3) GLDS(rsB, m0, 16384 + 8192, vb, sb + 8192);
    if (k == 4) GLDS_N(0xffff, rsB, m1, 16384 + 16384, vb2, sb);
  };
  auto issue = [&](int j, u32x4 rsA) __attribute__((always_inline)) {
    issue1(j, rsA, 0); issue1(j, rsA, 1); issue1(j, rsA, 2); issue1(j, rsA, 3); issue1(j, rsA, 4);
  };
  const unsigned at0 = (unsigned)((kq >> 1) * 8192 + (32 * mh + r16) * 128 + ((((kq & 1) * 4 + q) ^ (r16 & 7)) * 16));
  const unsigned ae0 = (unsigned)(16384 + kq * 4608 + lane * 16);
  const unsigned aeh = (unsigned)(16384 + kq * 4608 + 4096 + q * 128 + (lane & 7) * 16);

  f32x4 acc[5][2];
#pragma unroll
  for (int f = 0; f < 5; ++f) { acc[f][0] = f32x4{0, 0, 0, 0}; acc[f][1] = f32x4{0, 0, 0, 0}; }
  struct Fr { bf16x8 t0, t1, e[5]; };
  Fr fr[2];
  auto rd = [&](Fr& f, int j, int part) __attribute__((always_inline)) {
    const char* sl = smem + (j & 3) * K2_SLOT;
    if (part == 0) { f.t0 = *(const bf16x8*)(sl + at0); f.t1 = *(const bf16x8*)(sl + at0 + 2048); }
    if (part == 1) { f.e[0] = *(const bf16x8*)(sl + ae0); f.e[1] = *(const bf16x8*)(sl + ae0 + 1024); }
    if (part == 2) { f.e[2] = *(const bf16x8*)(sl + ae0 + 2048); f.e[3] = *(const bf16x8*)(sl + ae0 + 3072); }
    if (part == 3) f.e[4] = *(const bf16x8*)(sl + aeh);
  };
  auto stage = [&](int j, auto P_, u32x4 rsA, auto MODE_, auto RD_) __attribute__((always_inline)) {
    constexpr int P = decltype(P_)::value, MODE = decltype(MODE_)::value, RD = decltype(RD_)::value;
    const Fr& c = fr[P];
    Fr& nx = fr[P ^ 1];
#pragma unroll
    for (int f = 0; f < 5; ++f) {
      if (MODE == 1) issue1(j + 4, rsA, f);
      if (MODE == 2 && f == 0) {
        const int o0 = wid * 1024 + lane * 16, o1 = 8192 + wid * 128 + lane * 16;
        const int row0 = o0 / 144, row1 = o1 / 144;
        GLDS(rsX, mA, K2_XOFF, (unsigned)((64 * mt + row0) * 1152 + nt * 144 + (o0 - row0 * 144)), 0);
        GLDS_N(0xff, rsX, lds_base(smem) + wid * 128, K2_XOFF + 8192, (unsigned)((64 * mt + row1) * 1152 + nt * 144 + (o1 - row1 * 144)), 0);
      }
      if (MODE == 2 && f == 2) {
        CODE_TOUCH("k2_seq", lds_base(smem) + K2_SCR, i, wid);
        int n = wid * 14 + (lane < 13 ? lane : 13);
        n = i * 108 + (n < 107 ? n : 107);
        const int row = n / 9;
        TOUCH(mksrd(wg, D_ * F_ * 2), lds_base(smem) + K2_SCR, (unsigned)(row * 9216 + xcd * 1152 + (n - row * 9) * 128));
      }
      SCHED();
      acc[f][0] = MFMA16(c.e[f], c.t0, acc[f][0]); acc[f][1] = MFMA16(c.e[f], c.t1, acc[f][1]);
      SCHED();
      if (RD && K2_RDE == 0 && f < 4) rd(nx, j + 1, f);
      if (RD && K2_RDE == 1 && f < 2) { rd(nx, j + 1, 2 * f); rd(nx, j + 1, 2 * f + 1); }
      if (RD && K2_RDE == 2 && f < 3) { rd(nx, j + 1, f); if (f == 2) rd(nx, j + 1, 3); }
      if (RD && K2_RDE == 3 && f == 0) { rd(nx, j + 1, 0); rd(nx, j + 1, 1); rd(nx, j + 1, 2); rd(nx, j + 1, 3); }
      SCHED();
    }
    WAITL0();
  };
  using I0 = IC<0>; using I1 = IC<1>; using I2 = IC<2>;

  STAMP(ws, 1, 0);
  issue(0, rsRe); issue(1, rsRe); issue(2, rsRe); issue(3, rsRe);
  WAITV(15); BAR();
  STAMP(ws, 1, 1);
  rd(fr[0], 0, 0); rd(fr[0], 0, 1); rd(fr[0], 0, 2); rd(fr[0], 0, 3);
  WAITL0();
  WAITV(10); BAR();
  for (int j = 0; j < 12; j += 2) {
    stage(j, I0{}, rsRe, I1{}, I1{}); WAITV(10); BAR();
    stage(j + 1, I1{}, rsRe, I1{}, I1{}); WAITV(10); BAR();
  }
  for (int j = 12; j < 28; j += 2) {
    stage(j, I0{}, rsIm, I1{}, I1{}); WAITV(10); BAR();
    stage(j + 1, I1{}, rsIm, I1{}, I1{}); WAITV(10); BAR();
  }
  stage(28, I0{}, rsIm, I2{}, I1{}); WAITV(9); BAR();
  stage(29, I1{}, rsIm, I0{}, I1{}); WAITV(4); BAR();
  stage(30, I0{}, rsIm, I0{}, I1{});
  stage(31, I1{}, rsIm, I0{}, I0{});
  WAITV(2); BAR();
  STAMP(ws, 1, 2);
#pragma unroll
  for (int f = 0; f < 5; ++f)
#pragma unroll
    for (int tf = 0; tf < 2; ++tf)
      *(f32x4*)(smem + kq * K2_RKQ + (32 * mh + 16 * tf + r16) * K2_RSTR + (16 * f + 4 * q) * 4) = acc[f][tf];
  WAITL0(); BAR();
  const rsrc_t rsH = MKRS(ws + WS_HT, HT_BYTES);
  auto item = [&](int U) {
    const int t = lane;
    f32x4 lo = {0, 0, 0, 0}, hi = {0, 0, 0, 0};
#pragma unroll
    for (int k = 0; k < 4; ++k) {
      const char* p = smem + k * K2_RKQ + t * K2_RSTR + U * 32;
      lo += *(const f32x4*)p; hi += *(const f32x4*)(p + 16);
    }
    const u32x4 xv = *(const u32x4*)(smem + K2_XOFF + t * 144 + U * 16);
    u32x4 hv = {pk_bf16(lo[0] + bf_lo(xv[0]), lo[1] + bf_hi(xv[0])), pk_bf16(lo[2] + bf_lo(xv[1]), lo[3] + bf_hi(xv[1])),
                pk_bf16(hi[0] + bf_lo(xv[2]), hi[1] + bf_hi(xv[2])), pk_bf16(hi[2] + bf_lo(xv[3]), hi[3] + bf_hi(xv[3]))};
    float v[8] = {bf_lo(hv[0]), bf_hi(hv[0]), bf_lo(hv[1]), bf_hi(hv[1]), bf_lo(hv[2]), bf_hi(hv[2]), bf_lo(hv[3]), bf_hi(hv[3])};
    float m = 0.f;
#pragma unroll
    for (int k = 0; k < 8; ++k) m += v[k];
    m *= 0.125f;
    float m2 = 0.f;
#pragma unroll
    for (int k = 0; k < 8; ++k) { const float d = v[k] - m; m2 += d * d; }
    *(f32x2*)(smem + K2_SOFF + (t * 9 + U) * 8) = f32x2{m, m2};
    const int Ug = nt * 9 + U, tg = 64 * mt + t;
    __builtin_amdgcn_raw_buffer_store_b128(hv, rsH, (unsigned)(((tg >> 4) * 72 + Ug) * 256 + (tg & 15) * 16), 0, AUX_SC1);
  };
  item(wid);
  if (wid == 0) item(8);
  WAITL0(); BAR();
  if (wid == 0) {
    const char* p = smem + K2_SOFF + lane * 72;
    f32x2 s[9];
    float m = 0.f;
#pragma unroll
    for (int k = 0; k < 9; ++k) { s[k] = *(const f32x2*)(p + k * 8); m += s[k][0]; }
    m *= (1.f / 9.f);
    float m2 = 0.f;
#pragma unroll
    for (int k = 0; k < 9; ++k) { const float d = s[k][0] - m; m2 += s[k][1] + 8.f * d * d; }
    const rsrc_t rsS = MKRS(ws + WS_ST, ST_BYTES);
    __builtin_amdgcn_raw_buffer_store_b64(__builtin_bit_cast(u32x2, f32x2{m, m2}), rsS, (unsigned)(((64 * mt + lane) * 8 + nt) * 8), 0, AUX_SC1);
  }
  STAMP(ws, 1, 3);
}

constexpr int KF_RING = 92160, KF_ST = KF_RING + 65536, KF_SCR = KF_ST + 640, KF_LDS = 163840;
#define EPI 8
#define PB1 1
#define ROT 0
#define UW 1
#define CZERO 1
#define SILU4 1
#define TEARLY 1
#define LSTR 2
#define LOFF 0
#define TBAR 1
#define TPIPE 1
#define TPK 0
#define TW1 3
#define TW0 7
#define TPRI 2
#define PPRO 3
#define NPRE 5
#define LPR 1
#define PUT_ASM 0
#define PFIRST 0
#define PR_PRIO 0
#define PW0 0
#define PW1 5
#define WG0 10
#define WPG 1
#define PFINE 0
#define PG_N 12
#define PU_N 12
#define PB_N 10

#define MFMA_ACC(acc, a, b) asm volatile("v_mfma_f32_16x16x32_bf16 %0, %1, %2, %0" : "+v"(acc) : "v"(a), "v"(b))
#define MFMA_NEW(acc, a, b) asm volatile("v_mfma_f32_16x16x32_bf16 %0, %1, %2, 0" : "=&v"(acc) : "v"(a), "v"(b))
#define ACC_READY(A) asm volatile("s_nop 7\n\ts_nop 7" : "+v"(A[0][0]), "+v"(A[0][1]), "+v"(A[0][2]), "+v"(A[0][3]), "+v"(A[0][4]), \
    "+v"(A[1][0]), "+v"(A[1][1]), "+v"(A[1][2]), "+v"(A[1][3]), "+v"(A[1][4]), "+v"(A[2][0]), "+v"(A[2][1]), "+v"(A[2][2]), "+v"(A[2][3]), \
    "+v"(A[2][4]), "+v"(A[3][0]), "+v"(A[3][1]), "+v"(A[3][2]), "+v"(A[3][3]), "+v"(A[3][4]))
#define DSR128(dst, addr, off) asm volatile("ds_read_b128 %0, %1 offset:%2" : "+v"(dst) : "v"(addr), "i"(off))
#define DSRTR(dst, addr, off) asm volatile("ds_read_b64_tr_b16 %0, %1 offset:%2" : "+v"(dst) : "v"(addr), "i"(off))

extern "C" __global__ __launch_bounds__(512, 2) void k3_ffn(const bf16_t* wg, const bf16_t* wu, const bf16_t* wd, char* ws, const bf16_t* out) {
  __shared__ __attribute__((aligned(16))) char smem[KF_LDS];
  const int wid = __builtin_amdgcn_readfirstlane(threadIdx.x >> 6), lane = threadIdx.x & 63;
  const int b = blockIdx.x, xcd = b & 7, i = b >> 3;
  const u32x4 rsG = mksrd(wg, D_ * F_ * 2), rsU = mksrd(wu, D_ * F_ * 2), rsD = mksrd(wd, F_ * D_ * 2);
  const unsigned lds0 = lds_base(smem);
  if (b >= 234) {
    STAMP(ws, 2, 0); STAMP(ws, 2, 1);
    const int n0 = ((b - 234) * 8 + wid) * 64;
#pragma unroll 1
    for (int r = 0; r < 9; ++r) {
      const int n = r * 11264 + n0;
      if (n < 13824) TOUCH(rsG, lds0 + wid * 256, (unsigned)((27648 + n + lane) * 128));
      else if (n < 55296) TOUCH(rsU, lds0 + wid * 256, (unsigned)((n - 13824 + lane) * 128));
      else if (n < 96768) TOUCH(rsD, lds0 + wid * 256, (unsigned)((n - 55296 + lane) * 128));
    }
    WAITV(0);
    STAMP(ws, 2, 2); STAMP(ws, 2, 3); STAMP(ws, 2, 4); STAMP(ws, 2, 5);
    return;
  }
  const int id = xcd * 29 + (xcd < 2 ? xcd : 2) + i;
  const int tt = id < 130 ? id / 5 : (id - 130) >> 2, gp = id < 130 ? id - tt * 5 : 5 + ((id - 130) & 3);
  const u32x4 rsH = mksrd(ws + WS_HT, HT_BYTES), rsS = mksrd(ws + WS_ST, ST_BYTES);
  const rsrc_t rsP = MKRS(ws + WS_P, P_BYTES);
  STAMP(ws, 2, 0);
  WST(0);
  const int q = lane >> 4, r16 = lane & 15, r8 = lane >> 3, c8 = lane & 7, a4 = r16 >> 2, p4 = r16 & 3;
  const unsigned ring = lds0 + KF_RING + wid * 8192;
  unsigned vW[4];
#pragma unroll
  for (int j = 0; j < 4; ++j)
    vW[j] = (unsigned)((r8 + 8 * j) * 9216 + gp * 1024 + wid * 128 + ((c8 ^ (r8 & 2) ^ ((j & 1) * 4)) * 16) - j * 1024);
  auto ld = [&](u32x4 rs, int soff, int j) __attribute__((always_inline)) { RING_LD(rs, j * 1024, vW[j], soff); };
  auto ld4 = [&](u32x4 rs, int slot, int soff) __attribute__((always_inline)) {
    RING_M0(ring, slot * 4096);
    asm volatile("s_nop 0");
#pragma unroll
    for (int j = 0; j < 4; ++j) ld(rs, soff, j);
  };
  {
    f32x4 sp;
    const int tok = (wid < 5 ? wid : 4) * 16 + (lane >> 2);
    {
      int tg = tt * 80 + tok;
      tg = tg < S_ ? tg : S_ - 1;
      GLD(sp, rsS, (unsigned)(tg * 64 + (lane & 3) * 16));
    }
    constexpr int NF = 11;
    u32x4 hv[12];
    auto tile = [&](int T) { T += tt * 5; return (unsigned)((T < 128 ? T : 127) * 18432 + lane * 16); };
    auto ldh = [&](int k) __attribute__((always_inline)) {
      if (k < 10) GLD(hv[k], rsH, tile(k % 5) + wid * 1024 + (k / 5) * 8192);
      if (k == 10) GLD(hv[10], rsH, tile(wid >> 1) + (16 + (wid & 1)) * 1024);
      if (k == 11) GLD(hv[11], rsH, tile(4) + (16 + wid) * 1024);
    };
    asm volatile("" : "=v"(hv[11]));
    if (wid < 2) ldh(11);
    if (PR_PRIO == 1) PRIO_HI(wid);
    WSTP(3);
    BAR();
    WSTP(4);
#pragma unroll
    for (int k = 0; k < NPRE; ++k) ldh(k);
    WSTP(5);
    asm volatile("s_waitcnt vmcnt(%2)" : "+v"(sp), "+v"(hv[11]) : "n"(NPRE));
    WSTP(6);
    {
      auto qx1 = [](float v) { return __builtin_bit_cast(float, __builtin_amdgcn_mov_dpp(__builtin_bit_cast(int, v), 0xb1, 0xf, 0xf, true)); };
      auto qx2 = [](float v) { return __builtin_bit_cast(float, __builtin_amdgcn_mov_dpp(__builtin_bit_cast(int, v), 0x4e, 0xf, 0xf, true)); };
      float m = sp[0] + sp[2];
      m += qx1(m); m += qx2(m);
      m *= 0.125f;
      const float d0 = sp[0] - m, d1 = sp[2] - m;
      float m2 = sp[1] + sp[3] + 72.f * (d0 * d0 + d1 * d1);
      m2 += qx1(m2); m2 += qx2(m2);
      const float r = __builtin_amdgcn_rsqf(m2 * (1.f / 576.f) + LN_EPS);
      const float rs = SILU4 ? r * -1.4426950408889634f : r;
      if ((lane & 3) == 0 && wid < 5) *(f32x2*)(smem + KF_ST + tok * 8) = f32x2{rs, -m * rs};
    }
    WAITL0(); BAR();
    WST(1);
    if (PR_PRIO == 2) PRIO_HI(wid);
    f32x2 st[6];
#pragma unroll
    for (int k = 0; k < 5; ++k) st[k] = *(const f32x2*)(smem + KF_ST + (k * 16 + r16) * 8);
    st[5] = *(const f32x2*)(smem + KF_ST + ((wid >> 1) * 16 + r16) * 8);
    auto put = [&](u32x4 v, f32x2 c, unsigned off) __attribute__((always_inline)) {
      u32x4 o;
#pragma unroll
      for (int e = 0; e < 4; ++e) o[e] = pk_bf16(__builtin_fmaf(bf_lo(v[e]), c[0], c[1]), __builtin_fmaf(bf_hi(v[e]), c[0], c[1]));
      *(u32x4*)(smem + lane * 16 + off) = o;
    };
    constexpr int KW0 = (NF - NPRE + LPR - 1) / LPR + PW0, KW1 = KW0 + PW1;
    auto frag = [&](auto K_) __attribute__((always_inline)) {
      constexpr int k = decltype(K_)::value;
      auto req = [&]() __attribute__((always_inline)) {
#pragma unroll
        for (int j = NPRE + LPR * k; j < NPRE + LPR * (k + 1); ++j) if (j < NF) ldh(j);
        if (k == KW0) ld4(rsG, 0, 0);
        if (k == KW1) ld4(rsG, 1, 294912);
      };
      constexpr int kk = PFIRST ? k - 1 : k;
      constexpr int issued = (NPRE + LPR * (kk + 1) < NF ? NPRE + LPR * (kk + 1) : NF) + (kk >= KW0 ? 4 : 0) + (kk >= KW1 ? 4 : 0);
      if (!PFIRST) req();
      if (PR_PRIO == 3) { if (k & 1) PRIO_LO(wid); else PRIO_HI(wid); }
      if (PR_PRIO == 4 && k == 4) PRIO_HI(wid);
      if (k == 1) WSTP(7);
      if (k == NF - 1) WSTP(8);
      asm volatile("s_waitcnt vmcnt(%1)" : "+v"(hv[k]) : "n"(issued - k - 1));
      put(hv[k], st[k < 10 ? k % 5 : 5], (unsigned)(k < 10 ? (k % 5) * 18432 + (wid + (k / 5) * 8) * 1024 : (wid >> 1) * 18432 + (16 + (wid & 1)) * 1024));
      SCHED();
      if (PFIRST) req();
      SCHED();
    };
    if (wid < 2) put(hv[11], st[4], (unsigned)(4 * 18432 + (16 + wid) * 1024));
    SCHED();
    frag(IC<0>{}); frag(IC<1>{}); frag(IC<2>{}); frag(IC<3>{}); frag(IC<4>{}); frag(IC<5>{});
    frag(IC<6>{}); frag(IC<7>{}); frag(IC<8>{}); frag(IC<9>{}); frag(IC<10>{});
    if (KW1 == NF) ld4(rsG, 1, 294912);
    WAITV(4); WAITL0(); BAR();
  }
  STAMP(ws, 2, 1);
  WST(2);

  unsigned aA[4], aB[4];
#pragma unroll
  for (int c = 0; c < 4; ++c) {
    aA[c] = ring + (unsigned)((8 * q + a4) * 128 + ((c ^ ((a4 >> 1) | ((q & 1) << 1))) * 32) + p4 * 8);
  }
  unsigned at0 = lds0 + (unsigned)(lane * 16), at1 = at0 + 3 * 18432;
  s16x4 wl[2][4], wh[2][4];
  bf16x8 tk[5];
  auto fresh = [&]() __attribute__((always_inline)) {
#pragma unroll
    for (int c = 0; c < 4; ++c) asm volatile("" : "=v"(wl[0][c]), "=v"(wh[0][c]), "=v"(wl[1][c]), "=v"(wh[1][c]));
#pragma unroll
    for (int t = 0; t < 5; ++t) asm volatile("" : "=v"(tk[t]));
  };
  fresh();
  auto rdw = [&](int buf, const unsigned(&a)[4], int hi) __attribute__((always_inline)) {
#pragma unroll
    for (int c = 0; c < 4; ++c) { DSRTR(wl[buf][c], a[c], buf * 4096); DSRTR(wh[buf][c], a[c], buf * 4096 + hi); }
  };
  auto rdt = [&](int t, int str, int off) __attribute__((always_inline)) {
    if (t < 3) DSR128(tk[t], at0, t * str + off); else DSR128(tk[t], at1, (t - 3) * str + off);
  };
#define LANDED_L(n) asm volatile("s_waitcnt lgkmcnt(%0)" ::"n"(n))
#define LANDED_W(n, buf) asm volatile("s_waitcnt lgkmcnt(%12)" : "+v"(wl[buf][0]), "+v"(wl[buf][1]), "+v"(wl[buf][2]), "+v"(wl[buf][3]), \
    "+v"(wh[buf][0]), "+v"(wh[buf][1]), "+v"(wh[buf][2]), "+v"(wh[buf][3]), "+v"(tk[0]), "+v"(tk[1]), "+v"(tk[2]), "+v"(tk[3]) : "n"(n))
#define LANDED_WB(buf) asm volatile("s_waitcnt lgkmcnt(0)" : "+v"(wl[buf][0]), "+v"(wl[buf][1]), "+v"(wl[buf][2]), "+v"(wl[buf][3]), \
    "+v"(wh[buf][0]), "+v"(wh[buf][1]), "+v"(wh[buf][2]), "+v"(wh[buf][3]))
#define LANDED_T(n, t) asm volatile("s_waitcnt lgkmcnt(%1)" : "+v"(tk[t]) : "n"(n))
  auto opnd = [&](int buf, int c) __attribute__((always_inline)) -> bf16x8 {
    return __builtin_shufflevector(wl[buf][c], wh[buf][c], 0, 1, 2, 3, 4, 5, 6, 7);
  };
  auto step = [&](f32x4(&acc)[4][5], auto P_, auto LD_, auto NW_, auto NT_, int str, int toff, u32x4 rs, int soff, auto gap) __attribute__((always_inline)) {
    constexpr int P = decltype(P_)::value & 1, NW = decltype(NW_)::value;
    constexpr bool FIRST = decltype(P_)::value & 2;
    constexpr bool LD = decltype(LD_)::value, NT = decltype(NT_)::value;
    constexpr int W16 = (16 - WG0) * WPG < 8 ? (16 - WG0) * WPG : 8;
    if (LD) RING_M0(ring, P * 4096);
    LANDED_L(1);
#pragma unroll
    for (int t = 0; t < 5; ++t) {
      if (t == 4) LANDED_L((NT ? 4 : 0) + (NW ? W16 : 0));
      SCHED();
#pragma unroll
      for (int c = 0; c < 4; ++c) {
        const int g = t * 4 + c;
        if (FIRST) MFMA_NEW(acc[c][t], opnd(P, c), tk[t]); else MFMA_ACC(acc[c][t], opnd(P, c), tk[t]);
        SCHED();
        if (LD && g >= LOFF && (g - LOFF) % LSTR == 0 && (g - LOFF) / LSTR < 4) { ld(rs, soff, (g - LOFF) / LSTR); SCHED(); }
        if (NW && g >= WG0 && g < WG0 + 8 / WPG) {
          if (g == WG0) { if (LD) WAITV(4); else WAITV(0); }
#pragma unroll
          for (int k = (g - WG0) * WPG; k < (g - WG0 + 1) * WPG; ++k) {
            const unsigned a = NW == 1 ? aA[k >> 1] : aB[k >> 1];
            if (k & 1) DSRTR(wh[P ^ 1][k >> 1], a, (P ^ 1) * 4096 + (NW == 1 ? 512 : 2048)); else DSRTR(wl[P ^ 1][k >> 1], a, (P ^ 1) * 4096);
          }
        }
        gap(g);
      }
      if (NT) rdt(t, str, toff);
    }
  };
  auto nogap = [&](int) __attribute__((always_inline)) {};
  using I0 = IC<0>; using I1 = IC<1>; using I2 = IC<2>;

  f32x4 ag[4][5], au[4][5];

#pragma unroll
  for (int t = 0; t < 5; ++t) { rdt(t, 18432, 0); if (t == 3) rdw(0, aA, 512); }
  auto coarse = [&](int s, int n) __attribute__((always_inline)) {
    if (PRIO) asm volatile("s_cmp_lt_u32 %0, 4\n\ts_cbranch_scc1 2f\n\ts_cmp_lt_u32 %1, %2\n\ts_cbranch_scc1 1f\n\ts_setprio 0\n\ts_branch 2f\n1:\n\ts_setprio 1\n2:" ::"s"(wid), "s"(s), "s"(n) : "scc");
  };
  coarse(0, PG_N);
  step(ag, I2{}, I1{}, I1{}, I1{}, 18432, 1024, rsG, 2 * 294912, nogap);
  step(ag, I1{}, I1{}, I1{}, I1{}, 18432, 2048, rsG, 3 * 294912, nogap);
  at0 += 2048; at1 += 2048;
#pragma clang loop unroll(disable)
  for (int s = CZERO ? 2 : 0; s < 16; s += 2) {
    coarse(s, PG_N);
    step(ag, I0{}, I1{}, I1{}, I1{}, 18432, 1024, rsG, (s + 2) * 294912, nogap);
    step(ag, I1{}, I1{}, I1{}, I1{}, 18432, 2048, rsG, (s + 3) * 294912, nogap);
    at0 += 2048; at1 += 2048;
  }
  at0 -= 16 * 1024; at1 -= 16 * 1024;
  step(ag, I0{}, I1{}, I1{}, I1{}, 18432, 17 * 1024, rsU, 0, nogap);
  step(ag, I1{}, I1{}, I1{}, I1{}, 18432, 0, rsU, 294912, nogap);
  WST(3); WSTT(3);
  STAMP(ws, 2, 6);
  ACC_READY(ag);
  float sx[5] = {0.f, 0.f, 0.f, 0.f, 0.f}, gs[80];
#pragma unroll
  for (int v = 0; v < 80; ++v) gs[v] = ag[(v >> 2) & 3][v >> 4][v & 3];
  auto ustep = [&](auto S_c) __attribute__((always_inline)) {
    constexpr int S = decltype(S_c)::value, NV = S < 8 ? 5 : 4, V0 = S < 8 ? 5 * S : 40 + 4 * (S - 8), NOP = (SILU4 ? 4 : 5) * NV;
    auto gap = [&](int g) __attribute__((always_inline)) {
      const int nl = g < LOFF ? 0 : (g - LOFF) / LSTR + 1 < 4 ? (g - LOFF) / LSTR + 1 : 4;
      const int gi = g >= LOFF && (g - LOFF) % LSTR == 0 && (g - LOFF) / LSTR < 4 ? -1 : g - nl;
      if (gi < 0) return;
#pragma unroll
      for (int k = 0; k < NOP; ++k) {
        const int op = k / NV, e = k - op * NV, v = V0 + e;
        if ((NV == 4 ? k : op == 0 ? e : op == 1 ? 5 + e / 2 : op == 2 ? 8 + e : 13 + e / 2) != gi) continue;
        if (op == 0) asm volatile("v_exp_f32 %0, %1" : "+v"(sx[e]) : "v"(gs[v]));
        if (op == 1) asm volatile("v_fma_f32 %0, %0, %1, %1" : "+v"(sx[e]) : "s"(2.0813689810056077f));
        if (op == 2) asm volatile("v_rcp_f32 %0, %0" : "+v"(sx[e]));
        if (op == 3) asm volatile("v_mul_f32 %0, %0, %1" : "+v"(gs[v]) : "v"(sx[e]));
        continue;
        if (op == 0) asm volatile("v_mul_f32 %0, 0xbfb8aa3b, %1" : "+v"(sx[e]) : "v"(gs[v]));
        if (op == 1) asm volatile("v_exp_f32 %0, %0" : "+v"(sx[e]));
        if (op == 2) asm volatile("v_add_f32 %0, 1.0, %0" : "+v"(sx[e]));
        if (op == 3) asm volatile("v_rcp_f32 %0, %0" : "+v"(sx[e]));
        if (op == 4) asm volatile("v_mul_f32 %0, %0, %1" : "+v"(gs[v]) : "v"(sx[e]));
      }
      SCHED();
    };
    if (S == 0) PRIO_HI(wid);
    if (S == PU_N) PRIO_LO(wid);
    step(au, IC<(S & 1) | (S == 0 && CZERO ? 2 : 0)>{}, I1{}, IC<(S < 17 ? 1 : 2)>{}, IC<(S < 17 ? 1 : 0)>{}, 18432, (S + 1) * 1024, S < 16 ? rsU : rsD,
         S < 16 ? (S + 2) * 294912 : (gp * 512 + wid * 64 + (S & 1) * 32) * 1152 + 1024 - wid * 128, gap);
  };
  ustep(IC<0>{}); ustep(IC<1>{}); ustep(IC<2>{}); ustep(IC<3>{}); ustep(IC<4>{}); ustep(IC<5>{});
  ustep(IC<6>{}); ustep(IC<7>{}); ustep(IC<8>{}); ustep(IC<9>{}); ustep(IC<10>{}); ustep(IC<11>{});
  ustep(IC<12>{}); ustep(IC<13>{}); ustep(IC<14>{}); ustep(IC<15>{});
  {
    int l = lane;
    asm volatile("" : "+v"(l));
    const int q_ = l >> 4, a4_ = (l >> 2) & 3, r8_ = l >> 3;
#pragma unroll
    for (int j = 0; j < 4; ++j) vW[j] = (unsigned)((r8_ + 8 * j) * 1152 + wid * 128 + (((l & 7) ^ (r8_ & 6)) * 16) - j * 1024);
#pragma unroll
    for (int c = 0; c < 4; ++c) aB[c] = ring + (unsigned)((4 * q_ + a4_) * 128 + ((c ^ ((2 * q_ + (a4_ >> 1)) & 3)) * 32) + (l & 3) * 8);
  }
  ustep(IC<16>{}); ustep(IC<17>{});
  ACC_READY(au);
  STAMP(ws, 2, 4);

  f32x4 o8[4][5], om[4][5];
  const f32x4 zero4 = {0, 0, 0, 0};
  WSTT(4);
  WSTT(5);
  {
    if (TPRI) PRIO_HI(wid);
    const unsigned aw0 = lds0 + (unsigned)(wid * 2048 + lane * 16), aw1 = aw0 + 65536;
    u32x4 af[2];
    auto mul = [&](int i, int j) __attribute__((always_inline)) {
      const int pr = i / 5, t = i - 5 * pr;
      asm volatile("v_mul_f32 %0, %0, %1" : "+v"(gs[(t * 4 + 2 * pr) * 4 + j]) : "v"(au[2 * pr + (j >> 2)][t][j & 3]));
    };
    auto cvt = [&](int i, int k) __attribute__((always_inline)) {
      const int pr = i / 5, t = i - 5 * pr;
      af[i & 1][k] = pk_bf16(gs[(t * 4 + 2 * pr) * 4 + 2 * k], gs[(t * 4 + 2 * pr) * 4 + 2 * k + 1]);
    };
    auto put = [&](int i) __attribute__((always_inline)) {
      const int pr = i / 5, t = i - 5 * pr;
      if (t < 4) asm volatile("ds_write_b128 %0, %1 offset:%2" ::"v"(aw0), "v"(af[i & 1]), "i"(t * 16384 + pr * 1024) : "memory");
      else asm volatile("ds_write_b128 %0, %1 offset:%2" ::"v"(aw1), "v"(af[i & 1]), "i"(pr * 1024) : "memory");
    };
    LANDED_WB(0);
    RING_M0(ring, 0);
#pragma unroll
    for (int c = 0; c < 4; ++c) asm volatile("" : "=v"(wl[1][c]), "=v"(wh[1][c]));
#pragma unroll
    for (int j = 0; j < 8; ++j) mul(0, j);
    SCHED();
#pragma unroll
    for (int k = 0; k < 4; ++k) cvt(0, k);
    SCHED();
    if (!TBAR) put(0); else asm volatile("s_nop 0");
    mul(1, 0);
    WSTT(6);
#pragma unroll
    for (int i = 0; i < 10; ++i) {
      const int pr = i / 5, t = i - 5 * pr;
      if (i == TW1) WAITV(TW1);
      if (i == 5) {
        WSTT(7);
        LANDED_L(5 - TW1);
        RING_M0(ring, 4096);
        if (TPRI == 2) PRIO_LO(wid);
      }
      if (i == TW0) WAITV(TW0 - 5);
      SCHED();
      const bf16x8 f = __builtin_bit_cast(bf16x8, af[i & 1]);
#pragma unroll
      for (int c = 0; c < 4; ++c) {
        if (pr == 0) MFMA_NEW(o8[c][t], opnd(0, c), f); else MFMA_ACC(o8[c][t], opnd(1, c), f);
        SCHED();
        if (c == 0 && i < 4) ld(rsD, gp * 512 * 1152, i);
        if (c == 0 && i == 4) CODE_TOUCH("k4_sum", lds0 + KF_SCR, 0, wid);
        if (c == 0 && i >= 5 && i < 9) ld(rsD, (gp * 512 + 32) * 1152, i - 5);
        if (i == TW1) { DSRTR(wl[1][c], aB[c], 4096); DSRTR(wh[1][c], aB[c], 4096 + 2048); }
        if (i == TW0) { DSRTR(wl[0][c], aB[c], 0); DSRTR(wh[0][c], aB[c], 2048); }
        if (i < 9) {
          if (c == 0) { mul(i + 1, 1); mul(i + 1, 2); mul(i + 1, 3); }
          if (c == 1) { cvt(i + 1, 0); SCHED(); mul(i + 1, 4); mul(i + 1, 5); }
          if (c == 2) { cvt(i + 1, 1); SCHED(); mul(i + 1, 6); mul(i + 1, 7); }
          if (c == 3) {
            cvt(i + 1, 2); SCHED(); cvt(i + 1, 3); SCHED();
            if (TBAR && i == 0) {
              BAR();
              put(0);
            }
            put(i + 1);
            if (i < 8) mul(i + 2, 0); else asm volatile("s_nop 0");
          }
        }
        SCHED();
      }
    }
    ACC_READY(o8);
    if (TPRI == 1) PRIO_LO(wid);
  }
  WST(4); WSTT(8);
  WST(5);
  WAITL0(); BAR();
  WST(6);
  STAMP(ws, 2, 5);

  at0 = lds0 + (unsigned)(lane * 16); at1 = at0 + 3 * 16384;
#pragma unroll
  for (int c = 0; c < 4; ++c) asm volatile("" : "=v"(wl[1][c]), "=v"(wh[1][c]));
#pragma unroll
  for (int t = 0; t < 5; ++t) asm volatile("" : "=v"(tk[t]));
#pragma unroll
  for (int t = 0; t < 5; ++t) rdt(t, 16384, 0);
  coarse(0, PB_N);
  step(om, I2{}, I1{}, I2{}, I1{}, 16384, 1024, rsD, (gp * 512 + 2 * 32) * 1152, nogap);
  step(om, I1{}, I1{}, I2{}, I1{}, 16384, 2048, rsD, (gp * 512 + 3 * 32) * 1152, nogap);
  at0 += 2048; at1 += 2048;
#pragma clang loop unroll(disable)
  for (int s = CZERO ? 2 : 0; s < 14; s += 2) {
    coarse(s, PB_N);
    step(om, I0{}, I1{}, I2{}, I1{}, 16384, 1024, rsD, (gp * 512 + (s + 2) * 32) * 1152, nogap);
    step(om, I1{}, I1{}, I2{}, I1{}, 16384, 2048, rsD, (gp * 512 + (s + 3) * 32) * 1152, nogap);
    at0 += 2048; at1 += 2048;
  }
  step(om, I0{}, I0{}, I2{}, I1{}, 16384, 1024, rsD, 0, nogap);
  step(om, I1{}, I0{}, I0{}, I0{}, 16384, 0, rsD, 0, nogap);
  PRIO_LO(wid);
  ACC_READY(om);
  WST(7);
  STAMP(ws, 2, 2);

  const int uo = (q & 1) * 2 + (q >> 1);
  auto emit = [&](f32x4 lo, f32x4 hi, int t, int E) __attribute__((always_inline)) {
    const auto x = __builtin_amdgcn_permlane16_swap(pk_bf16(lo[0], lo[1]), pk_bf16(hi[0], hi[1]), false, false);
    const auto y = __builtin_amdgcn_permlane16_swap(pk_bf16(lo[2], lo[3]), pk_bf16(hi[2], hi[3]), false, false);
    const int T16 = tt * 5 + t;
    if (T16 < 128)
      __builtin_amdgcn_raw_buffer_store_b128(u32x4{x[0], y[0], x[1], y[1]}, rsP, (unsigned)(((gp * 128 + T16) * 72 + E * 4 + uo) * 256 + r16 * 16), 0, P_AUX);
  };
#pragma unroll
  for (int k = 0; k < EPI - 1; ++k) emit(om[(k & 1) * 2][k >> 1], om[(k & 1) * 2 + 1][k >> 1], k >> 1, 2 * wid + (k & 1));
  SCHED();
  BAR();
#pragma unroll
  for (int t = 0; t < 5; ++t) {
#pragma unroll
    for (int c = 0; c < 4; ++c) *(f32x4*)(smem + (wid * 20 + t * 4 + c) * 1024 + lane * 16) = o8[c][t];
    SCHED();
  }
  WAITL0(); BAR();
#pragma unroll
  for (int k = EPI - 1; k < 10; ++k) emit(om[(k & 1) * 2][k >> 1], om[(k & 1) * 2 + 1][k >> 1], k >> 1, 2 * wid + (k & 1));
  SCHED();
  WST(8);
  WST(9);
  auto unit = [&](int u) __attribute__((always_inline)) {
    const char* p = smem + u * 2048 + lane * 16;
    f32x4 lo = {0, 0, 0, 0}, hi = {0, 0, 0, 0};
#pragma unroll
    for (int k = 0; k < 8; ++k) { lo += *(const f32x4*)(p + k * 20480); hi += *(const f32x4*)(p + k * 20480 + 1024); }
    emit(lo, hi, u >> 1, 16 + (u & 1));
  };
  unit(wid);
  if (wid < 2) unit(8 + wid);
  WST(10);
  WAITV(0);
  WST(11);
  WST_OUT(ws, 12);
  STAMP(ws, 2, 3);
}

extern "C" __global__ __launch_bounds__(576) void k4_sum(char* ws, bf16_t* out) {
  STAMP(ws, 3, 0);
  const int wv = threadIdx.x >> 6, lane = threadIdx.x & 63;
  const int T16 = blockIdx.x >> 1, t = (blockIdx.x & 1) * 8 + (lane >> 3), U = wv * 8 + (lane & 7);
  const rsrc_t rsP = MKRS(ws + WS_P, P_BYTES), rsH = MKRS(ws + WS_HT, HT_BYTES), rsO = MKRS(out, S_ * D_ * 2);
  const unsigned off = (unsigned)((T16 * 72 + U) * 256 + t * 16);
  u32x4 p[9];
#pragma unroll
  for (int g = 0; g < 9; ++g) p[g] = __builtin_amdgcn_raw_buffer_load_b128(rsP, off, g * (int)HT_BYTES, 0);
  u32x4 hv = __builtin_amdgcn_raw_buffer_load_b128(rsH, off, 0, 0);
  STAMP(ws, 3, 1);
  float a[8];
#pragma unroll
  for (int k = 0; k < 4; ++k) { a[2 * k] = bf_lo(p[0][k]); a[2 * k + 1] = bf_hi(p[0][k]); }
#pragma unroll
  for (int g = 1; g < 9; ++g)
#pragma unroll
    for (int k = 0; k < 4; ++k) { a[2 * k] += bf_lo(p[g][k]); a[2 * k + 1] += bf_hi(p[g][k]); }
  u32x4 ov;
#pragma unroll
  for (int k = 0; k < 4; ++k) ov[k] = pk_bf16(a[2 * k] + bf_lo(hv[k]), a[2 * k + 1] + bf_hi(hv[k]));
  STAMP(ws, 3, 2);
  __builtin_amdgcn_raw_buffer_store_b128(ov, rsO, (unsigned)((T16 * 16 + t) * 1152 + U * 16), 0, K4_AUX);
  STAMP(ws, 3, 3);
}

extern "C" void kernel_launch(void* const* d_in, const int* in_sizes, int n_in, void* d_out, int out_size, void* d_ws, size_t ws_size,
                              hipStream_t stream) {
  const bf16_t* x = (const bf16_t*)d_in[0];
  const bf16_t* cre = (const bf16_t*)d_in[1];
  const bf16_t* cim = (const bf16_t*)d_in[2];
  const bf16_t* wre = (const bf16_t*)d_in[3];
  const bf16_t* wim = (const bf16_t*)d_in[4];
  const bf16_t* wg = (const bf16_t*)d_in[6];
  const bf16_t* wu = (const bf16_t*)d_in[7];
  const bf16_t* wd = (const bf16_t*)d_in[8];
  char* ws = (char*)d_ws + WS_PLACE(ws_size);
  k1_hid<<<256, 512, 0, stream>>>(x, wre, wim, cre, cim, ws);
  k2_seq<<<256, 512, 0, stream>>>(x, cre, cim, wg, wu, ws);
  k3_ffn<<<256, 512, 0, stream>>>(wg, wu, wd, ws, (const bf16_t*)d_out);
  k4_sum<<<256, 576, 0, stream>>>(ws, (bf16_t*)d_out);
}
```
